```python
import torch

def _softmax(x, axis=-1):
    return torch.softmax(x, dim=axis)
def _gelu(x):
    return torch.nn.functional.gelu(x, approximate="tanh")
def _silu(x):
    return torch.nn.functional.silu(x)
def _sigmoid(x):
    return torch.sigmoid(x)
def _layernorm(x, axis=-1, eps=1e-5):
    m = x.mean(dim=axis, keepdim=True)
    v = x.var(dim=axis, keepdim=True, unbiased=False)
    return (x - m) / torch.sqrt(v + eps)
def _scatter_add(tgt, idx, src, axis=0):
    return tgt.clone().index_add_(axis, idx, src)
def _segment_sum(src, seg, n):
    out = torch.zeros((n,) + src.shape[1:], dtype=src.dtype)
    return out.index_add_(0, seg, src)
def _pad(x, pw):
    flat = tuple(v for pair in reversed(pw) for v in pair)
    return torch.nn.functional.pad(x, flat)

kernel_name = 'interleave_layers__sliding_block__mixer_channel_mlp__prompt'

def setup_inputs():

    _w = (lambda a: (lambda t: t.to(torch.bfloat16) if t.is_floating_point() else t)(torch.as_tensor(a)))
    def _A_setup():
        B, S, D = 1, 2048, 512
        return {
            "x": _w((lambda *s: torch.randn(*s, dtype=torch.bfloat16) * 0.05)(B, S, D)),
            "wqkv": _w((lambda *s: torch.randn(*s, dtype=torch.bfloat16) * 0.05)(D, 3 * D)),
            "wo": _w((lambda *s: torch.randn(*s, dtype=torch.bfloat16) * 0.05)(D, D)),
        }
    _A_in = _A_setup()

    def _B_setup():
        B, S, D, F = 1, 2048, 512, 1792
        return {
            "x": _w((lambda *s: torch.randn(*s, dtype=torch.bfloat16) * 0.05)(B, S, D)),
            "w1": _w((lambda *s: torch.randn(*s, dtype=torch.bfloat16) * 3.2)(D, F) * (D ** -0.5)),
            "b1": _w((lambda *s: torch.randn(*s, dtype=torch.bfloat16) * 0.8)(F)),
            "w2": _w((lambda *s: torch.randn(*s, dtype=torch.bfloat16) * 0.2)(F, D) * (F ** -0.5)),
            "b2": _w((lambda *s: torch.randn(*s, dtype=torch.bfloat16) * 0.2)(D)),
        }
    _B_in = _B_setup()

    return {
        "A_x": _A_in["x"],
        "A_wqkv": _A_in["wqkv"],
        "A_wo": _A_in["wo"],
        "B_x": _B_in["x"],
        "B_w1": _B_in["w1"],
        "B_b1": _B_in["b1"],
        "B_w2": _B_in["w2"],
        "B_b2": _B_in["b2"],
    }

def reference(A_x, A_wqkv, A_wo, B_x, B_w1, B_b1, B_w2, B_b2):
    def _A_ref(x, wqkv, wo):
        B, S, D = x.shape
        H = 32; Dh = D // H; W = 256
        qkv = (lambda x, *d: x.permute(*d))((x @ wqkv).reshape(B, S, 3, H, Dh), 2, 0, 3, 1, 4)
        q, k, v = qkv[0], qkv[1], qkv[2]
        scores = torch.einsum("bhqd,bhkd->bhqk", q, k) * (Dh ** -0.5)
        idx = torch.arange(S)
        mask = (idx[:, None] - idx[None, :] >= 0) & (idx[:, None] - idx[None, :] < W)
        scores = torch.where(mask[None, None], scores, -1e9)
        p = _softmax(scores, axis=-1)
        o = torch.einsum("bhqk,bhkd->bhqd", p, v)
        return (lambda x, *d: x.permute(*d))(o, 0, 2, 1, 3).reshape(B, S, D) @ wo

    def _B_ref(x, w1, b1, w2, b2):
        y = _layernorm(x, axis=-1)
        h = _gelu(torch.einsum("bsd,df->bsf", y, w1) + b1)
        return torch.einsum("bsf,fd->bsd", h, w2) + b2

    x = A_x
    x = x + _A_ref(_layernorm(x, axis=-1), A_wqkv, A_wo)
    x = x + _B_ref(_layernorm(x, axis=-1), B_w1, B_b1, B_w2, B_b2)
    x = x + _A_ref(_layernorm(x, axis=-1), A_wqkv, A_wo)
    x = x + _B_ref(_layernorm(x, axis=-1), B_w1, B_b1, B_w2, B_b2)
    return x
```

```cpp
#include <hip/hip_runtime.h>
#include <hip/hip_bf16.h>
#include <stdint.h>

typedef unsigned short u16;
__device__ __forceinline__ float bf2f(u16 b){ return __builtin_bit_cast(float, (unsigned)b << 16); }
__device__ __forceinline__ u16 f2bf(float f){ unsigned u = __builtin_bit_cast(unsigned, f); u += 0x7FFFu + ((u >> 16) & 1u); return (u16)(u >> 16); }

constexpr int S = 2048, D = 512, H = 32, DH = 16, W = 256, F = 1792;

__global__ void k_stats(const u16* x, float* mean, float* rs, int dbl) {
  int row = blockIdx.x; int t = threadIdx.x;
  float s = 0;
  for (int i = t; i < D; i += 64) s += bf2f(x[row * D + i]);
  for (int o = 32; o; o >>= 1) s += __shfl_xor(s, o);
  float m = s / D; float v = 0;
  for (int i = t; i < D; i += 64) { float d = bf2f(x[row * D + i]) - m; v += d * d; }
  for (int o = 32; o; o >>= 1) v += __shfl_xor(v, o);
  v /= D;
  float r1 = 1.0f / sqrtf(v + 1e-5f);
  if (dbl) { float v2 = v * r1 * r1; r1 *= 1.0f / sqrtf(v2 + 1e-5f); }
  if (t == 0) { mean[row] = m; rs[row] = r1; }
}
__global__ void k_csum(const u16* w, int K, int N, float* cs) {
  int n = blockIdx.x * blockDim.x + threadIdx.x; if (n >= N) return;
  float s = 0; for (int k = 0; k < K; ++k) s += bf2f(w[k * N + n]); cs[n] = s;
}
__global__ void k_gemm(const u16* a, const u16* w, int K, int N, const float* mean, const float* rs, const float* cs,
                       const u16* bias, const u16* res, u16* out, int mode) {
  int n = blockIdx.x * blockDim.x + threadIdx.x; int m = blockIdx.y; if (n >= N) return;
  float acc = 0; for (int k = 0; k < K; ++k) acc += bf2f(a[m * K + k]) * bf2f(w[k * N + n]);
  if (mode == 0) out[m * N + n] = f2bf(rs[m] * (acc - mean[m] * cs[n]));
  else if (mode == 1) { float z = rs[m] * (acc - mean[m] * cs[n]) + bf2f(bias[n]);
    float u = 0.7978845608028654f * (z + 0.044715f * z * z * z); float g = 0.5f * z * (1.0f + tanhf(u)); out[m * N + n] = f2bf(g); }
  else { float z = acc; if (bias) z += bf2f(bias[n]); out[m * N + n] = f2bf(bf2f(res[m * N + n]) + bf2f(f2bf(z))); }
}
__global__ void k_attn(const u16* qkv, u16* o) {
  int idx = blockIdx.x * blockDim.x + threadIdx.x; int h = idx % H; int q = idx / H;
  float qv[DH]; for (int d = 0; d < DH; ++d) qv[d] = bf2f(qkv[q * 3 * D + h * DH + d]);
  int k0 = q - W + 1; if (k0 < 0) k0 = 0;
  float mx = -1e30f;
  for (int k = k0; k <= q; ++k) { float s = 0; for (int d = 0; d < DH; ++d) s += qv[d] * bf2f(qkv[k * 3 * D + D + h * DH + d]); s *= 0.25f; mx = fmaxf(mx, s); }
  float l = 0; float acc[DH]; for (int d = 0; d < DH; ++d) acc[d] = 0;
  for (int k = k0; k <= q; ++k) { float s = 0; for (int d = 0; d < DH; ++d) s += qv[d] * bf2f(qkv[k * 3 * D + D + h * DH + d]); s *= 0.25f;
    float p = expf(s - mx); l += p; float pb = bf2f(f2bf(p)); for (int d = 0; d < DH; ++d) acc[d] += pb * bf2f(qkv[k * 3 * D + 2 * D + h * DH + d]); }
  for (int d = 0; d < DH; ++d) o[q * D + h * DH + d] = f2bf(acc[d] / l);
}

extern "C" void kernel_launch(void* const* d_in, const int* in_sizes, int n_in, void* d_out, int out_size, void* d_ws, size_t ws_size, hipStream_t stream) {
  const u16* x = (const u16*)d_in[0]; const u16* wqkv = (const u16*)d_in[1]; const u16* wo = (const u16*)d_in[2];
  const u16* w1 = (const u16*)d_in[4]; const u16* b1 = (const u16*)d_in[5]; const u16* w2 = (const u16*)d_in[6]; const u16* b2 = (const u16*)d_in[7];
  char* ws = (char*)d_ws;
  float* mean = (float*)ws; float* rs = mean + S; float* csq = rs + S; float* cs1 = csq + 3 * D;
  u16* qkv = (u16*)(ws + (1 << 20)); u16* o = qkv + S * 3 * D; u16* x1 = o + S * D; u16* x2 = x1 + S * D; u16* h = x2 + S * D;
  k_csum<<<(3 * D + 255) / 256, 256, 0, stream>>>(wqkv, D, 3 * D, csq);
  k_csum<<<(F + 255) / 256, 256, 0, stream>>>(w1, D, F, cs1);
  const u16* cur = x;
  for (int l = 0; l < 2; ++l) {
    k_stats<<<S, 64, 0, stream>>>(cur, mean, rs, 0);
    k_gemm<<<dim3(3 * D / 256, S), 256, 0, stream>>>(cur, wqkv, D, 3 * D, mean, rs, csq, nullptr, nullptr, qkv, 0);
    k_attn<<<S * H / 256, 256, 0, stream>>>(qkv, o);
    k_gemm<<<dim3(D / 256, S), 256, 0, stream>>>(o, wo, D, D, nullptr, nullptr, nullptr, nullptr, cur, x1, 2);
    k_stats<<<S, 64, 0, stream>>>(x1, mean, rs, 1);
    k_gemm<<<dim3(F / 256, S), 256, 0, stream>>>(x1, w1, D, F, mean, rs, cs1, b1, nullptr, h, 1);
    u16* dst = l == 1 ? (u16*)d_out : x2;
    k_gemm<<<dim3(D / 256, S), 256, 0, stream>>>(h, w2, F, D, nullptr, nullptr, nullptr, b2, x1, dst, 2);
    cur = x2;
  }
}
```

```cpp
#include <hip/hip_runtime.h>
#include <stdint.h>

namespace fk {
typedef unsigned short u16;
typedef unsigned v4u __attribute__((ext_vector_type(4)));
typedef unsigned v2u __attribute__((ext_vector_type(2)));
typedef float f32x4 __attribute__((ext_vector_type(4)));
typedef float f32x2 __attribute__((ext_vector_type(2)));
typedef float f32x16 __attribute__((ext_vector_type(16)));
typedef __bf16 bf8v __attribute__((ext_vector_type(8)));
typedef __bf16 bf2v __attribute__((ext_vector_type(2)));
#define LAS __attribute__((address_space(3)))
#define GAS __attribute__((address_space(1)))
typedef __amdgpu_buffer_rsrc_t rsrc_t;

constexpr int SEQ = 2048, DM = 512, FF = 1792;
constexpr int KB = 1024;
constexpr int WQ_OFF = 0;
constexpr int WO_OFF = WQ_OFF + 8 * 4 * 48 * KB;
constexpr int W1_OFF = WO_OFF + 8 * 4 * 16 * KB;
constexpr int W2_OFF = W1_OFF + 8 * 4 * 64 * KB;
constexpr int CS_OFF = W2_OFF + 8 * 4 * 56 * KB;
constexpr int CTRL_OFF = CS_OFF + 16 * KB;
constexpr int TILE_B = 64 * KB;
constexpr int KV_B = 32 * 64 * 2 * KB;
constexpr int HT_C = 2 * 224 * 512;
constexpr int KV0_OFF = CTRL_OFF + 16 * KB;
constexpr int OT0_OFF = KV0_OFF + KV_B;
constexpr int KV1_OFF = OT0_OFF + 32 * TILE_B;
constexpr int OT1_OFF = KV1_OFF + KV_B;
constexpr int X1T0_OFF = OT1_OFF + 32 * TILE_B;
constexpr int X1T1_OFF = X1T0_OFF + 32 * TILE_B;
constexpr int XT1_OFF = X1T1_OFF + 32 * TILE_B;
constexpr int HT0_OFF = XT1_OFF + 32 * TILE_B;
constexpr int HT1_OFF = HT0_OFF + 32 * HT_C;
constexpr int STAMP_OFF = HT1_OFF + 32 * HT_C;
constexpr int WS_END = STAMP_OFF + 256 * 64 * 8;
constexpr unsigned SENT = 0xFFFFFFFFu;
constexpr int AUX_SC1 = 16;
constexpr unsigned SPIN_CAP = 1u << 14;

constexpr int L_TILE = 0;
constexpr int L_RED = 64 * KB;
constexpr int L_PST = L_RED + 48 * KB;
constexpr int L_FST = L_PST + 2 * KB;
constexpr int L_C1 = L_FST + 2 * KB;
constexpr int L_BYTES = L_C1 + 2 * KB;

__device__ __forceinline__ float bf2f(unsigned b) { return __builtin_bit_cast(float, b << 16); }
__device__ __forceinline__ float bflo(unsigned d) { return __builtin_bit_cast(float, d << 16); }
__device__ __forceinline__ float bfhi(unsigned d) { return __builtin_bit_cast(float, d & 0xFFFF0000u); }
__device__ __forceinline__ unsigned pk(float lo, float hi) {
  bf2v r = {(__bf16)lo, (__bf16)hi};
  return __builtin_bit_cast(unsigned, r);
}
__device__ __forceinline__ f32x4 mfma16(v4u a, v4u b, f32x4 c) {
  return __builtin_amdgcn_mfma_f32_16x16x32_bf16(__builtin_bit_cast(bf8v, a), __builtin_bit_cast(bf8v, b), c, 0, 0, 0);
}
__device__ __forceinline__ unsigned nosent(unsigned d) { return d < 0xFFFFFFFEu ? d : 0xFFFFFFFEu; }
__device__ __forceinline__ v4u nosent4(v4u v) { return v4u{nosent(v.x), nosent(v.y), nosent(v.z), nosent(v.w)}; }
__device__ __forceinline__ v4u pack_T(f32x4 a, f32x4 b) {
  unsigned x0 = pk(a[0], a[1]), x1 = pk(a[2], a[3]), y0 = pk(b[0], b[1]), y1 = pk(b[2], b[3]);
  auto r0 = __builtin_amdgcn_permlane16_swap(x0, y0, false, false);
  auto r1 = __builtin_amdgcn_permlane16_swap(x1, y1, false, false);
  return v4u{r0[0], r1[0], r0[1], r1[1]};
}
#define PIN_A(x) asm("" : "+a"(x))
#define PIN_V(x) asm("" : "+v"(x))
#define SB() __builtin_amdgcn_sched_barrier(0)

__device__ __forceinline__ v4u ldw(rsrc_t rs, int voff, int sbase, int i) {
  return __builtin_amdgcn_raw_buffer_load_b128(rs, voff + (i & 3) * 1024, sbase + (i >> 2) * 4096, 0);
}
__device__ __forceinline__ v4u ld_sc1(rsrc_t rs, int voff, int soff) { return __builtin_amdgcn_raw_buffer_load_b128(rs, voff, soff, AUX_SC1); }
__device__ __forceinline__ void st_sc1(v4u v, rsrc_t rs, int voff, int soff) { __builtin_amdgcn_raw_buffer_store_b128(v, rs, voff + soff, 0, AUX_SC1); }
__device__ __forceinline__ unsigned max4(unsigned m, v4u d) {
  m = __builtin_elementwise_max(__builtin_elementwise_max(m, d.x), d.y);
  return __builtin_elementwise_max(__builtin_elementwise_max(m, d.z), d.w);
}
__device__ __forceinline__ bool spin_fail(unsigned& spins, GAS unsigned* tmo, unsigned code, int lane) {
  ++spins;
  if (spins > SPIN_CAP) {
    if (lane == 0) __hip_atomic_store(tmo, code, __ATOMIC_RELAXED, __HIP_MEMORY_SCOPE_AGENT);
    return true;
  }
  if ((spins & 63u) == 0u && __hip_atomic_load(tmo, __ATOMIC_RELAXED, __HIP_MEMORY_SCOPE_AGENT) != 0u) return true;
  __builtin_amdgcn_s_sleep(2);
  return false;
}

struct Params {
  const u16* x;
  const u16* wqkv;
  const u16* wo;
  const u16* w1;
  const u16* b1;
  const u16* w2;
  const u16* b2;
  u16* out;
  unsigned char* ws;
};

constexpr int NFRAG = 8 * 4 * (48 + 16 + 64 + 56);
constexpr int PB_TILE = NFRAG * 16 / 256;
constexpr int PB_CS = (1536 + 1792) / 64;
constexpr int CLEAN_B = KV_B + 32 * TILE_B;
constexpr int PB_CLEAN = CLEAN_B / (256 * 64);
constexpr int PREP_BLOCKS = PB_TILE + PB_CS + PB_CLEAN;

__global__ void __launch_bounds__(256) k_prep(Params p) {
  const int b = blockIdx.x, tid = threadIdx.x;
  if (b < PB_TILE) {
    const int F = b * 16 + (tid >> 4), t16 = tid & 15, cq = t16 & 3, g = t16 >> 2;
    const u16* W; int ldw_, k0, col;
    if (F < 8 * 4 * 48) {
      const int jw = F / 48, r = F % 48, s = r / 3, t = r % 3;
      W = p.wqkv; ldw_ = 1536; k0 = 32 * s; col = t * 512 + jw * 16;
    } else if (F < 8 * 4 * 64) {
      const int f = F - 8 * 4 * 48, jw = f >> 4, s = f & 15;
      W = p.wo; ldw_ = 512; k0 = 32 * s; col = jw * 16;
    } else if (F < 8 * 4 * 128) {
      const int f = F - 8 * 4 * 64, jw = f >> 6, r = f & 63, s = r >> 2, t = r & 3, j = jw >> 2, w = jw & 3;
      W = p.w1; ldw_ = 1792; k0 = 32 * s; col = 224 * j + 16 * (t < 3 ? 3 * w + t : 12 + (w >> 1));
    } else {
      const int f = F - 8 * 4 * 128, jw = f / 56, r = f % 56, s = r >> 2, nt = r & 3, j = jw >> 2, w = jw & 3;
      W = p.w2; ldw_ = 512; k0 = 448 * w + 32 * s; col = 64 * j + 16 * nt;
    }
    const u16* src = W + (size_t)(k0 + 8 * g) * ldw_ + col + 4 * cq;
    v2u r[8];
#pragma unroll
    for (int e = 0; e < 8; ++e) r[e] = *(const v2u*)(src + (size_t)e * ldw_);
    v4u o[4];
#pragma unroll
    for (int e = 0; e < 4; ++e) {
      const unsigned a0 = r[2 * e].x, a1 = r[2 * e + 1].x, c0 = r[2 * e].y, c1 = r[2 * e + 1].y;
      o[0][e] = (a0 & 0xFFFFu) | (a1 << 16);
      o[1][e] = (a0 >> 16) | (a1 & 0xFFFF0000u);
      o[2][e] = (c0 & 0xFFFFu) | (c1 << 16);
      o[3][e] = (c0 >> 16) | (c1 & 0xFFFF0000u);
    }
    v4u* dst = (v4u*)(p.ws + (size_t)F * 1024 + (g * 16 + 4 * cq) * 16);
#pragma unroll
    for (int i = 0; i < 4; ++i) dst[i] = o[i];
  } else if (b < PB_TILE + PB_CS) {
    __shared__ float red[16][64];
    const int cb = b - PB_TILE;
    const u16* W; int ldw_, col;
    if (cb < 24) { W = p.wqkv; ldw_ = 1536; col = cb * 64; } else { W = p.w1; ldw_ = 1792; col = (cb - 24) * 64; }
    const int cq = tid & 15, rg = tid >> 4;
    float s0 = 0, s1 = 0, s2 = 0, s3 = 0;
    const u16* src = W + (size_t)(rg * 32) * ldw_ + col + 4 * cq;
#pragma unroll
    for (int e = 0; e < 32; ++e) {
      const v2u v = *(const v2u*)(src + (size_t)e * ldw_);
      s0 += bflo(v.x); s1 += bfhi(v.x); s2 += bflo(v.y); s3 += bfhi(v.y);
    }
    red[rg][4 * cq + 0] = s0; red[rg][4 * cq + 1] = s1; red[rg][4 * cq + 2] = s2; red[rg][4 * cq + 3] = s3;
    __syncthreads();
    if (tid < 64) {
      float s = 0;
#pragma unroll
      for (int r = 0; r < 16; ++r) s += red[r][tid];
      ((float*)(p.ws + CS_OFF))[cb * 64 + tid] = s;
    }
    if (cb == 0 && tid == 0) *(unsigned*)(p.ws + CTRL_OFF) = 0u;
  } else {
    const int cb = b - PB_TILE - PB_CS;
    v4u* dst = (v4u*)(p.ws + KV0_OFF + (size_t)cb * (256 * 64)) + tid;
    const v4u s = {SENT, SENT, SENT, SENT};
#pragma unroll
    for (int i = 0; i < 4; ++i) dst[i * 256] = s;
  }
}

#define STAMP(i) do {} while (0)

template <int N>
__device__ __forceinline__ void load_checked(v4u (&d)[N], rsrc_t rs, int voff, int soff, int stride, GAS unsigned* tmo, unsigned code, int lane) {
  unsigned spins = 0;
  for (;;) {
    asm volatile("" ::: "memory");
#pragma unroll
    for (int i = 0; i < N; ++i) d[i] = ld_sc1(rs, voff, soff + i * stride);
    unsigned m = 0;
#pragma unroll
    for (int i = 0; i < N; ++i) m = max4(m, d[i]);
    if (!__any(m == SENT)) break;
    if (spin_fail(spins, tmo, code, lane)) break;
  }
}

__device__ __forceinline__ float dot2(unsigned a, unsigned b, float c) {
  return __builtin_amdgcn_fdot2_f32_bf16(__builtin_bit_cast(bf2v, a), __builtin_bit_cast(bf2v, b), c, false);
}

template <bool STATS>
__device__ __forceinline__ void fill_half(LAS unsigned char* L, int buf, int w, int lane, const v4u (&d)[8], int hf) {
#pragma unroll
  for (int i = 0; i < 8; ++i) *(LAS v4u*)(L + L_TILE + buf * 32768 + (16 * w + 2 * i) * 512 + lane * 16) = d[i];
  if (STATS) {
    float s = 0.f, q = 0.f;
#pragma unroll
    for (int i = 0; i < 8; ++i)
#pragma unroll
      for (int e = 0; e < 4; ++e) { s = dot2(d[i][e], 0x3F803F80u, s); q = dot2(d[i][e], d[i][e], q); }
    s += __shfl_xor(s, 32); q += __shfl_xor(q, 32);
    if (lane < 32) *(LAS f32x2*)(L + L_PST + ((hf * 4 + w) * 32 + lane) * 8) = f32x2{s, q};
  }
}
template <bool DOUBLE_LN>
__device__ __forceinline__ void final_stats(LAS unsigned char* L, int w, int lane, int hf) {
  const int t = lane & 31;
  float s = 0.f, q = 0.f;
#pragma unroll
  for (int ww = 0; ww < 4; ++ww) { const f32x2 v = *(LAS f32x2*)(L + L_PST + ((hf * 4 + ww) * 32 + t) * 8); s += v.x; q += v.y; }
  const float mean = s * (1.0f / DM);
  float var = q * (1.0f / DM) - mean * mean;
  var = var > 0.f ? var : 0.f;
  float r = __builtin_amdgcn_rsqf(var + 1e-5f);
  if (DOUBLE_LN) {
    const float v2 = var * r * r;
    r *= __builtin_amdgcn_rsqf(v2 + 1e-5f);
  }
  if (lane < 32) *(LAS f32x2*)(L + L_FST + ((w * 2 + hf) * 32 + t) * 8) = f32x2{r, -mean * r};
}

#define FRESH() int lane = lane_, c = c_, j = j_, w = w_; asm volatile("" : "+v"(lane), "+s"(c), "+s"(j), "+s"(w)); \
  const int i16 = lane & 15, g = lane >> 4, vo = lane * 16, xrd = g * 512 + i16 * 16, jw = j * 4 + w, head = jw; (void)i16; (void)g; (void)vo; (void)xrd; (void)head
__global__ void __launch_bounds__(256, 1) k_main(Params p) {
  extern __shared__ __attribute__((aligned(16))) unsigned char smem[];
  LAS unsigned char* L = (LAS unsigned char*)smem;
  const int b = blockIdx.x, tid = threadIdx.x, lane_ = tid & 63, w_ = __builtin_amdgcn_readfirstlane(tid >> 6);
#define MAPPING 0
  int c_, j_;
  if (MAPPING == 0) { c_ = (b & 7) * 4 + (b >> 6); j_ = (b >> 3) & 7; }
  else { c_ = ((b & 7) >> 1) * 8 + (b >> 5); j_ = (b & 1) * 4 + ((b >> 3) & 3); }
  const rsrc_t rs = __builtin_amdgcn_make_buffer_rsrc((void*)p.ws, 0, WS_END, 0x00020000);
  const rsrc_t rx = __builtin_amdgcn_make_buffer_rsrc((void*)p.x, 0, SEQ * DM * 2, 0x00020000);
  GAS unsigned* tmo = (GAS unsigned*)(p.ws + CTRL_OFF);
  STAMP(0);
  f32x4 csq, csk, b2v, xr[4]; float csv; v4u wq[48];
  const v4u SENT4 = {SENT, SENT, SENT, SENT};
  {
  FRESH();

  const float* cs = (const float*)(p.ws + CS_OFF);
  csq = *(const f32x4*)(cs + head * 16 + 4 * g); csk = *(const f32x4*)(cs + 512 + head * 16 + 4 * g);
  csv = cs[1024 + head * 16 + i16];
  if (tid < 224) {
    *(LAS float*)(L + L_C1 + tid * 4) = cs[1536 + 224 * j + tid];
    *(LAS float*)(L + L_C1 + 1024 + tid * 4) = bf2f(p.b1[224 * j + tid]);
  }
  { const v2u v = *(const v2u*)(p.b2 + 64 * j + 16 * w + 4 * g); b2v = f32x4{bflo(v.x), bfhi(v.x), bflo(v.y), bfhi(v.y)}; }
#pragma unroll
  for (int m = 0; m < 4; ++m) {
    const v2u v = *(const v2u*)(p.x + (size_t)(64 * c + 16 * m + i16) * DM + 64 * j + 16 * w + 4 * g);
    xr[m] = f32x4{bflo(v.x), bfhi(v.x), bflo(v.y), bfhi(v.y)};
  }
#pragma unroll
  for (int i = 0; i < 4; ++i) st_sc1(SENT4, rs, vo + i * 1024, KV1_OFF + (head * 64 + 2 * c) * 2048);

#pragma unroll
  for (int i = 0; i < 48; ++i) wq[i] = ldw(rs, vo, WQ_OFF + jw * 48 * KB, i);
  SB();
  }

  for (int lay = 0; lay < 2; ++lay) {
    const int KV_OFF = lay ? KV1_OFF : KV0_OFF, OT_OFF = lay ? OT1_OFF : OT0_OFF, X1T_OFF = lay ? X1T1_OFF : X1T0_OFF, HT_OFF = lay ? HT1_OFF : HT0_OFF;
    v4u Qf[2], Kf[2], Vf[2];
    {
      FRESH();
      f32x4 aq[2][2], ak[2][2], av[2][2];
#pragma unroll
      for (int hf = 0; hf < 2; ++hf) {
        v4u st[8];
        if (lay == 0) {
#pragma unroll
          for (int i = 0; i < 8; ++i)
            st[i] = __builtin_amdgcn_raw_buffer_load_b128(rx, (lane & 31) * 1024 + (lane >> 5) * 16 + i * 32, (64 * c + 32 * hf) * 1024 + w * 256, 0);
        } else {
          load_checked<8>(st, rs, vo, XT1_OFF + c * TILE_B + hf * 32768 + w * 8192, 1024, tmo, 0x10 + hf, lane);
        }
        STAMP(16 + lay * 24 + hf * 4 + 0);
        fill_half<true>(L, hf, w, lane, st, hf);
        __syncthreads();
        STAMP(16 + lay * 24 + hf * 4 + 1);
#pragma unroll
        for (int m = 0; m < 2; ++m) { aq[hf][m] = f32x4{0, 0, 0, 0}; ak[hf][m] = f32x4{0, 0, 0, 0}; av[hf][m] = f32x4{0, 0, 0, 0}; }
        const LAS unsigned char* xb = L + L_TILE + hf * 32768 + xrd;
        v4u x0 = *(LAS v4u*)(xb), x1 = *(LAS v4u*)(xb + 256);
#pragma unroll
        for (int s = 0; s < 16; ++s) {
          v4u n0, n1;
          if (s < 15) { n0 = *(LAS v4u*)(xb + (s + 1) * 2048); n1 = *(LAS v4u*)(xb + (s + 1) * 2048 + 256); }
          SB();
          if (hf == 0) { PIN_A(wq[3 * s]); PIN_A(wq[3 * s + 1]); PIN_A(wq[3 * s + 2]); }
          aq[hf][0] = mfma16(wq[3 * s], x0, aq[hf][0]);
          aq[hf][1] = mfma16(wq[3 * s], x1, aq[hf][1]);
          ak[hf][0] = mfma16(wq[3 * s + 1], x0, ak[hf][0]);
          ak[hf][1] = mfma16(wq[3 * s + 1], x1, ak[hf][1]);
          av[hf][0] = mfma16(x0, wq[3 * s + 2], av[hf][0]);
          av[hf][1] = mfma16(x1, wq[3 * s + 2], av[hf][1]);
          SB();
          x0 = n0; x1 = n1;
        }
        STAMP(16 + lay * 24 + hf * 4 + 2);
        final_stats<false>(L, w, lane, hf);
        f32x4 q2[2], k2[2], v2[2];
#pragma unroll
        for (int m = 0; m < 2; ++m) {
          const f32x2 ab = *(LAS f32x2*)(L + L_FST + ((w * 2 + hf) * 32 + 16 * m + i16) * 8);
          q2[m] = aq[hf][m] * ab.x + csq * ab.y;
          k2[m] = ak[hf][m] * ab.x + csk * ab.y;
          const f32x4 t0 = *(LAS f32x4*)(L + L_FST + ((w * 2 + hf) * 32 + 16 * m + 4 * g) * 8);
          const f32x4 t1 = *(LAS f32x4*)(L + L_FST + ((w * 2 + hf) * 32 + 16 * m + 4 * g + 2) * 8);
          v2[m] = f32x4{av[hf][m][0] * t0[0] + csv * t0[1], av[hf][m][1] * t0[2] + csv * t0[3], av[hf][m][2] * t1[0] + csv * t1[1], av[hf][m][3] * t1[2] + csv * t1[3]};
        }
        Qf[hf] = pack_T(q2[0], q2[1]);
        Kf[hf] = nosent4(pack_T(k2[0], k2[1]));
        v4u vs = nosent4(v4u{pk(v2[0][0], v2[0][1]), pk(v2[0][2], v2[0][3]), pk(v2[1][0], v2[1][1]), pk(v2[1][2], v2[1][3])});
        st_sc1(Kf[hf], rs, vo, KV_OFF + (head * 64 + 2 * c + hf) * 2048);
        const int sg = ((g & 1) << 1) | (g >> 1);
        st_sc1(vs, rs, 1024 + (sg * 16 + i16) * 16, KV_OFF + (head * 64 + 2 * c + hf) * 2048);
        {
          auto a = __builtin_amdgcn_permlane16_swap(vs[0], vs[1], false, false);
          auto bb = __builtin_amdgcn_permlane32_swap(a[0], a[1], false, false);
          auto cc = __builtin_amdgcn_permlane16_swap(bb[0], bb[1], false, false);
          auto a2 = __builtin_amdgcn_permlane16_swap(vs[2], vs[3], false, false);
          auto b2 = __builtin_amdgcn_permlane32_swap(a2[0], a2[1], false, false);
          auto c2 = __builtin_amdgcn_permlane16_swap(b2[0], b2[1], false, false);
          Vf[hf] = v4u{cc[0], cc[1], c2[0], c2[1]};
        }
      }
    }
    STAMP(1 + lay * 8);
    v4u wo[16];
    {
    FRESH();
#pragma unroll
    for (int hf = 0; hf < 2; ++hf) {
      st_sc1(SENT4, rs, vo, X1T_OFF + c * TILE_B + hf * 32768 + (8 * j + 2 * w) * 512);
#pragma unroll
      for (int t = 0; t < 3; ++t) st_sc1(SENT4, rs, vo, HT_OFF + c * HT_C + hf * 114688 + (28 * j + 2 * (3 * w + t)) * 512);
      __builtin_amdgcn_raw_buffer_store_b64(v2u{SENT, SENT}, rs, (g >> 1) * 512 + (16 * (w & 1) + i16) * 16 + (g & 1) * 8,
                                            HT_OFF + c * HT_C + hf * 114688 + (28 * j + 2 * (12 + (w >> 1))) * 512, AUX_SC1);
      if (lay == 0) {
        st_sc1(SENT4, rs, vo, XT1_OFF + c * TILE_B + hf * 32768 + (8 * j + 2 * w) * 512);
        st_sc1(SENT4, rs, vo, OT1_OFF + c * TILE_B + hf * 32768 + (2 * head) * 512);
      }
    }
#pragma unroll
    for (int i = 0; i < 16; ++i) wo[i] = ldw(rs, vo, WO_OFF + jw * 16 * KB, i);
    SB();
    }
    {
      FRESH();
      v4u Kn[8], Vn[8];
      {
        unsigned spins = 0;
        for (;;) {
          asm volatile("" ::: "memory");
#pragma unroll
          for (int i = 0; i < 8; ++i) {
            const int kt = 2 * c - 8 + i;
            const int so = kt >= 0 ? KV_OFF + (head * 64 + kt) * 2048 : 0x7FFF0000;
            Kn[i] = ld_sc1(rs, vo, so);
            Vn[i] = ld_sc1(rs, vo + 1024, so);
          }
          unsigned m = 0;
#pragma unroll
          for (int i = 0; i < 8; ++i) { m = max4(m, Kn[i]); m = max4(m, Vn[i]); }
          if (!__any(m == SENT)) break;
          if (spin_fail(spins, tmo, 0x20, lane)) break;
        }
      }
      STAMP(16 + lay * 24 + 8);
      const float cexp = 0.25f * 1.4426950408889634f;
      const int qcol = lane & 31, hh = lane >> 5;
      const v4u ones = {0x3F803F80u, 0x3F803F80u, 0x3F803F80u, 0x3F803F80u};
#define QK(dst, kf, qf) asm volatile("s_nop 1\n\tv_mfma_f32_32x32x16_bf16 %0, %1, %2, 0" : "=&v"(dst) : "v"(kf), "v"(qf))
#pragma unroll
      for (int hf = 0; hf < 2; ++hf) {
        auto ktile = [&](int i) -> v4u { const int idx = hf + i; return idx < 8 ? Kn[idx] : Kf[idx - 8]; };
        const bool old_ok = (2 * c + hf - 8) >= 0;
        f32x16 Sa, Sb, Sc;
        QK(Sa, ktile(0), Qf[hf]); QK(Sb, ktile(8), Qf[hf]); QK(Sc, ktile(1), Qf[hf]);
        asm volatile("s_nop 7" : "+v"(Sa), "+v"(Sb));
        SB();
        f32x16 Sm;
#pragma unroll
        for (int r = 0; r < 16; ++r) {
          const int row = (r & 3) + 8 * (r >> 2) + 4 * hh;
          const float so = old_ok ? Sa[r] : -1e30f;
          Sm[r] = row <= qcol ? Sb[r] : so;
        }
        float mx = Sm[0];
#pragma unroll
        for (int r = 1; r < 16; ++r) mx = fmaxf(mx, Sm[r]);
        SB();
        QK(Sa, ktile(2), Qf[hf]);
        SB();
#pragma unroll
        for (int i = 1; i < 8; ++i) {
          f32x16& cur = (i % 3 == 1) ? Sc : (i % 3 == 2) ? Sa : Sb;
          f32x16& nx2 = ((i + 2) % 3 == 1) ? Sc : ((i + 2) % 3 == 2) ? Sa : Sb;
          (void)nx2;
          f32x16& nx1 = ((i + 2) % 3 == 1) ? Sc : ((i + 2) % 3 == 2) ? Sa : Sb;
          (void)nx1;
          if (i == 1) { QK(Sb, ktile(3), Qf[hf]); SB(); }
          asm volatile("" : "+v"(cur));
          float t = cur[0];
#pragma unroll
          for (int r = 1; r < 16; ++r) t = fmaxf(t, cur[r]);
          const bool ok = (2 * c + hf - 8 + i) >= 0;
          mx = fmaxf(mx, ok ? t : -1e30f);
          SB();
          if (i + 3 < 8) { QK(cur, ktile(i + 3), Qf[hf]); }
          else if (i + 3 < 11) { QK(cur, ktile(i + 3 - 7), Qf[hf]); }
          SB();
        }
        mx = fmaxf(mx, __shfl_xor(mx, 32));
        const float mc = mx * cexp;
        f32x4 O[2] = {f32x4{0, 0, 0, 0}, f32x4{0, 0, 0, 0}}, Ls[2] = {f32x4{0, 0, 0, 0}, f32x4{0, 0, 0, 0}};
        {
          unsigned pp[8];
#pragma unroll
          for (int r = 0; r < 8; ++r)
            pp[r] = pk(__builtin_amdgcn_exp2f(Sm[2 * r] * cexp - mc), __builtin_amdgcn_exp2f(Sm[2 * r + 1] * cexp - mc));
          unsigned pn[8], po[8];
#pragma unroll
          for (int r = 0; r < 8; ++r) {
            const int row0 = ((2 * r) & 3) + 8 * ((2 * r) >> 2) + 4 * hh;
            const unsigned mk = (row0 <= qcol ? 0xFFFFu : 0u) | (row0 + 1 <= qcol ? 0xFFFF0000u : 0u);
            pn[r] = pp[r] & mk; po[r] = pp[r] & ~mk;
          }
          v4u B0, B1, C0, C1;
          { auto r0 = __builtin_amdgcn_permlane16_swap(pn[0], pn[2], false, false); B0[0] = r0[0]; B1[0] = r0[1]; }
          { auto r0 = __builtin_amdgcn_permlane16_swap(pn[1], pn[3], false, false); B0[1] = r0[0]; B1[1] = r0[1]; }
          { auto r0 = __builtin_amdgcn_permlane16_swap(pn[4], pn[6], false, false); B0[2] = r0[0]; B1[2] = r0[1]; }
          { auto r0 = __builtin_amdgcn_permlane16_swap(pn[5], pn[7], false, false); B0[3] = r0[0]; B1[3] = r0[1]; }
          { auto r0 = __builtin_amdgcn_permlane16_swap(po[0], po[2], false, false); C0[0] = r0[0]; C1[0] = r0[1]; }
          { auto r0 = __builtin_amdgcn_permlane16_swap(po[1], po[3], false, false); C0[1] = r0[0]; C1[1] = r0[1]; }
          { auto r0 = __builtin_amdgcn_permlane16_swap(po[4], po[6], false, false); C0[2] = r0[0]; C1[2] = r0[1]; }
          { auto r0 = __builtin_amdgcn_permlane16_swap(po[5], po[7], false, false); C0[3] = r0[0]; C1[3] = r0[1]; }
          const v4u vnew = Vf[hf];
          const v4u vold = Vn[hf];
          O[0] = mfma16(vnew, B0, O[0]); O[1] = mfma16(vnew, B1, O[1]);
          O[0] = mfma16(vold, C0, O[0]); O[1] = mfma16(vold, C1, O[1]);
          Ls[0] = mfma16(ones, B0, Ls[0]); Ls[1] = mfma16(ones, B1, Ls[1]);
          Ls[0] = mfma16(ones, C0, Ls[0]); Ls[1] = mfma16(ones, C1, Ls[1]);
          SB();
        }
#pragma unroll
        for (int i = 1; i < 8; ++i) {
          f32x16& cur = ((i + 7) % 3 == 1) ? Sc : ((i + 7) % 3 == 2) ? Sa : Sb;
          asm volatile("" : "+v"(cur));
          const bool ok = (2 * c + hf - 8 + i) >= 0;
          const float mci = ok ? mc : 1e30f;
          unsigned pp[8];
#pragma unroll
          for (int r = 0; r < 8; ++r)
            pp[r] = pk(__builtin_amdgcn_exp2f(cur[2 * r] * cexp - mci), __builtin_amdgcn_exp2f(cur[2 * r + 1] * cexp - mci));
          SB();
          if (i + 3 < 8) { QK(cur, ktile(i + 3), Qf[hf]); }
          v4u B0, B1;
          { auto r0 = __builtin_amdgcn_permlane16_swap(pp[0], pp[2], false, false); B0[0] = r0[0]; B1[0] = r0[1]; }
          { auto r0 = __builtin_amdgcn_permlane16_swap(pp[1], pp[3], false, false); B0[1] = r0[0]; B1[1] = r0[1]; }
          { auto r0 = __builtin_amdgcn_permlane16_swap(pp[4], pp[6], false, false); B0[2] = r0[0]; B1[2] = r0[1]; }
          { auto r0 = __builtin_amdgcn_permlane16_swap(pp[5], pp[7], false, false); B0[3] = r0[0]; B1[3] = r0[1]; }
          const int idx = hf + i;
          const v4u vf = idx < 8 ? Vn[idx] : Vf[idx - 8];
          O[0] = mfma16(vf, B0, O[0]); O[1] = mfma16(vf, B1, O[1]);
          Ls[0] = mfma16(ones, B0, Ls[0]); Ls[1] = mfma16(ones, B1, Ls[1]);
          SB();
        }
        const float r0 = __builtin_amdgcn_rcpf(Ls[0][0]), r1 = __builtin_amdgcn_rcpf(Ls[1][0]);
        st_sc1(nosent4(pack_T(O[0] * r0, O[1] * r1)), rs, vo, OT_OFF + c * TILE_B + hf * 32768 + (2 * head) * 512);
        STAMP(16 + lay * 24 + 9 + hf);
      }
    }
    STAMP(2 + lay * 8);
    v4u w1[64];
    {
    FRESH();
#pragma unroll
    for (int i = 0; i < 64; ++i) w1[i] = ldw(rs, vo, W1_OFF + jw * 64 * KB, i);
    SB();
    }
#pragma unroll
    for (int hf = 0; hf < 2; ++hf) {
      FRESH();
      v4u st[8];
      load_checked<8>(st, rs, vo, OT_OFF + c * TILE_B + hf * 32768 + w * 8192, 1024, tmo, 0x30 + hf, lane);
      STAMP(16 + lay * 24 + 11 + hf * 2);
      fill_half<false>(L, hf, w, lane, st, hf);
      __syncthreads();
      STAMP(16 + lay * 24 + 12 + hf * 2);
      f32x4 a0 = {0, 0, 0, 0}, a1 = {0, 0, 0, 0};
      const LAS unsigned char* xb = L + L_TILE + hf * 32768 + xrd;
#pragma unroll
      for (int s = 0; s < 16; ++s) {
        const v4u x0 = *(LAS v4u*)(xb + s * 2048), x1 = *(LAS v4u*)(xb + s * 2048 + 256);
        if (hf == 0) PIN_A(wo[s]);
        a0 = mfma16(wo[s], x0, a0);
        a1 = mfma16(wo[s], x1, a1);
      }
      const v4u o = nosent4(pack_T(xr[2 * hf] + a0, xr[2 * hf + 1] + a1));
      st_sc1(o, rs, vo, X1T_OFF + c * TILE_B + hf * 32768 + (8 * j + 2 * w) * 512);
      {
        const f32x4 s0 = xr[2 * hf] + a0, s1 = xr[2 * hf + 1] + a1;
        const unsigned u0 = pk(s0[0], s0[1]), u1 = pk(s0[2], s0[3]), u2 = pk(s1[0], s1[1]), u3 = pk(s1[2], s1[3]);
        xr[2 * hf] = f32x4{bflo(u0), bfhi(u0), bflo(u1), bfhi(u1)};
        xr[2 * hf + 1] = f32x4{bflo(u2), bfhi(u2), bflo(u3), bfhi(u3)};
      }
    }
    STAMP(3 + lay * 8);
    v4u w2[56];
#pragma unroll
    for (int hf = 0; hf < 2; ++hf) {
      FRESH();
      v4u st[8];
      load_checked<8>(st, rs, vo, X1T_OFF + c * TILE_B + hf * 32768 + w * 8192, 1024, tmo, 0x40 + hf, lane);
      STAMP(16 + lay * 24 + 15 + hf * 3);
      __syncthreads();
      fill_half<true>(L, hf, w, lane, st, hf);
      __syncthreads();
      STAMP(16 + lay * 24 + 16 + hf * 3);
      f32x4 ah[3][2], as = {0, 0, 0, 0};
#pragma unroll
      for (int t = 0; t < 3; ++t) { ah[t][0] = f32x4{0, 0, 0, 0}; ah[t][1] = f32x4{0, 0, 0, 0}; }
      const LAS unsigned char* xb = L + L_TILE + hf * 32768 + xrd;
      const int so = (w & 1) * 256;
      v4u x0 = *(LAS v4u*)(xb), x1 = *(LAS v4u*)(xb + 256), xs = *(LAS v4u*)(xb + so);
#pragma unroll
      for (int s = 0; s < 16; ++s) {
        v4u n0, n1, ns;
        if (s < 15) { n0 = *(LAS v4u*)(xb + (s + 1) * 2048); n1 = *(LAS v4u*)(xb + (s + 1) * 2048 + 256); ns = *(LAS v4u*)(xb + (s + 1) * 2048 + so); }
        SB();
        if (hf == 0) {
#pragma unroll
          for (int t = 0; t < 4; ++t) { if (4 * s + t < 40) PIN_A(w1[4 * s + t]); else PIN_V(w1[4 * s + t]); }
        }
#pragma unroll
        for (int t = 0; t < 3; ++t) { ah[t][0] = mfma16(w1[4 * s + t], x0, ah[t][0]); ah[t][1] = mfma16(w1[4 * s + t], x1, ah[t][1]); }
        as = mfma16(w1[4 * s + 3], xs, as);
        if (hf == 1 && s < 14) {
#pragma unroll
          for (int t = 0; t < 4; ++t) w2[4 * s + t] = ldw(rs, vo, W2_OFF + jw * 56 * KB, 4 * s + t);
        }
        SB();
        x0 = n0; x1 = n1; xs = ns;
      }
      STAMP(16 + lay * 24 + 17 + hf * 3);
      final_stats<true>(L, w, lane, hf);
      f32x2 ab[2];
#pragma unroll
      for (int m = 0; m < 2; ++m) ab[m] = *(LAS f32x2*)(L + L_FST + ((w * 2 + hf) * 32 + 16 * m + i16) * 8);
      const f32x2 abs_ = (w & 1) ? ab[1] : ab[0];
      const float c1 = -2.0f * 0.7978845608028654f * 1.4426950408889634f, c2 = c1 * 0.044715f;
      auto gelu4 = [&](f32x4 acc, f32x2 a_b, int nt) {
        const f32x4 csn = *(LAS f32x4*)(L + L_C1 + (16 * nt + 4 * g) * 4), bn = *(LAS f32x4*)(L + L_C1 + 1024 + (16 * nt + 4 * g) * 4);
        f32x4 o;
#pragma unroll
        for (int r = 0; r < 4; ++r) {
          const float z = acc[r] * a_b.x + (csn[r] * a_b.y + bn[r]);
          const float e = __builtin_amdgcn_exp2f(z * (c1 + c2 * z * z));
          o[r] = z * __builtin_amdgcn_rcpf(1.0f + e);
        }
        return o;
      };
#pragma unroll
      for (int t = 0; t < 3; ++t) {
        const f32x4 h0 = gelu4(ah[t][0], ab[0], 3 * w + t), h1 = gelu4(ah[t][1], ab[1], 3 * w + t);
        st_sc1(nosent4(pack_T(h0, h1)), rs, vo, HT_OFF + c * HT_C + hf * 114688 + (28 * j + 2 * (3 * w + t)) * 512);
      }
      {
        const f32x4 hs = gelu4(as, abs_, 12 + (w >> 1));
        __builtin_amdgcn_raw_buffer_store_b64(v2u{nosent(pk(hs[0], hs[1])), nosent(pk(hs[2], hs[3]))}, rs,
                                              (g >> 1) * 512 + (16 * (w & 1) + i16) * 16 + (g & 1) * 8,
                                              HT_OFF + c * HT_C + hf * 114688 + (28 * j + 2 * (12 + (w >> 1))) * 512, AUX_SC1);
      }
    }
    STAMP(4 + lay * 8);
#pragma unroll
    for (int hf = 0; hf < 2; ++hf) {
      FRESH();
      v4u h0[14], h1[14];
      {
        unsigned spins = 0;
        const int base = HT_OFF + c * HT_C + hf * 114688 + 56 * w * 512;
        for (;;) {
          asm volatile("" ::: "memory");
#pragma unroll
          for (int s = 0; s < 14; ++s) { h0[s] = ld_sc1(rs, xrd, base + s * 2048); h1[s] = ld_sc1(rs, xrd + 256, base + s * 2048); }
          unsigned m = 0;
#pragma unroll
          for (int s = 0; s < 14; ++s) { m = max4(m, h0[s]); m = max4(m, h1[s]); }
          if (!__any(m == SENT)) break;
          if (spin_fail(spins, tmo, 0x50 + hf, lane)) break;
        }
      }
      STAMP(16 + lay * 24 + 21 + hf);
      f32x4 ac[4][2];
#pragma unroll
      for (int t = 0; t < 4; ++t) { ac[t][0] = f32x4{0, 0, 0, 0}; ac[t][1] = f32x4{0, 0, 0, 0}; }
#pragma unroll
      for (int s = 0; s < 14; ++s) {
#pragma unroll
        for (int t = 0; t < 4; ++t) {
          if (hf == 0) { if (4 * s + t < 48) PIN_A(w2[4 * s + t]); else PIN_V(w2[4 * s + t]); }
          ac[t][0] = mfma16(w2[4 * s + t], h0[s], ac[t][0]);
          ac[t][1] = mfma16(w2[4 * s + t], h1[s], ac[t][1]);
        }
        if (hf == 1) {
#pragma unroll
          for (int t = 0; t < 4; ++t) if (4 * s + t < 48) wq[4 * s + t] = ldw(rs, vo, lay == 0 ? WQ_OFF + jw * 48 * KB : 0x7FF00000, 4 * s + t);
        }
        SB();
      }
      LAS unsigned char* R = L + L_RED + hf * 24576;
#pragma unroll
      for (int t = 0; t < 4; ++t) {
        if (t != w) {
          const int slot = (t * 3 + (w > t ? w - 1 : w)) * 2;
          *(LAS f32x4*)(R + slot * 1024 + vo) = ac[t][0];
          *(LAS f32x4*)(R + (slot + 1) * 1024 + vo) = ac[t][1];
        }
      }
      __syncthreads();
      f32x4 s0, s1;
      s0 = w == 0 ? ac[0][0] : w == 1 ? ac[1][0] : w == 2 ? ac[2][0] : ac[3][0];
      s1 = w == 0 ? ac[0][1] : w == 1 ? ac[1][1] : w == 2 ? ac[2][1] : ac[3][1];
#pragma unroll
      for (int k = 0; k < 3; ++k) {
        s0 += *(LAS f32x4*)(R + ((w * 3 + k) * 2) * 1024 + vo);
        s1 += *(LAS f32x4*)(R + ((w * 3 + k) * 2 + 1) * 1024 + vo);
      }
      s0 += xr[2 * hf] + b2v; s1 += xr[2 * hf + 1] + b2v;
      const v4u o = pack_T(s0, s1);
      if (lay == 0) {
        st_sc1(nosent4(o), rs, vo, XT1_OFF + c * TILE_B + hf * 32768 + (8 * j + 2 * w) * 512);
        const unsigned u0 = pk(s0[0], s0[1]), u1 = pk(s0[2], s0[3]), u2 = pk(s1[0], s1[1]), u3 = pk(s1[2], s1[3]);
        xr[2 * hf] = f32x4{bflo(u0), bfhi(u0), bflo(u1), bfhi(u1)};
        xr[2 * hf + 1] = f32x4{bflo(u2), bfhi(u2), bflo(u3), bfhi(u3)};
      } else {
        *(v4u*)(p.out + (size_t)(64 * c + 32 * hf + (lane & 31)) * DM + 64 * j + 16 * w + 8 * (lane >> 5)) = o;
      }
    }
    STAMP(5 + lay * 8);
    if (lay == 0) __syncthreads();
  }
}
}

extern "C" void kernel_launch(void* const* d_in, const int* in_sizes, int n_in, void* d_out, int out_size, void* d_ws, size_t ws_size,
                              hipStream_t stream) {
  using namespace fk;
  Params p;
  p.x = (const u16*)d_in[0]; p.wqkv = (const u16*)d_in[1]; p.wo = (const u16*)d_in[2];
  p.w1 = (const u16*)d_in[4]; p.b1 = (const u16*)d_in[5]; p.w2 = (const u16*)d_in[6]; p.b2 = (const u16*)d_in[7];
  p.out = (u16*)d_out; p.ws = (unsigned char*)d_ws;
  static bool attr_set = false;
  if (!attr_set) { (void)hipFuncSetAttribute((const void*)k_main, hipFuncAttributeMaxDynamicSharedMemorySize, L_BYTES); attr_set = true; }
  k_prep<<<PREP_BLOCKS, 256, 0, stream>>>(p);
  k_main<<<256, 256, L_BYTES, stream>>>(p);
}
```

```cpp
#include <hip/hip_runtime.h>
#include <stdint.h>

namespace fk {
typedef unsigned short u16;
typedef unsigned v4u __attribute__((ext_vector_type(4)));
typedef unsigned v2u __attribute__((ext_vector_type(2)));
typedef float f32x4 __attribute__((ext_vector_type(4)));
typedef float f32x2 __attribute__((ext_vector_type(2)));
typedef float f32x16 __attribute__((ext_vector_type(16)));
typedef __bf16 bf8v __attribute__((ext_vector_type(8)));
typedef __bf16 bf2v __attribute__((ext_vector_type(2)));
#define LAS __attribute__((address_space(3)))
#define GAS __attribute__((address_space(1)))
typedef __amdgpu_buffer_rsrc_t rsrc_t;

constexpr int SEQ = 2048, DM = 512, FF = 1792;
constexpr int KB = 1024;
constexpr int WQ_OFF = 0;
constexpr int WO_OFF = WQ_OFF + 8 * 192 * KB;
constexpr int W1_OFF = WO_OFF + 8 * 64 * KB;
constexpr int W2_OFF = W1_OFF + 8 * 224 * KB;
constexpr int CS_OFF = W2_OFF + 8 * 224 * KB;
constexpr int CTRL_OFF = CS_OFF + 16 * KB;
constexpr int TILE_B = 64 * KB;
constexpr int KV_B = 32 * 64 * 2 * KB;
constexpr int HT_C = 2 * 224 * 512;
constexpr int KV0_OFF = CTRL_OFF + 16 * KB;
constexpr int OT0_OFF = KV0_OFF + KV_B;
constexpr int KV1_OFF = OT0_OFF + 32 * TILE_B;
constexpr int OT1_OFF = KV1_OFF + KV_B;
constexpr int X1T0_OFF = OT1_OFF + 32 * TILE_B;
constexpr int X1T1_OFF = X1T0_OFF + 32 * TILE_B;
constexpr int XT1_OFF = X1T1_OFF + 32 * TILE_B;
constexpr int HT0_OFF = XT1_OFF + 32 * TILE_B;
constexpr int HT1_OFF = HT0_OFF + 32 * HT_C;
constexpr int STAMP_OFF = HT1_OFF + 32 * HT_C;
constexpr int WS_END = STAMP_OFF + 256 * 8 * 128 * 8;
constexpr unsigned SENT = 0xFFFFFFFFu;
constexpr int AUX_SC1 = 16;
constexpr unsigned SPIN_CAP = 1u << 14;

constexpr int L_TILE = 0;
constexpr int L_KV = 0;
constexpr int L_Q = 80 * KB;
constexpr int L_XCH = 88 * KB;
constexpr int L_PST = 136 * KB;
constexpr int L_FST = 144 * KB;
constexpr int L_C1 = 145 * KB;
constexpr int L_CQ = 147 * KB;
constexpr int L_ONES = 148 * KB;
constexpr int L_BYTES = 148 * KB + 64;

__device__ __forceinline__ float bf2f(unsigned b) { return __builtin_bit_cast(float, b << 16); }
__device__ __forceinline__ float bflo(unsigned d) { return __builtin_bit_cast(float, d << 16); }
__device__ __forceinline__ float bfhi(unsigned d) { return __builtin_bit_cast(float, d & 0xFFFF0000u); }
__device__ __forceinline__ unsigned pk(float lo, float hi) {
  bf2v r = {(__bf16)lo, (__bf16)hi};
  return __builtin_bit_cast(unsigned, r);
}
__device__ __forceinline__ f32x4 mfma16(v4u a, v4u b, f32x4 c) {
  return __builtin_amdgcn_mfma_f32_16x16x32_bf16(__builtin_bit_cast(bf8v, a), __builtin_bit_cast(bf8v, b), c, 0, 0, 0);
}
__device__ __forceinline__ unsigned nosent(unsigned d) { return d < 0xFFFFFFFEu ? d : 0xFFFFFFFEu; }
__device__ __forceinline__ v4u nosent4(v4u v) { return v4u{nosent(v.x), nosent(v.y), nosent(v.z), nosent(v.w)}; }
__device__ __forceinline__ v4u pack_T(f32x4 a, f32x4 b) {
  unsigned x0 = pk(a[0], a[1]), x1 = pk(a[2], a[3]), y0 = pk(b[0], b[1]), y1 = pk(b[2], b[3]);
  auto r0 = __builtin_amdgcn_permlane16_swap(x0, y0, false, false);
  auto r1 = __builtin_amdgcn_permlane16_swap(x1, y1, false, false);
  return v4u{r0[0], r1[0], r0[1], r1[1]};
}
__device__ __forceinline__ v2u pk4(f32x4 a) { return v2u{pk(a[0], a[1]), pk(a[2], a[3])}; }
__device__ __forceinline__ f32x4 unpk4(v2u v) { return f32x4{bflo(v.x), bfhi(v.x), bflo(v.y), bfhi(v.y)}; }
__device__ __forceinline__ v4u swap_T(v2u x, v2u y) {
  auto r0 = __builtin_amdgcn_permlane16_swap(x.x, y.x, false, false);
  auto r1 = __builtin_amdgcn_permlane16_swap(x.y, y.y, false, false);
  return v4u{r0[0], r1[0], r0[1], r1[1]};
}
#define KEEP(x) asm volatile("" : "+v"(x))
#define WAITV0() asm volatile("s_waitcnt vmcnt(0)" ::: "memory")
#define SB() __builtin_amdgcn_sched_barrier(0)

__device__ __forceinline__ v4u ldw(rsrc_t rs, int voff, int sbase, int i) {
  return __builtin_amdgcn_raw_buffer_load_b128(rs, voff + (i & 3) * 1024, sbase + (i >> 2) * 4096, 0);
}
__device__ __forceinline__ v4u ld_sc1(rsrc_t rs, int voff, int soff) {
  return __builtin_amdgcn_raw_buffer_load_b128(rs, voff, soff, AUX_SC1); }
__device__ __forceinline__ v4u ld_seq(rsrc_t rs, int voff, int sbase, int i) { return ld_sc1(rs, voff + (i & 3) * 1024, sbase + (i >> 2) * 4096); }
#define RFL(x) __builtin_amdgcn_readfirstlane(x)
__device__ __forceinline__ void st_sc1(v4u v, rsrc_t rs, int voff, int soff) {
  __builtin_amdgcn_raw_buffer_store_b128(v, rs, voff + soff, 0, AUX_SC1); }
__device__ __forceinline__ unsigned max4(unsigned m, v4u d) {
  m = __builtin_elementwise_max(__builtin_elementwise_max(m, d.x), d.y);
  return __builtin_elementwise_max(__builtin_elementwise_max(m, d.z), d.w);
}
__device__ __forceinline__ bool spin_fail(unsigned& spins, GAS unsigned* tmo, unsigned code, int lane) {
  ++spins;
  if (spins > SPIN_CAP) {
    if (lane == 0) __hip_atomic_store(tmo, code, __ATOMIC_RELAXED, __HIP_MEMORY_SCOPE_AGENT);
    return true;
  }
  if ((spins & 63u) == 0u && __hip_atomic_load(tmo, __ATOMIC_RELAXED, __HIP_MEMORY_SCOPE_AGENT) != 0u) return true;
  return false;
}

struct Params {
  const u16* x;
  const u16* wqkv;
  const u16* wo;
  const u16* w1;
  const u16* b1;
  const u16* w2;
  const u16* b2;
  u16* out;
  unsigned char* ws;
};

constexpr int NFRAG = 8 * 704;
constexpr int PB_TILE = NFRAG * 16 / 256;
constexpr int PB_CS = (1536 + 1792) / 64;
constexpr int CLEAN_B = KV_B + 32 * TILE_B;
constexpr int PB_CLEAN = CLEAN_B / (256 * 64);
constexpr int PREP_BLOCKS = PB_TILE + PB_CS + PB_CLEAN;

__global__ void __launch_bounds__(256) k_prep(Params p) {
  const int b = blockIdx.x, tid = threadIdx.x;
  if (b < PB_TILE) {
    const int F = b * 16 + (tid >> 4), t16 = tid & 15, cq = t16 & 7, h = t16 >> 3;
    const u16* W; int ldw_, k0, col;
    if (F < 8 * 192) {
      const int j = F / 192, i = F % 192;
      W = p.wqkv; ldw_ = 1536;
      if (i < 128) { const int head = 4 * j + (i >> 5); k0 = 16 * (i & 31); col = cq < 4 ? head * 16 + 4 * cq : 512 + head * 16 + 4 * (cq - 4); }
      else { const int u = i - 128, wv = u >> 4; k0 = 16 * (16 * (wv & 1) + (u & 15)); col = 1024 + (4 * j + 2 * (wv >> 1)) * 16 + 4 * cq; }
    } else if (F < 8 * 256) {
      const int f = F - 8 * 192, j = f >> 6, u = f & 63, v = u >> 3;
      W = p.wo; ldw_ = 512; k0 = 16 * (8 * (v >> 1) + (u & 7)); col = 64 * j + 32 * (v & 1) + 4 * (cq ^ (4 * (v >> 2)));
    } else if (F < 8 * 480) {
      const int f = F - 8 * 256, j = f / 224, u = f % 224;
      W = p.w1; ldw_ = 1792; k0 = 16 * (u & 31); col = 224 * j + 32 * (u >> 5) + 4 * cq;
    } else {
      const int f = F - 8 * 480, j = f / 224, u = f % 224, v = u / 28, q = u % 28;
      W = p.w2; ldw_ = 512; k0 = 224 * v + 16 * (q >> 1); col = 64 * j + 32 * (q & 1) + 4 * cq;
    }
    const u16* src = W + (size_t)(k0 + 8 * h) * ldw_ + col;
    v2u r[8];
#pragma unroll
    for (int e = 0; e < 8; ++e) r[e] = *(const v2u*)(src + (size_t)e * ldw_);
    v4u o[4];
#pragma unroll
    for (int e = 0; e < 4; ++e) {
      const unsigned a0 = r[2 * e].x, a1 = r[2 * e + 1].x, c0 = r[2 * e].y, c1 = r[2 * e + 1].y;
      o[0][e] = (a0 & 0xFFFFu) | (a1 << 16);
      o[1][e] = (a0 >> 16) | (a1 & 0xFFFF0000u);
      o[2][e] = (c0 & 0xFFFFu) | (c1 << 16);
      o[3][e] = (c0 >> 16) | (c1 & 0xFFFF0000u);
    }
    v4u* dst = (v4u*)(p.ws + (size_t)F * 1024 + (h * 32 + 4 * cq) * 16);
#pragma unroll
    for (int i = 0; i < 4; ++i) dst[i] = o[i];
  } else if (b < PB_TILE + PB_CS) {
    __shared__ float red[16][64];
    const int cb = b - PB_TILE;
    const u16* W; int ldw_, col;
    if (cb < 24) { W = p.wqkv; ldw_ = 1536; col = cb * 64; } else { W = p.w1; ldw_ = 1792; col = (cb - 24) * 64; }
    const int cq = tid & 15, rg = tid >> 4;
    float s0 = 0, s1 = 0, s2 = 0, s3 = 0;
    const u16* src = W + (size_t)(rg * 32) * ldw_ + col + 4 * cq;
#pragma unroll
    for (int e = 0; e < 32; ++e) {
      const v2u v = *(const v2u*)(src + (size_t)e * ldw_);
      s0 += bflo(v.x); s1 += bfhi(v.x); s2 += bflo(v.y); s3 += bfhi(v.y);
    }
    red[rg][4 * cq + 0] = s0; red[rg][4 * cq + 1] = s1; red[rg][4 * cq + 2] = s2; red[rg][4 * cq + 3] = s3;
    __syncthreads();
    if (tid < 64) {
      float s = 0;
#pragma unroll
      for (int r = 0; r < 16; ++r) s += red[r][tid];
      ((float*)(p.ws + CS_OFF))[cb * 64 + tid] = s;
    }
    if (cb == 0 && tid == 0) *(unsigned*)(p.ws + CTRL_OFF) = 0u;
  } else {
    const int cb = b - PB_TILE - PB_CS;
    v4u* dst = (v4u*)(p.ws + KV0_OFF + (size_t)cb * (256 * 64)) + tid;
    const v4u s = {SENT, SENT, SENT, SENT};
#pragma unroll
    for (int i = 0; i < 4; ++i) dst[i * 256] = s;
  }
}

#define STAMP(i) do {} while (0)
#define STAMP_FLUSH() do {} while (0)

__device__ __forceinline__ float max3f(float a, float b, float c) { float r; asm("v_max3_f32 %0, %1, %2, %3" : "=v"(r) : "v"(a), "v"(b), "v"(c)); return r; }
__device__ __forceinline__ unsigned max3u(unsigned a, unsigned b, unsigned c) { unsigned r; asm("v_max3_u32 %0, %1, %2, %3" : "=v"(r) : "v"(a), "v"(b), "v"(c)); return r; }
__device__ __forceinline__ f32x16 mfma32(v4u a, v4u b, f32x16 c) {
  return __builtin_amdgcn_mfma_f32_32x32x16_bf16(__builtin_bit_cast(bf8v, a), __builtin_bit_cast(bf8v, b), c, 0, 0, 0);
}
template <int N> __device__ __forceinline__ bool any_sent(const v4u (&d)[N]) {
  unsigned m0 = 0, m1 = 0;
#pragma unroll
  for (int i = 0; i < N; ++i) { if (i & 1) m1 = max3u(m1, d[i].x, d[i].z); else m0 = max3u(m0, d[i].x, d[i].z); }
  return __any(max3u(m0, m1, 0) == SENT);
}
template <int N> __device__ __forceinline__ void issue(v4u (&d)[N], rsrc_t rs, int voff, int soff, int stride) {
#pragma unroll
  for (int i = 0; i < N; ++i) d[i] = ld_sc1(rs, voff, soff + i * stride);
}
template <int N, class F> __device__ __forceinline__ void verify(v4u (&d)[N], F reissue, GAS unsigned* tmo, unsigned code, int lane) {
  if (any_sent<N>(d)) {
    unsigned spins = 0;
    do {
      __builtin_amdgcn_s_sleep(2);
      asm volatile("" ::: "memory");
      reissue();
    } while (any_sent<N>(d) && !spin_fail(spins, tmo, code, lane));
  }
}
__device__ __forceinline__ f32x2 both_halves(float v) {
  const unsigned a = __builtin_bit_cast(unsigned, v);
  auto r = __builtin_amdgcn_permlane32_swap(a, a, false, false);
  const unsigned r0 = r[0], r1 = r[1];
  return f32x2{__builtin_bit_cast(float, r0), __builtin_bit_cast(float, r1)};
}
__device__ __forceinline__ float dot2(unsigned a, unsigned b, float c) {
  return __builtin_amdgcn_fdot2_f32_bf16(__builtin_bit_cast(bf2v, a), __builtin_bit_cast(bf2v, b), c, false);
}
__device__ __forceinline__ v4u nosent2(v4u v) { return v4u{nosent(v.x), v.y, nosent(v.z), v.w}; }
__device__ __forceinline__ f32x4 round4(f32x4 s) {
  const unsigned u0 = pk(s[0], s[1]), u1 = pk(s[2], s[3]);
  return f32x4{bflo(u0), bfhi(u0), bflo(u1), bfhi(u1)};
}

__device__ __forceinline__ v4u pk8(const f32x16& a, int r0, float sc = 1.0f) {
  return v4u{pk(a[r0] * sc, a[r0 + 1] * sc), pk(a[r0 + 2] * sc, a[r0 + 3] * sc), pk(a[r0 + 4] * sc, a[r0 + 5] * sc), pk(a[r0 + 6] * sc, a[r0 + 7] * sc)};
}
__device__ __forceinline__ v4u swap_T8(v4u p) {
  auto r0 = __builtin_amdgcn_permlane32_swap(p[0], p[2], false, false);
  auto r1 = __builtin_amdgcn_permlane32_swap(p[1], p[3], false, false);
  return v4u{r0[0], r1[0], r0[1], r1[1]};
}
#define FRESH() int lane = lane_, c = c_, j = j_, v = v_; asm volatile("" : "+v"(lane), "+s"(c), "+s"(j), "+s"(v)); \
  const int vo = lane * 16, tk = lane & 31, hh = lane >> 5; (void)vo; (void)tk; (void)hh
#define LDS4(off) (*(const LAS v4u*)(L + (off)))
#define LDSF4(off) (*(const LAS f32x4*)(L + (off)))
constexpr int OOB = 0x7FF00000;

template <bool STATS>
__device__ __forceinline__ void fill_tile(LAS unsigned char* L, int v, int lane, const v4u (&d)[8]) {
#pragma unroll
  for (int i = 0; i < 8; ++i) *(LAS v4u*)(L + L_TILE + (i >> 2) * 32768 + v * 4096 + (i & 3) * 1024 + lane * 16) = d[i];
  if (STATS) {
    float r[4];
#pragma unroll
    for (int hf = 0; hf < 2; ++hf) {
      float s0 = 0.f, s1 = 0.f, q0 = 0.f, q1 = 0.f;
#pragma unroll
      for (int i = 0; i < 4; ++i) {
        const v4u x = d[4 * hf + i];
        s0 = dot2(x[0], 0x3F803F80u, s0); q0 = dot2(x[0], x[0], q0);
        s1 = dot2(x[1], 0x3F803F80u, s1); q1 = dot2(x[1], x[1], q1);
        s0 = dot2(x[2], 0x3F803F80u, s0); q0 = dot2(x[2], x[2], q0);
        s1 = dot2(x[3], 0x3F803F80u, s1); q1 = dot2(x[3], x[3], q1);
      }
      r[2 * hf] = s0 + s1; r[2 * hf + 1] = q0 + q1;
    }
    *(LAS f32x4*)(L + L_PST + ((v * 2 + (lane >> 5)) * 32 + (lane & 31)) * 16) = f32x4{r[0], r[1], r[2], r[3]};
  }
}
template <bool DOUBLE_LN>
__device__ __forceinline__ void final_stats(LAS unsigned char* L, int lane, f32x2& abA, f32x2& abB) {
  const int t = lane & 31, hh = lane >> 5;
  f32x4 s = {0, 0, 0, 0};
#pragma unroll
  for (int k = 0; k < 8; ++k) s += *(LAS f32x4*)(L + L_PST + ((hh * 8 + k) * 32 + t) * 16);
  f32x4 tot;
#pragma unroll
  for (int e = 0; e < 4; ++e) { const f32x2 r = both_halves(s[e]); tot[e] = r.x + r.y; }
  f32x2 ab[2];
#pragma unroll
  for (int hf = 0; hf < 2; ++hf) {
    const float mean = tot[2 * hf] * (1.0f / DM);
    float var = tot[2 * hf + 1] * (1.0f / DM) - mean * mean;
    var = var > 0.f ? var : 0.f;
    float r = __builtin_amdgcn_rsqf(var + 1e-5f);
    if (DOUBLE_LN) {
      const float v2 = var * r * r;
      r *= __builtin_amdgcn_rsqf(v2 + 1e-5f);
    }
    ab[hf] = f32x2{r, -mean * r};
  }
  abA = ab[0]; abB = ab[1];
  *(LAS f32x2*)(L + L_FST + lane * 8) = hh ? ab[1] : ab[0];
}

__global__ void __launch_bounds__(512, 2) k_main(Params p) {
  extern __shared__ __attribute__((aligned(16))) unsigned char smem[];
  LAS unsigned char* L = (LAS unsigned char*)smem;
  const int b = blockIdx.x, tid = threadIdx.x, lane_ = tid & 63, v_ = __builtin_amdgcn_readfirstlane(tid >> 6);
#define MAPPING 1
  int c_, j_;
  if (MAPPING == 0) { c_ = (b & 7) * 4 + (b >> 6); j_ = (b >> 3) & 7; }
  else { c_ = ((b & 7) >> 1) * 8 + (b >> 5); j_ = (b & 1) * 4 + ((b >> 3) & 3); }
  const rsrc_t rs = __builtin_amdgcn_make_buffer_rsrc((void*)p.ws, 0, WS_END, 0x00020000);
  const rsrc_t rx = __builtin_amdgcn_make_buffer_rsrc((void*)p.x, 0, SEQ * DM * 2, 0x00020000);
  GAS unsigned* tmo = (GAS unsigned*)(p.ws + CTRL_OFF);
  STAMP(0);
  v4u xr, wq[32], st[8];
  const v4u SENT4 = {SENT, SENT, SENT, SENT};
  f32x16 Z;
#pragma unroll
  for (int q = 0; q < 16; ++q) Z[q] = 0.f;
  {
    FRESH();
#pragma unroll
    for (int i = 0; i < 8; ++i) st[i] = __builtin_amdgcn_raw_buffer_load_b128(rx, tk * 1024 + hh * 16 + (i & 3) * 32, (64 * c + 32 * (i >> 2)) * 1024 + v * 128, 0);
    const int wqb = WQ_OFF + (j * 192 + (v < 4 ? v * 32 : 128 + (v - 4) * 16)) * KB;
#pragma unroll
    for (int i = 0; i < 16; ++i) wq[i] = ldw(rs, vo, wqb, i);
#pragma unroll
    for (int i = 16; i < 32; ++i) wq[i] = ldw(rs, vo, v < 4 ? wqb : OOB, i);
    SB();
    const float* cs = (const float*)(p.ws + CS_OFF);
    if (tid < 224) {
      *(LAS float*)(L + L_C1 + tid * 4) = cs[1536 + 224 * j + tid];
      *(LAS float*)(L + L_C1 + 1024 + tid * 4) = bf2f(p.b1[224 * j + tid]);
    } else if (tid >= 256) {
      const int t = tid - 256;
      if (t < 192) *(LAS float*)(L + L_CQ + t * 4) = cs[(t >> 6) * 512 + 64 * j + (t & 63)];
      else *(LAS float*)(L + L_CQ + t * 4) = bf2f(p.b2[64 * j + t - 192]);
    } else if (tid < 228) *(LAS unsigned*)(L + L_ONES + (tid - 224) * 4) = 0x3F803F80u;
    {
      const u16* src = p.x + (size_t)(64 * c + 32 * ((v >> 1) & 1) + tk) * DM + 64 * j + 32 * (v & 1) + 16 * (v >> 2) + 4 * hh;
      const v2u a = *(const v2u*)src, bb = *(const v2u*)(src + 8);
      xr = v4u{a.x, a.y, bb.x, bb.y};
    }
    if (v < 4) {
#pragma unroll
      for (int hf = 0; hf < 2; ++hf) st_sc1(SENT4, rs, vo, KV1_OFF + ((4 * j + v) * 64 + 2 * c + hf) * 2048);
    } else {
      const int hl = 4 * j + 2 * ((v - 4) >> 1) + ((lane >> 4) & 1);
#pragma unroll
      for (int s = 0; s < 2; ++s) st_sc1(SENT4, rs, (hl * 64) * 2048 + 1024 + (32 * hh + 16 * s + (lane & 15)) * 16, KV1_OFF + (2 * c + (v & 1)) * 2048);
    }
    SB();
  }

  for (int lay = 0; lay < 2; ++lay) {
    const int KV_OFF = lay ? KV1_OFF : KV0_OFF, OT_OFF = lay ? OT1_OFF : OT0_OFF, X1T_OFF = lay ? X1T1_OFF : X1T0_OFF, HT_OFF = lay ? HT1_OFF : HT0_OFF;
    v4u wo[8], w1[32], w2[28];
    {
      FRESH();
      if (lay) {
        verify<8>(st, [&]() {
#pragma unroll
            for (int i = 0; i < 8; ++i) st[i] = ld_seq(rs, vo, RFL(XT1_OFF + c * TILE_B + v * 4096) + (i >> 2) * 32768, i & 3);
      }, tmo, 0x10, lane);
      }
      STAMP(16 + lay * 24 + 0);
      fill_tile<true>(L, v, lane, st);
      __syncthreads();
      STAMP(16 + lay * 24 + 1);
      const int wob = __builtin_amdgcn_readfirstlane(WO_OFF + (j * 64 + v * 8) * KB);
      f32x2 abA, abB;
      if (v < 4) {
        f32x16 aA = Z, aB = Z;
        v4u xa[3], xb[3];
#pragma unroll
        for (int s = 0; s < 2; ++s) { xa[s] = LDS4(L_TILE + s * 1024 + vo); xb[s] = LDS4(L_TILE + 32768 + s * 1024 + vo); }
#pragma unroll
        for (int s = 0; s < 32; ++s) {
          if (s + 2 < 32) { xa[(s + 2) % 3] = LDS4(L_TILE + (s + 2) * 1024 + vo); xb[(s + 2) % 3] = LDS4(L_TILE + 32768 + (s + 2) * 1024 + vo); }
          SB();
          aA = mfma32(wq[s], xa[s % 3], aA);
          aB = mfma32(wq[s], xb[s % 3], aB);
          if (s >= 24) wo[s - 24] = ldw(rs, vo, wob, s - 24);
          SB();
        }
        KEEP(aA); KEEP(aB);
        STAMP(16 + lay * 24 + 2);
        final_stats<false>(L, lane, abA, abB);
        f32x4 csn[4];
#pragma unroll
        for (int u = 0; u < 4; ++u) csn[u] = LDSF4(L_CQ + ((u >> 1) * 64 + 16 * v + 8 * (u & 1) + 4 * hh) * 4);
        const float cexp = 0.25f * 1.4426950408889634f;
#pragma unroll
        for (int hf = 0; hf < 2; ++hf) {
          const f32x16& a = hf ? aB : aA;
          const f32x2 ab = hf ? abB : abA;
          f32x16 val;
#pragma unroll
          for (int q = 0; q < 16; ++q) { const float sc = q < 8 ? cexp : 1.0f; val[q] = a[q] * (ab.x * sc) + csn[q >> 2][q & 3] * (ab.y * sc); }
          const v4u K = nosent2(pk8(val, 8));
          st_sc1(K, rs, vo, KV_OFF + ((4 * j + v) * 64 + 2 * c + hf) * 2048);
          *(LAS v4u*)(L + L_KV + ((8 + hf) * 4 + v) * 2048 + vo) = K;
          *(LAS v4u*)(L + L_Q + (v * 2 + hf) * 1024 + vo) = pk8(val, 0);
        }
        __syncthreads();
      } else {
        const int kh = v & 1, pp = (v - 4) >> 1;
        const int x0 = L_TILE + kh * 32768 + kh * 16384 + vo, x1 = L_TILE + (1 - kh) * 32768 + kh * 16384 + vo;
        f32x16 a0 = Z, a1 = Z;
        v4u xa[3], xb[3];
#pragma unroll
        for (int s = 0; s < 2; ++s) { xa[s] = LDS4(x0 + s * 1024); xb[s] = LDS4(x1 + s * 1024); }
        final_stats<false>(L, lane, abA, abB);
#pragma unroll
        for (int s = 0; s < 16; ++s) {
          if (s + 2 < 16) { xa[(s + 2) % 3] = LDS4(x0 + (s + 2) * 1024); xb[(s + 2) % 3] = LDS4(x1 + (s + 2) * 1024); }
          SB();
          a0 = mfma32(xa[s % 3], wq[s], a0);
          a1 = mfma32(xb[s % 3], wq[s], a1);
          if (s >= 8) wo[s - 8] = ldw(rs, vo, wob, s - 8);
          SB();
        }
        KEEP(a0); KEEP(a1);
        STAMP(16 + lay * 24 + 2);
#pragma unroll
        for (int u = 0; u < 4; ++u) *(LAS f32x4*)(L + L_XCH + v * 4096 + u * 1024 + vo) = f32x4{a1[4 * u], a1[4 * u + 1], a1[4 * u + 2], a1[4 * u + 3]};
        __syncthreads();
        const float csv = *(LAS float*)(L + L_CQ + (128 + 32 * pp + tk) * 4);
        f32x16 val;
#pragma unroll
        for (int u = 0; u < 4; ++u) {
          const f32x4 o = LDSF4(L_XCH + (v ^ 1) * 4096 + u * 1024 + vo);
          const f32x4 t0 = LDSF4(L_FST + (kh * 32 + 8 * u + 4 * hh) * 8), t1 = LDSF4(L_FST + (kh * 32 + 8 * u + 4 * hh + 2) * 8);
          val[4 * u + 0] = (a0[4 * u + 0] + o[0]) * t0[0] + csv * t0[1];
          val[4 * u + 1] = (a0[4 * u + 1] + o[1]) * t0[2] + csv * t0[3];
          val[4 * u + 2] = (a0[4 * u + 2] + o[2]) * t1[0] + csv * t1[1];
          val[4 * u + 3] = (a0[4 * u + 3] + o[3]) * t1[2] + csv * t1[3];
        }
        const int hb = (lane >> 4) & 1, d = lane & 15;
#pragma unroll
        for (int s = 0; s < 2; ++s) {
          const v4u V = nosent2(pk8(val, 8 * s));
          st_sc1(V, rs, ((4 * j + 2 * pp + hb) * 64) * 2048 + 1024 + (32 * hh + 16 * s + d) * 16, KV_OFF + (2 * c + kh) * 2048);
          *(LAS v4u*)(L + L_KV + ((8 + kh) * 4 + 2 * pp + hb) * 2048 + 1024 + (32 * hh + 16 * s + d) * 16) = V;
        }
      }
      STAMP(16 + lay * 24 + 4);
      SB();
      v4u kvn[8];
      auto kv_issue = [&]() {
#pragma unroll
        for (int i = 0; i < 4; ++i) {
          const int kt = 2 * c - 8 + 4 * (v >> 2) + i;
          const int so = __builtin_amdgcn_readfirstlane(kt >= 0 ? KV_OFF + ((4 * j + (v & 3)) * 64 + kt) * 2048 : OOB);
          kvn[2 * i] = ld_sc1(rs, vo, so);
          kvn[2 * i + 1] = ld_sc1(rs, vo + 1024, so);
        }
      };
      WAITV0();
      __syncthreads();
      kv_issue();
      const int w1b = __builtin_amdgcn_readfirstlane(v < 7 ? W1_OFF + (j * 224 + v * 32) * KB : OOB);
#pragma unroll
      for (int i = 0; i < 4; ++i) w1[i] = ldw(rs, vo, w1b, i);
      SB();
      {
        const int tsl = ((v >> 1) & 1) * 32768 + (8 * j + 4 * (v & 1) + 2 * (v >> 2)) * 512;
        st_sc1(SENT4, rs, vo, X1T_OFF + c * TILE_B + tsl);
        if (v < 7) {
#pragma unroll
          for (int u = 0; u < 4; ++u) st_sc1(SENT4, rs, vo, HT_OFF + c * HT_C + (u >> 1) * 114688 + (28 * j + 4 * v + 2 * (u & 1)) * 512);
        }
        if (lay == 0) {
          st_sc1(SENT4, rs, vo, XT1_OFF + c * TILE_B + tsl);
          st_sc1(SENT4, rs, vo, OT1_OFF + c * TILE_B + (v >> 2) * 32768 + (2 * (4 * j + (v & 3))) * 512);
        }
      }
      SB();
      verify<8>(kvn, [&]() {
          kv_issue();
      }, tmo, 0x20, lane);
#pragma unroll
      for (int i = 0; i < 4; ++i) {
        *(LAS v4u*)(L + L_KV + ((4 * (v >> 2) + i) * 4 + (v & 3)) * 2048 + vo) = kvn[2 * i];
        *(LAS v4u*)(L + L_KV + ((4 * (v >> 2) + i) * 4 + (v & 3)) * 2048 + 1024 + vo) = kvn[2 * i + 1];
      }
      __syncthreads();
    }
    STAMP(1 + lay * 8);
    {
      FRESH();
      const int hq = v & 3, r = v >> 2, tq = tk - 4 * hh;
      const int kb = L_KV + (r * 4 + hq) * 2048;
      const v4u Q = LDS4(L_Q + (hq * 2 + r) * 1024 + vo);
      v4u Kf[9];
#pragma unroll
      for (int i = 0; i < 9; ++i) Kf[i] = LDS4(kb + i * 8192 + vo);
      const bool ones_lane = (lane & 16) != 0;
      const unsigned vsel = ones_lane ? 0u : 1u, vbase = ones_lane ? (unsigned)L_ONES : (unsigned)(kb + 1024 + (32 * hh + (lane & 15)) * 16);
      auto vop = [&](int i, int s) -> v4u { return LDS4(vbase + vsel * (unsigned)(i * 8192 + s * 256)); };
      const int first_ok = 8 - r - 2 * c;
      const int w1b = __builtin_amdgcn_readfirstlane(v < 7 ? W1_OFF + (j * 224 + v * 32) * KB : OOB);
      auto tmax = [&](const f32x16& x, float m) {
        const float a = max3f(x[0], x[1], x[2]), b2_ = max3f(x[3], x[4], x[5]), c2 = max3f(x[6], x[7], x[8]), d = max3f(x[9], x[10], x[11]), e = max3f(x[12], x[13], x[14]);
        return max3f(max3f(a, b2_, x[15]), max3f(c2, d, e), m);
      };
      f32x16 Sm, S[2];
      {
        f32x16 Sa = mfma32(Kf[0], Q, Z);
        const f32x16 Sb = mfma32(Kf[8], Q, Z);
        S[1] = mfma32(Kf[1], Q, Z);
        if (first_ok > 0) {
#pragma unroll
          for (int q = 0; q < 16; ++q) Sa[q] = -1e30f;
        }
#pragma unroll
        for (int q = 0; q < 16; ++q) { const int row = (q & 3) + 8 * (q >> 2); Sm[q] = row <= tq ? Sb[q] : Sa[q]; }
      }
      float mx = tmax(Sm, -1e30f);
      SB();
#pragma unroll
      for (int i = 1; i < 8; ++i) {
        if (i + 1 < 8) S[(i + 1) & 1] = mfma32(Kf[i + 1], Q, Z);
        if (i >= first_ok) mx = tmax(S[i & 1], mx);
        if (i < 6) w1[4 + (i - 1)] = ldw(rs, vo, w1b, 4 + (i - 1));
        SB();
      }
      { const f32x2 rr = both_halves(mx); mx = max3f(rr.x, rr.y, rr.y); }
      f32x16 Cm;
#pragma unroll
      for (int q = 0; q < 16; ++q) Cm[q] = -mx;
      S[1] = mfma32(Kf[1], Q, Cm);
      f32x16 O;
      {
        const v4u vn0 = vop(8, 0), vn1 = vop(8, 1), vo0 = vop(0, 0), vo1 = vop(0, 1);
        unsigned pp[8];
#pragma unroll
        for (int q = 0; q < 8; ++q) pp[q] = pk(__builtin_amdgcn_exp2f(Sm[2 * q] - mx), __builtin_amdgcn_exp2f(Sm[2 * q + 1] - mx));
        v4u pn[2], po[2];
#pragma unroll
        for (int q = 0; q < 8; ++q) {
          const int row0 = ((2 * q) & 3) + 8 * ((2 * q) >> 2);
          const unsigned mk = (row0 <= tq ? 0xFFFFu : 0u) | (row0 + 1 <= tq ? 0xFFFF0000u : 0u);
          pn[q >> 2][q & 3] = pp[q] & mk; po[q >> 2][q & 3] = pp[q] & ~mk;
        }
        O = mfma32(vn0, pn[0], Z); O = mfma32(vn1, pn[1], O);
        O = mfma32(vo0, po[0], O); O = mfma32(vo1, po[1], O);
        SB();
      }
#pragma unroll
      for (int i = 1; i < 8; ++i) {
        if (i + 1 < 8) S[(i + 1) & 1] = mfma32(Kf[i + 1], Q, Cm);
        const v4u va0 = vop(i, 0), va1 = vop(i, 1);
        if (i >= first_ok) {
          const f32x16& cur = S[i & 1];
          v4u pp[2];
#pragma unroll
          for (int q = 0; q < 8; ++q) pp[q >> 2][q & 3] = pk(__builtin_amdgcn_exp2f(cur[2 * q]), __builtin_amdgcn_exp2f(cur[2 * q + 1]));
          O = mfma32(va0, pp[0], O); O = mfma32(va1, pp[1], O);
        }
        w1[9 + (i - 1)] = ldw(rs, vo, w1b, 9 + (i - 1));
        SB();
      }
      KEEP(O);
      const float rl = __builtin_amdgcn_rcpf(O[8]);
      asm volatile("s_waitcnt vmcnt(12)" ::: "memory");
      st_sc1(nosent2(swap_T8(pk8(O, 0, rl))), rs, vo, OT_OFF + c * TILE_B + r * 32768 + (2 * (4 * j + hq)) * 512);
    }
    STAMP(2 + lay * 8);
    {
      FRESH();
      auto o_issue = [&]() {
#pragma unroll
        for (int i = 0; i < 8; ++i) st[i] = ld_seq(rs, vo, RFL(OT_OFF + c * TILE_B + v * 4096) + (i >> 2) * 32768, i & 3);
      };
      WAITV0();
      __syncthreads();
      o_issue();
      const int w1b = __builtin_amdgcn_readfirstlane(v < 7 ? W1_OFF + (j * 224 + v * 32) * KB : OOB);
#pragma unroll
      for (int i = 16; i < 24; ++i) w1[i] = ldw(rs, vo, w1b, i);
      verify<8>(st, [&]() {
          o_issue();
      }, tmo, 0x30, lane);
      STAMP(16 + lay * 24 + 8);
      fill_tile<false>(L, v, lane, st);
      __syncthreads();
      STAMP(16 + lay * 24 + 9);
      const int ho = (v >> 1) & 1;
      const int x0 = L_TILE + ho * 32768 + (v >> 1) * 8192 + vo, x1 = L_TILE + (1 - ho) * 32768 + (v >> 1) * 8192 + vo;
      v4u xs0[3], xs1[3];
#pragma unroll
      for (int s = 0; s < 2; ++s) { xs0[s] = LDS4(x0 + s * 1024); xs1[s] = LDS4(x1 + s * 1024); }
      f32x16 a0 = Z, a1 = Z;
#pragma unroll
      for (int s = 0; s < 8; ++s) {
        if (s + 2 < 8) { xs0[(s + 2) % 3] = LDS4(x0 + (s + 2) * 1024); xs1[(s + 2) % 3] = LDS4(x1 + (s + 2) * 1024); }
        SB();
        a0 = mfma32(wo[s], xs0[s % 3], a0); a1 = mfma32(wo[s], xs1[s % 3], a1);
        SB();
      }
      KEEP(a0); KEEP(a1);
      {
#pragma unroll
        for (int u = 0; u < 2; ++u) {
          *(LAS f32x4*)(L + L_XCH + (((v ^ 4) * 3 + 0) * 2 + u) * 1024 + vo) = f32x4{a0[8 + 4 * u], a0[9 + 4 * u], a0[10 + 4 * u], a0[11 + 4 * u]};
          *(LAS f32x4*)(L + L_XCH + (((v ^ 2) * 3 + 1) * 2 + u) * 1024 + vo) = f32x4{a1[4 * u], a1[1 + 4 * u], a1[2 + 4 * u], a1[3 + 4 * u]};
          *(LAS f32x4*)(L + L_XCH + (((v ^ 6) * 3 + 2) * 2 + u) * 1024 + vo) = f32x4{a1[8 + 4 * u], a1[9 + 4 * u], a1[10 + 4 * u], a1[11 + 4 * u]};
        }
      }
#pragma unroll
      for (int i = 24; i < 32; ++i) w1[i] = ldw(rs, vo, w1b, i);
      __syncthreads();
      f32x4 o0 = {a0[0], a0[1], a0[2], a0[3]}, o1 = {a0[4], a0[5], a0[6], a0[7]};
#pragma unroll
      for (int k = 0; k < 3; ++k) { o0 += LDSF4(L_XCH + ((v * 3 + k) * 2) * 1024 + vo); o1 += LDSF4(L_XCH + ((v * 3 + k) * 2 + 1) * 1024 + vo); }
      xr = v4u{pk(bflo(xr[0]) + o0[0], bfhi(xr[0]) + o0[1]), pk(bflo(xr[1]) + o0[2], bfhi(xr[1]) + o0[3]),
               pk(bflo(xr[2]) + o1[0], bfhi(xr[2]) + o1[1]), pk(bflo(xr[3]) + o1[2], bfhi(xr[3]) + o1[3])};
      st_sc1(nosent2(swap_T8(xr)), rs, vo, X1T_OFF + c * TILE_B + ((v >> 1) & 1) * 32768 + (8 * j + 4 * (v & 1) + 2 * (v >> 2)) * 512);
    }
    STAMP(3 + lay * 8);
    v4u hA[14];
    {
      FRESH();
      auto x_issue = [&]() {
#pragma unroll
        for (int i = 0; i < 8; ++i) st[i] = ld_seq(rs, vo, RFL(X1T_OFF + c * TILE_B + v * 4096) + (i >> 2) * 32768, i & 3);
      };
      const int w2b = __builtin_amdgcn_readfirstlane(W2_OFF + (j * 224 + v * 28) * KB);
      WAITV0();
      x_issue();
      verify<8>(st, [&]() {
          x_issue();
      }, tmo, 0x40, lane);
      STAMP(16 + lay * 24 + 10);
      fill_tile<true>(L, v, lane, st);
      __syncthreads();
      STAMP(16 + lay * 24 + 11);
      {
        const int hb = __builtin_amdgcn_readfirstlane(v < 7 ? HT_OFF + c * HT_C + (28 * j + 4 * v) * 512 : OOB);
        f32x16 aA = Z, aB = Z;
        v4u xa[3], xb[3];
#pragma unroll
        for (int s = 0; s < 2; ++s) { xa[s] = LDS4(L_TILE + s * 1024 + vo); xb[s] = LDS4(L_TILE + 32768 + s * 1024 + vo); }
        f32x2 abA, abB;
        final_stats<true>(L, lane, abA, abB);
#pragma unroll
        for (int s = 0; s < 32; ++s) {
          if (s + 2 < 32) { xa[(s + 2) % 3] = LDS4(L_TILE + (s + 2) * 1024 + vo); xb[(s + 2) % 3] = LDS4(L_TILE + 32768 + (s + 2) * 1024 + vo); }
          SB();
          aA = mfma32(w1[s], xa[s % 3], aA);
          aB = mfma32(w1[s], xb[s % 3], aB);
          if (s >= 4) w2[s - 4] = ldw(rs, vo, w2b, s - 4);
          SB();
        }
        KEEP(aA); KEEP(aB);
        STAMP(16 + lay * 24 + 12);
        const float c1 = -2.0f * 0.7978845608028654f * 1.4426950408889634f, c2 = c1 * 0.044715f;
#pragma unroll
        for (int hf = 0; hf < 2; ++hf) {
          const f32x16& a = hf ? aB : aA;
          const f32x2 ab = hf ? abB : abA;
          v4u ho[2];
#pragma unroll
          for (int u2 = 0; u2 < 2; ++u2) {
            f32x16 hv;
#pragma unroll
            for (int u = 2 * u2; u < 2 * u2 + 2; ++u) {
              const f32x4 csn = LDSF4(L_C1 + (32 * v + 8 * u + 4 * hh) * 4), bn = LDSF4(L_C1 + 1024 + (32 * v + 8 * u + 4 * hh) * 4);
#pragma unroll
              for (int i = 0; i < 4; ++i) {
                const float z = a[4 * u + i] * ab.x + (csn[i] * ab.y + bn[i]);
                const float e = __builtin_amdgcn_exp2f(z * (z * z * c2 + c1));
                hv[4 * u + i] = z * __builtin_amdgcn_rcpf(1.0f + e);
              }
            }
            ho[u2] = nosent2(swap_T8(pk8(hv, 8 * u2)));
            if (hf == 0) st_sc1(ho[u2], rs, vo, hb + 2 * u2 * 512);
          }
          if (hf == 0) STAMP(16 + lay * 24 + 13);
          else {
#pragma unroll
            for (int s = 0; s < 14; ++s) hA[s] = ld_seq(rs, vo, RFL(HT_OFF + c * HT_C + 28 * v * 512), s);
            SB();
#pragma unroll
            for (int u2 = 0; u2 < 2; ++u2) st_sc1(ho[u2], rs, vo, hb + 114688 + 2 * u2 * 512);
          }
        }
      }
    }
    STAMP(4 + lay * 8);
    {
      FRESH();
      f32x16 ac[2];
      const int wqb = __builtin_amdgcn_readfirstlane(lay == 0 ? WQ_OFF + (j * 192 + (v < 4 ? v * 32 : 128 + (v - 4) * 16)) * KB : OOB);
      const int wqb2 = __builtin_amdgcn_readfirstlane(v < 4 ? wqb : OOB);
#pragma unroll
      for (int hf = 0; hf < 2; ++hf) {
        verify<14>(hA, [&]() {
#pragma unroll
            for (int s = 0; s < 14; ++s) hA[s] = ld_seq(rs, vo, RFL(HT_OFF + c * HT_C + 28 * v * 512) + hf * 114688, s);
      }, tmo, 0x50 + hf, lane);
        STAMP(16 + lay * 24 + 17 + 2 * hf);
        ac[0] = Z; ac[1] = Z;
#pragma unroll
        for (int s = 0; s < 14; ++s) {
          ac[0] = mfma32(w2[2 * s], hA[s], ac[0]);
          ac[1] = mfma32(w2[2 * s + 1], hA[s], ac[1]);
          if (hf == 1) wq[s] = ldw(rs, vo, wqb, s);
          SB();
        }
        KEEP(ac[0]); KEEP(ac[1]);
        if (hf == 0) {
          WAITV0();
#pragma unroll
          for (int s = 0; s < 14; ++s) hA[s] = ld_seq(rs, vo, RFL(HT_OFF + c * HT_C + 28 * v * 512) + 114688, s);
          SB();
        }
        STAMP(16 + lay * 24 + 18 + 2 * hf);
#pragma unroll
        for (int nt = 0; nt < 2; ++nt)
#pragma unroll
          for (int u = 0; u < 4; ++u) {
            const int o = nt + 2 * hf + 4 * (u >> 1);
            *(LAS f32x4*)(L + (o * 8 + v) * 2048 + (u & 1) * 1024 + vo) = f32x4{ac[nt][4 * u], ac[nt][4 * u + 1], ac[nt][4 * u + 2], ac[nt][4 * u + 3]};
          }
      }
#pragma unroll
      for (int i = 14; i < 16; ++i) wq[i] = ldw(rs, vo, wqb, i);
#pragma unroll
      for (int i = 16; i < 32; ++i) wq[i] = ldw(rs, vo, wqb2, i);
      __syncthreads();
      f32x4 s0 = {0, 0, 0, 0}, s1 = {0, 0, 0, 0};
#pragma unroll
      for (int k = 0; k < 8; ++k) { s0 += LDSF4((v * 8 + k) * 2048 + vo); s1 += LDSF4((v * 8 + k) * 2048 + 1024 + vo); }
      const int bcol = 192 + 32 * (v & 1) + 16 * (v >> 2) + 4 * hh;
      const f32x4 b0 = LDSF4(L_CQ + bcol * 4), b1 = LDSF4(L_CQ + (bcol + 8) * 4);
      xr = v4u{pk(bflo(xr[0]) + s0[0] + b0[0], bfhi(xr[0]) + s0[1] + b0[1]), pk(bflo(xr[1]) + s0[2] + b0[2], bfhi(xr[1]) + s0[3] + b0[3]),
               pk(bflo(xr[2]) + s1[0] + b1[0], bfhi(xr[2]) + s1[1] + b1[1]), pk(bflo(xr[3]) + s1[2] + b1[2], bfhi(xr[3]) + s1[3] + b1[3])};
      const v4u o = swap_T8(xr);
      if (lay == 0) {
        st_sc1(nosent2(o), rs, vo, XT1_OFF + c * TILE_B + ((v >> 1) & 1) * 32768 + (8 * j + 4 * (v & 1) + 2 * (v >> 2)) * 512);
      } else {
        *(v4u*)(p.out + (size_t)(64 * c + 32 * ((v >> 1) & 1) + tk) * DM + 64 * j + 32 * (v & 1) + 16 * (v >> 2) + 8 * hh) = o;
      }
      WAITV0();
      const int xnb = __builtin_amdgcn_readfirstlane(lay == 0 ? XT1_OFF + c * TILE_B + v * 4096 : OOB);
#pragma unroll
      for (int i = 0; i < 8; ++i) st[i] = ld_seq(rs, vo, xnb + (i >> 2) * 32768, i & 3);
      __syncthreads();
    }
    STAMP(5 + lay * 8);
  }
  STAMP_FLUSH();
}
}

extern "C" void kernel_launch(void* const* d_in, const int* in_sizes, int n_in, void* d_out, int out_size, void* d_ws, size_t ws_size,
                              hipStream_t stream) {
  using namespace fk;
  Params p;
  p.x = (const u16*)d_in[0]; p.wqkv = (const u16*)d_in[1]; p.wo = (const u16*)d_in[2];
  p.w1 = (const u16*)d_in[4]; p.b1 = (const u16*)d_in[5]; p.w2 = (const u16*)d_in[6]; p.b2 = (const u16*)d_in[7];
  p.out = (u16*)d_out; p.ws = (unsigned char*)d_ws;
  static bool attr_set = false;
  if (!attr_set) { (void)hipFuncSetAttribute((const void*)k_main, hipFuncAttributeMaxDynamicSharedMemorySize, L_BYTES); attr_set = true; }
  k_prep<<<PREP_BLOCKS, 256, 0, stream>>>(p);
  k_main<<<256, 512, L_BYTES, stream>>>(p);
}
```

```cpp
#include <hip/hip_runtime.h>
#include <stdint.h>

namespace fk {
typedef unsigned short u16;
typedef unsigned v4u __attribute__((ext_vector_type(4)));
typedef unsigned v2u __attribute__((ext_vector_type(2)));
typedef float f32x4 __attribute__((ext_vector_type(4)));
typedef float f32x2 __attribute__((ext_vector_type(2)));
typedef float f32x16 __attribute__((ext_vector_type(16)));
typedef __bf16 bf8v __attribute__((ext_vector_type(8)));
typedef __bf16 bf2v __attribute__((ext_vector_type(2)));
#define LAS __attribute__((address_space(3)))
#define GAS __attribute__((address_space(1)))
typedef __amdgpu_buffer_rsrc_t rsrc_t;

constexpr int SEQ = 2048, DM = 512, FF = 1792;
constexpr int KB = 1024;
constexpr int WQ_OFF = 0;
constexpr int WO_OFF = WQ_OFF + 8 * 192 * KB;
constexpr int W1_OFF = WO_OFF + 8 * 64 * KB;
constexpr int W2_OFF = W1_OFF + 8 * 224 * KB;
constexpr int CS_OFF = W2_OFF + 8 * 224 * KB;
constexpr int CTRL_OFF = CS_OFF + 16 * KB;
constexpr int TILE_B = 64 * KB;
constexpr int KV_B = 32 * 64 * 2 * KB;
constexpr int HT_C = 2 * 224 * 512;
constexpr int KV0_OFF = CTRL_OFF + 16 * KB;
constexpr int OT0_OFF = KV0_OFF + KV_B;
constexpr int KV1_OFF = OT0_OFF + 32 * TILE_B;
constexpr int OT1_OFF = KV1_OFF + KV_B;
constexpr int X1T0_OFF = OT1_OFF + 32 * TILE_B;
constexpr int X1T1_OFF = X1T0_OFF + 32 * TILE_B;
constexpr int XT1_OFF = X1T1_OFF + 32 * TILE_B;
constexpr int HT0_OFF = XT1_OFF + 32 * TILE_B;
constexpr int HT1_OFF = HT0_OFF + 32 * HT_C;
constexpr int STAMP_OFF = HT1_OFF + 32 * HT_C;
constexpr int WS_END = STAMP_OFF + 256 * 8 * 128 * 8;
constexpr unsigned SENT = 0xFFFFFFFFu;
constexpr int AUX_SC1 = 16;
constexpr unsigned SPIN_CAP = 1u << 14;

constexpr int L_TILE = 0;
constexpr int L_KV = 0;
constexpr int L_RB = 64 * KB;
constexpr int L_Q = 80 * KB;
constexpr int L_XCH = 88 * KB;
constexpr int L_PST = 136 * KB;
constexpr int L_FST = 144 * KB;
constexpr int L_C1 = 145 * KB;
constexpr int L_CQ = 147 * KB;
constexpr int L_ONES = 148 * KB;
constexpr int L_BYTES = 148 * KB + 64;

__device__ __forceinline__ float bf2f(unsigned b) { return __builtin_bit_cast(float, b << 16); }
__device__ __forceinline__ float bflo(unsigned d) { return __builtin_bit_cast(float, d << 16); }
__device__ __forceinline__ float bfhi(unsigned d) { return __builtin_bit_cast(float, d & 0xFFFF0000u); }
__device__ __forceinline__ unsigned pk(float lo, float hi) {
  bf2v r = {(__bf16)lo, (__bf16)hi};
  return __builtin_bit_cast(unsigned, r);
}
__device__ __forceinline__ f32x4 mfma16(v4u a, v4u b, f32x4 c) {
  return __builtin_amdgcn_mfma_f32_16x16x32_bf16(__builtin_bit_cast(bf8v, a), __builtin_bit_cast(bf8v, b), c, 0, 0, 0);
}
__device__ __forceinline__ unsigned nosent(unsigned d) { return d < 0xFFFFFFFEu ? d : 0xFFFFFFFEu; }
__device__ __forceinline__ v4u nosent4(v4u v) { return v4u{nosent(v.x), nosent(v.y), nosent(v.z), nosent(v.w)}; }
__device__ __forceinline__ v4u pack_T(f32x4 a, f32x4 b) {
  unsigned x0 = pk(a[0], a[1]), x1 = pk(a[2], a[3]), y0 = pk(b[0], b[1]), y1 = pk(b[2], b[3]);
  auto r0 = __builtin_amdgcn_permlane16_swap(x0, y0, false, false);
  auto r1 = __builtin_amdgcn_permlane16_swap(x1, y1, false, false);
  return v4u{r0[0], r1[0], r0[1], r1[1]};
}
__device__ __forceinline__ v2u pk4(f32x4 a) { return v2u{pk(a[0], a[1]), pk(a[2], a[3])}; }
__device__ __forceinline__ f32x4 unpk4(v2u v) { return f32x4{bflo(v.x), bfhi(v.x), bflo(v.y), bfhi(v.y)}; }
__device__ __forceinline__ v4u swap_T(v2u x, v2u y) {
  auto r0 = __builtin_amdgcn_permlane16_swap(x.x, y.x, false, false);
  auto r1 = __builtin_amdgcn_permlane16_swap(x.y, y.y, false, false);
  return v4u{r0[0], r1[0], r0[1], r1[1]};
}
#define KEEP(x) asm volatile("" : "+v"(x))
#define WAITV0() asm volatile("s_waitcnt vmcnt(0)" ::: "memory")
#define PRIO(x) __builtin_amdgcn_s_setprio(x)
#define SB() __builtin_amdgcn_sched_barrier(0)

__device__ __forceinline__ v4u ldw(rsrc_t rs, int voff, int sbase, int i) {
  return __builtin_amdgcn_raw_buffer_load_b128(rs, voff + (i & 3) * 1024, sbase + (i >> 2) * 4096, 0);
}
__device__ __forceinline__ v4u ld_sc1(rsrc_t rs, int voff, int soff) {
  return __builtin_amdgcn_raw_buffer_load_b128(rs, voff, soff, AUX_SC1); }
__device__ __forceinline__ v4u ld_seq(rsrc_t rs, int voff, int sbase, int i) { return ld_sc1(rs, voff + (i & 3) * 1024, sbase + (i >> 2) * 4096); }
#define RFL(x) __builtin_amdgcn_readfirstlane(x)
__device__ __forceinline__ void st_sc1(v4u v, rsrc_t rs, int voff, int soff) {
  __builtin_amdgcn_raw_buffer_store_b128(v, rs, voff + soff, 0, AUX_SC1); }
__device__ __forceinline__ unsigned max4(unsigned m, v4u d) {
  m = __builtin_elementwise_max(__builtin_elementwise_max(m, d.x), d.y);
  return __builtin_elementwise_max(__builtin_elementwise_max(m, d.z), d.w);
}
__device__ __forceinline__ bool spin_fail(unsigned& spins, GAS unsigned* tmo, unsigned code, int lane) {
  ++spins;
  if (spins > SPIN_CAP) {
    if (lane == 0) __hip_atomic_store(tmo, code, __ATOMIC_RELAXED, __HIP_MEMORY_SCOPE_AGENT);
    return true;
  }
  if ((spins & 63u) == 0u && __hip_atomic_load(tmo, __ATOMIC_RELAXED, __HIP_MEMORY_SCOPE_AGENT) != 0u) return true;
  return false;
}

struct Params {
  const u16* x;
  const u16* wqkv;
  const u16* wo;
  const u16* w1;
  const u16* b1;
  const u16* w2;
  const u16* b2;
  u16* out;
  unsigned char* ws;
};

constexpr int NFRAG = 8 * 704;
constexpr int PB_TILE = NFRAG * 16 / 256;
constexpr int PB_CS = (1536 + 1792) / 64;
constexpr int CLEAN_B = KV_B + 32 * TILE_B;
constexpr int PB_CLEAN = CLEAN_B / (256 * 64);
constexpr int PREP_BLOCKS = PB_TILE + PB_CS + PB_CLEAN;

__global__ void __launch_bounds__(256) k_prep(Params p) {
  const int b = blockIdx.x, tid = threadIdx.x;
  if (b < PB_TILE) {
    const int F = b * 16 + (tid >> 4), t16 = tid & 15, cq = t16 & 7, h = t16 >> 3;
    const u16* W; int ldw_, k0, col;
    if (F < 8 * 192) {
      const int j = F / 192, i = F % 192;
      W = p.wqkv; ldw_ = 1536;
      if (i < 128) { const int head = 4 * j + (i >> 5); k0 = 16 * (i & 31); col = cq < 4 ? head * 16 + 4 * cq : 512 + head * 16 + 4 * (cq - 4); }
      else { const int u = i - 128, wv = u >> 4; k0 = 16 * (16 * (wv & 1) + (u & 15)); col = 1024 + (4 * j + 2 * (wv >> 1)) * 16 + 4 * cq; }
    } else if (F < 8 * 256) {
      const int f = F - 8 * 192, j = f >> 6, u = f & 63, v = u >> 3;
      W = p.wo; ldw_ = 512; k0 = 16 * (8 * (v >> 1) + (u & 7)); col = 64 * j + 32 * (v & 1) + 4 * (cq ^ (2 * (v >> 1)));
    } else if (F < 8 * 480) {
      const int f = F - 8 * 256, j = f / 224, u = f % 224;
      W = p.w1; ldw_ = 1792; k0 = 16 * (u & 31); col = 224 * j + 32 * (u >> 5) + 4 * cq;
    } else {
      const int f = F - 8 * 480, j = f / 224, u = f % 224, v = u / 28, q = u % 28;
      W = p.w2; ldw_ = 512; k0 = 224 * v + 16 * (q >> 1); col = 64 * j + 32 * (q & 1) + 4 * cq;
    }
    const u16* src = W + (size_t)(k0 + 8 * h) * ldw_ + col;
    v2u r[8];
#pragma unroll
    for (int e = 0; e < 8; ++e) r[e] = *(const v2u*)(src + (size_t)e * ldw_);
    v4u o[4];
#pragma unroll
    for (int e = 0; e < 4; ++e) {
      const unsigned a0 = r[2 * e].x, a1 = r[2 * e + 1].x, c0 = r[2 * e].y, c1 = r[2 * e + 1].y;
      o[0][e] = (a0 & 0xFFFFu) | (a1 << 16);
      o[1][e] = (a0 >> 16) | (a1 & 0xFFFF0000u);
      o[2][e] = (c0 & 0xFFFFu) | (c1 << 16);
      o[3][e] = (c0 >> 16) | (c1 & 0xFFFF0000u);
    }
    v4u* dst = (v4u*)(p.ws + (size_t)F * 1024 + (h * 32 + 4 * cq) * 16);
#pragma unroll
    for (int i = 0; i < 4; ++i) dst[i] = o[i];
  } else if (b < PB_TILE + PB_CS) {
    __shared__ float red[16][64];
    const int cb = b - PB_TILE;
    const u16* W; int ldw_, col;
    if (cb < 24) { W = p.wqkv; ldw_ = 1536; col = cb * 64; } else { W = p.w1; ldw_ = 1792; col = (cb - 24) * 64; }
    const int cq = tid & 15, rg = tid >> 4;
    float s0 = 0, s1 = 0, s2 = 0, s3 = 0;
    const u16* src = W + (size_t)(rg * 32) * ldw_ + col + 4 * cq;
#pragma unroll
    for (int e = 0; e < 32; ++e) {
      const v2u v = *(const v2u*)(src + (size_t)e * ldw_);
      s0 += bflo(v.x); s1 += bfhi(v.x); s2 += bflo(v.y); s3 += bfhi(v.y);
    }
    red[rg][4 * cq + 0] = s0; red[rg][4 * cq + 1] = s1; red[rg][4 * cq + 2] = s2; red[rg][4 * cq + 3] = s3;
    __syncthreads();
    if (tid < 64) {
      float s = 0;
#pragma unroll
      for (int r = 0; r < 16; ++r) s += red[r][tid];
      ((float*)(p.ws + CS_OFF))[cb * 64 + tid] = s;
    }
    if (cb == 0 && tid == 0) *(unsigned*)(p.ws + CTRL_OFF) = 0u;
  } else {
    const int cb = b - PB_TILE - PB_CS;
    v4u* dst = (v4u*)(p.ws + KV0_OFF + (size_t)cb * (256 * 64)) + tid;
    const v4u s = {SENT, SENT, SENT, SENT};
#pragma unroll
    for (int i = 0; i < 4; ++i) dst[i * 256] = s;
  }
}

#define STAMP(i) do {} while (0)
#define STAMP_FLUSH() do {} while (0)

__device__ __forceinline__ float max3f(float a, float b, float c) { float r; asm("v_max3_f32 %0, %1, %2, %3" : "=v"(r) : "v"(a), "v"(b), "v"(c)); return r; }
__device__ __forceinline__ unsigned max3u(unsigned a, unsigned b, unsigned c) { unsigned r; asm("v_max3_u32 %0, %1, %2, %3" : "=v"(r) : "v"(a), "v"(b), "v"(c)); return r; }
__device__ __forceinline__ f32x16 mfma32(v4u a, v4u b, f32x16 c) {
  return __builtin_amdgcn_mfma_f32_32x32x16_bf16(__builtin_bit_cast(bf8v, a), __builtin_bit_cast(bf8v, b), c, 0, 0, 0);
}
template <int N> __device__ __forceinline__ bool any_sent(const v4u (&d)[N]) {
  unsigned m0 = 0, m1 = 0;
#pragma unroll
  for (int i = 0; i < N; ++i) { if (i & 1) m1 = max3u(m1, d[i].x, d[i].z); else m0 = max3u(m0, d[i].x, d[i].z); }
  return __any(max3u(m0, m1, 0) == SENT);
}
template <int N> __device__ __forceinline__ void issue(v4u (&d)[N], rsrc_t rs, int voff, int soff, int stride) {
#pragma unroll
  for (int i = 0; i < N; ++i) d[i] = ld_sc1(rs, voff, soff + i * stride);
}
template <int N, class F> __device__ __forceinline__ void verify(v4u (&d)[N], F reissue, GAS unsigned* tmo, unsigned code, int lane) {
  if (any_sent<N>(d)) {
    unsigned spins = 0;
    do {
      __builtin_amdgcn_s_sleep(2);
      asm volatile("" ::: "memory");
      reissue();
    } while (any_sent<N>(d) && !spin_fail(spins, tmo, code, lane));
  }
}
__device__ __forceinline__ f32x2 both_halves(float v) {
  const unsigned a = __builtin_bit_cast(unsigned, v);
  auto r = __builtin_amdgcn_permlane32_swap(a, a, false, false);
  const unsigned r0 = r[0], r1 = r[1];
  return f32x2{__builtin_bit_cast(float, r0), __builtin_bit_cast(float, r1)};
}
__device__ __forceinline__ float dot2(unsigned a, unsigned b, float c) {
  return __builtin_amdgcn_fdot2_f32_bf16(__builtin_bit_cast(bf2v, a), __builtin_bit_cast(bf2v, b), c, false);
}
__device__ __forceinline__ v4u nosent2(v4u v) { return v4u{nosent(v.x), v.y, nosent(v.z), v.w}; }
__device__ __forceinline__ f32x4 round4(f32x4 s) {
  const unsigned u0 = pk(s[0], s[1]), u1 = pk(s[2], s[3]);
  return f32x4{bflo(u0), bfhi(u0), bflo(u1), bfhi(u1)};
}

__device__ __forceinline__ v4u pk8(const f32x16& a, int r0, float sc = 1.0f) {
  return v4u{pk(a[r0] * sc, a[r0 + 1] * sc), pk(a[r0 + 2] * sc, a[r0 + 3] * sc), pk(a[r0 + 4] * sc, a[r0 + 5] * sc), pk(a[r0 + 6] * sc, a[r0 + 7] * sc)};
}
__device__ __forceinline__ v4u swap_T8(v4u p) {
  auto r0 = __builtin_amdgcn_permlane32_swap(p[0], p[2], false, false);
  auto r1 = __builtin_amdgcn_permlane32_swap(p[1], p[3], false, false);
  return v4u{r0[0], r1[0], r0[1], r1[1]};
}
__device__ __forceinline__ f32x4 q4(const f32x16& a, int u) { return f32x4{a[4 * u], a[4 * u + 1], a[4 * u + 2], a[4 * u + 3]}; }
#define FRESH() int lane = lane_, c = c_, j = j_, v = v_; asm volatile("" : "+v"(lane), "+s"(c), "+s"(j), "+s"(v)); \
  const int vo = lane * 16, tk = lane & 31, hh = lane >> 5; (void)vo; (void)tk; (void)hh
#define LDS4(off) (*(const LAS v4u*)(L + (off)))
#define LDSF4(off) (*(const LAS f32x4*)(L + (off)))
constexpr int OOB = 0x7FF00000;

template <bool STATS>
__device__ __forceinline__ void fill_half(LAS unsigned char* L, int hf, int v, int lane, const v4u (&d)[4]) {
#pragma unroll
  for (int i = 0; i < 4; ++i) *(LAS v4u*)(L + L_TILE + hf * 32768 + v * 4096 + i * 1024 + lane * 16) = d[i];
  if (STATS) {
    float s0 = 0.f, s1 = 0.f, q0 = 0.f, q1 = 0.f;
#pragma unroll
    for (int i = 0; i < 4; ++i) {
      const v4u x = d[i];
      s0 = dot2(x[0], 0x3F803F80u, s0); q0 = dot2(x[0], x[0], q0);
      s1 = dot2(x[1], 0x3F803F80u, s1); q1 = dot2(x[1], x[1], q1);
      s0 = dot2(x[2], 0x3F803F80u, s0); q0 = dot2(x[2], x[2], q0);
      s1 = dot2(x[3], 0x3F803F80u, s1); q1 = dot2(x[3], x[3], q1);
    }
    *(LAS f32x2*)(L + L_PST + (((hf * 8 + v) * 2 + (lane >> 5)) * 32 + (lane & 31)) * 8) = f32x2{s0 + s1, q0 + q1};
  }
}
template <bool DOUBLE_LN>
__device__ __forceinline__ f32x2 final_stats(LAS unsigned char* L, int lane, int hf) {
  const int t = lane & 31, hh = lane >> 5;
  f32x2 pv[8];
#pragma unroll
  for (int k = 0; k < 8; ++k) pv[k] = *(LAS f32x2*)(L + L_PST + ((hf * 16 + hh * 8 + k) * 32 + t) * 8);
  SB();
  const f32x2 s = ((pv[0] + pv[1]) + (pv[2] + pv[3])) + ((pv[4] + pv[5]) + (pv[6] + pv[7]));
  const f32x2 rs_ = both_halves(s.x), rq = both_halves(s.y);
  const float mean = (rs_.x + rs_.y) * (1.0f / DM);
  float var = (rq.x + rq.y) * (1.0f / DM) - mean * mean;
  var = var > 0.f ? var : 0.f;
  float r = __builtin_amdgcn_rsqf(var + 1e-5f);
  if (DOUBLE_LN) {
    const float v2 = var * r * r;
    r *= __builtin_amdgcn_rsqf(v2 + 1e-5f);
  }
  const f32x2 ab = {r, -mean * r};
  *(LAS f32x2*)(L + L_FST + (hf * 32 + t) * 8) = ab;
  return ab;
}

__global__ void __launch_bounds__(512, 2) k_main(Params p) {
  extern __shared__ __attribute__((aligned(16))) unsigned char smem[];
  LAS unsigned char* L = (LAS unsigned char*)smem;
  const int b = blockIdx.x, tid = threadIdx.x, lane_ = tid & 63, v_ = __builtin_amdgcn_readfirstlane(tid >> 6);
#define MAPPING 1
  int c_, j_;
  if (MAPPING == 0) { c_ = (b & 7) * 4 + (b >> 6); j_ = (b >> 3) & 7; }
  else { c_ = ((b & 7) >> 1) * 8 + (b >> 5); j_ = (b & 1) * 4 + ((b >> 3) & 3); }
  const rsrc_t rs = __builtin_amdgcn_make_buffer_rsrc((void*)p.ws, 0, WS_END, 0x00020000);
  const rsrc_t rx = __builtin_amdgcn_make_buffer_rsrc((void*)p.x, 0, SEQ * DM * 2, 0x00020000);
  GAS unsigned* tmo = (GAS unsigned*)(p.ws + CTRL_OFF);
  STAMP(0);
  v2u xr[2]; v4u wq[32], stA[4];
  f32x16 Z;
#pragma unroll
  for (int q = 0; q < 16; ++q) Z[q] = 0.f;
  {
    FRESH();
#pragma unroll
    for (int i = 0; i < 4; ++i) stA[i] = __builtin_amdgcn_raw_buffer_load_b128(rx, tk * 1024 + hh * 16 + i * 32, (64 * c) * 1024 + v * 128, 0);
    const int wqb = RFL(WQ_OFF + (j * 192 + (v < 4 ? v * 32 : 128 + (v - 4) * 16)) * KB), wqb2 = RFL(v < 4 ? wqb : OOB);
#pragma unroll
    for (int i = 0; i < 16; ++i) wq[i] = ldw(rs, vo, wqb, i);
#pragma unroll
    for (int i = 16; i < 32; ++i) wq[i] = ldw(rs, vo, wqb2, i);
    SB();
    const float* cs = (const float*)(p.ws + CS_OFF);
    if (tid < 224) {
      *(LAS float*)(L + L_C1 + tid * 4) = cs[1536 + 224 * j + tid];
      *(LAS float*)(L + L_C1 + 1024 + tid * 4) = bf2f(p.b1[224 * j + tid]);
    } else if (tid >= 256) {
      const int t = tid - 256;
      if (t < 192) *(LAS float*)(L + L_CQ + t * 4) = cs[(t >> 6) * 512 + 64 * j + (t & 63)];
      else *(LAS float*)(L + L_CQ + t * 4) = bf2f(p.b2[64 * j + t - 192]);
    } else if (tid < 228) *(LAS unsigned*)(L + L_ONES + (tid - 224) * 4) = 0x3F803F80u;
#pragma unroll
    for (int hf = 0; hf < 2; ++hf) xr[hf] = *(const v2u*)(p.x + (size_t)(64 * c + 32 * hf + tk) * DM + 64 * j + 32 * (v & 1) + 8 * (v >> 1) + 4 * hh);
    v4u SENT4 = {SENT, SENT, SENT, SENT};
    asm volatile("" : "+v"(SENT4));
#pragma unroll
    for (int hf = 0; hf < 2; ++hf) {
      if (v < 4) st_sc1(SENT4, rs, vo, KV1_OFF + ((4 * j + v) * 64 + 2 * c + hf) * 2048);
      else st_sc1(SENT4, rs, ((4 * j + 2 * ((v - 4) >> 1) + ((lane >> 4) & 1)) * 64) * 2048 + 1024 + (32 * hh + 16 * (v & 1) + (lane & 15)) * 16, KV1_OFF + (2 * c + hf) * 2048);
    }
    SB();
  }

  for (int lay = 0; lay < 2; ++lay) {
    const int KV_OFF = lay ? KV1_OFF : KV0_OFF, OT_OFF = lay ? OT1_OFF : OT0_OFF, X1T_OFF = lay ? X1T1_OFF : X1T0_OFF, HT_OFF = lay ? HT1_OFF : HT0_OFF;
    v4u wo[8], w1[32], w2[28], stB[4];
    {
      FRESH();
      const int xtb = RFL(XT1_OFF + c * TILE_B + v * 4096);
      const int wob = RFL(WO_OFF + (j * 64 + v * 8) * KB);
      f32x16 acc[2];
      auto xb_issue = [&]() {
        if (lay) {
#pragma unroll
          for (int i = 0; i < 4; ++i) stB[i] = ld_seq(rs, vo, xtb + 32768, i);
        } else {
#pragma unroll
          for (int i = 0; i < 4; ++i) stB[i] = __builtin_amdgcn_raw_buffer_load_b128(rx, tk * 1024 + hh * 16 + i * 32, (64 * c + 32) * 1024 + v * 128, 0);
        }
      };
      auto pass_qk = [&](int hf) {
        v4u xs[3];
        const int x0 = L_TILE + hf * 32768 + vo;
#pragma unroll
        for (int s = 0; s < 2; ++s) xs[s] = LDS4(x0 + s * 1024);
        acc[hf] = Z;
        PRIO(1);
#pragma unroll
        for (int s = 0; s < 32; ++s) {
          if (s + 2 < 32) xs[(s + 2) % 3] = LDS4(x0 + (s + 2) * 1024);
          SB();
          acc[hf] = mfma32(wq[s], xs[s % 3], acc[hf]);
          if (hf == 0 && s == 12) xb_issue();
          if (hf == 1 && s >= 24) wo[s - 24] = ldw(rs, vo, wob, s - 24);
          SB();
        }
        KEEP(acc[hf]);
        PRIO(0);
      };
      auto pass_v = [&](int hf) {
        const int kh = v & 1;
        v4u xs[3];
        const int x0 = L_TILE + hf * 32768 + kh * 16384 + (vo ^ (kh * 256));
#pragma unroll
        for (int s = 0; s < 2; ++s) xs[s] = LDS4(x0 + s * 1024);
        acc[hf] = Z;
        PRIO(1);
#pragma unroll
        for (int s = 0; s < 16; ++s) {
          if (s + 2 < 16) xs[(s + 2) % 3] = LDS4(x0 + (s + 2) * 1024);
          SB();
          acc[hf] = mfma32(xs[s % 3], wq[s], acc[hf]);
          if (hf == 0 && s == 12) xb_issue();
          if (hf == 1 && s >= 8) wo[s - 8] = ldw(rs, vo, wob, s - 8);
          SB();
        }
        KEEP(acc[hf]);
        PRIO(0);
#pragma unroll
        for (int u = 0; u < 2; ++u) *(LAS f32x4*)(L + L_XCH + ((hf * 8 + v) * 2 + u) * 1024 + vo) = q4(acc[hf], 2 + u);
      };
      auto epi_qk = [&](int hf) {
        const f32x2 ab = final_stats<false>(L, lane, hf);
        f32x4 csn[4];
#pragma unroll
        for (int u = 0; u < 4; ++u) csn[u] = LDSF4(L_CQ + ((u >> 1) * 64 + 16 * v + 8 * (u & 1) + 4 * hh) * 4);
        const float cexp = 0.25f * 1.4426950408889634f;
        f32x16 val;
#pragma unroll
        for (int q = 0; q < 16; ++q) { const float sc = q < 8 ? cexp : 1.0f; val[q] = acc[hf][q] * (ab.x * sc) + csn[q >> 2][q & 3] * (ab.y * sc); }
        const v4u K = nosent2(pk8(val, 8));
        st_sc1(K, rs, vo, KV_OFF + ((4 * j + v) * 64 + 2 * c + hf) * 2048);
        *(LAS v4u*)(L + L_KV + ((8 + hf) * 4 + v) * 2048 + vo) = K;
        *(LAS v4u*)(L + L_Q + (v * 2 + hf) * 1024 + vo) = pk8(val, 0);
      };
      auto epi_v = [&](int hf) {
        const int kh = v & 1, pp = (v - 4) >> 1;
        (void)final_stats<false>(L, lane, hf);
        const float csv = *(LAS float*)(L + L_CQ + (128 + 32 * pp + tk) * 4);
        f32x16 val;
#pragma unroll
        for (int u = 0; u < 2; ++u) {
          const f32x4 o = LDSF4(L_XCH + ((hf * 8 + (v ^ 1)) * 2 + u) * 1024 + vo);
          const f32x4 t0 = LDSF4(L_FST + (hf * 32 + 16 * kh + 8 * u + 4 * hh) * 8), t1 = LDSF4(L_FST + (hf * 32 + 16 * kh + 8 * u + 4 * hh + 2) * 8);
          val[4 * u + 0] = (acc[hf][4 * u + 0] + o[0]) * t0[0] + csv * t0[1];
          val[4 * u + 1] = (acc[hf][4 * u + 1] + o[1]) * t0[2] + csv * t0[3];
          val[4 * u + 2] = (acc[hf][4 * u + 2] + o[2]) * t1[0] + csv * t1[1];
          val[4 * u + 3] = (acc[hf][4 * u + 3] + o[3]) * t1[2] + csv * t1[3];
        }
        const int hb = (lane >> 4) & 1, d = lane & 15;
        const v4u V = nosent2(pk8(val, 0));
        st_sc1(V, rs, ((4 * j + 2 * pp + hb) * 64) * 2048 + 1024 + (32 * hh + 16 * kh + d) * 16, KV_OFF + (2 * c + hf) * 2048);
        *(LAS v4u*)(L + L_KV + ((8 + hf) * 4 + 2 * pp + hb) * 2048 + 1024 + (32 * hh + 16 * kh + d) * 16) = V;
      };
      if (lay) verify<4>(stA, [&]() {
#pragma unroll
        for (int i = 0; i < 4; ++i) stA[i] = ld_seq(rs, vo, xtb, i);
      }, tmo, 0x10, lane);
      STAMP(16 + lay * 24 + 0);
      fill_half<true>(L, 0, v, lane, stA);
      __syncthreads();
      STAMP(16 + lay * 24 + 1);
      if (v < 4) pass_qk(0); else pass_v(0);
      STAMP(16 + lay * 24 + 2);
      if (lay) verify<4>(stB, xb_issue, tmo, 0x11, lane);
      fill_half<true>(L, 1, v, lane, stB);
      __syncthreads();
      STAMP(16 + lay * 24 + 3);
      if (v < 4) { epi_qk(0); pass_qk(1); } else { pass_v(1); epi_v(0); }
      __syncthreads();
      if (v < 4) epi_qk(1); else epi_v(1);
      STAMP(16 + lay * 24 + 4);
      v4u kvn[8];
      auto kv_issue = [&]() {
#pragma unroll
        for (int i = 0; i < 4; ++i) {
          const int kt = 2 * c - 8 + 4 * (v >> 2) + i;
          const int so = RFL(kt >= 0 ? KV_OFF + ((4 * j + (v & 3)) * 64 + kt) * 2048 : OOB);
          kvn[2 * i] = ld_sc1(rs, vo, so);
          kvn[2 * i + 1] = ld_sc1(rs, vo + 1024, so);
        }
      };
      WAITV0();
      __syncthreads();
      STAMP(16 + lay * 24 + 6);
      kv_issue();
      const int w1b = RFL(v < 7 ? W1_OFF + (j * 224 + v * 32) * KB : OOB);
#pragma unroll
      for (int i = 0; i < 4; ++i) w1[i] = ldw(rs, vo, w1b, i);
      SB();
      verify<8>(kvn, kv_issue, tmo, 0x20, lane);
      STAMP(16 + lay * 24 + 7);
      {
        v4u SENT4 = {SENT, SENT, SENT, SENT};
        asm volatile("" : "+v"(SENT4));
        const int tsl = (8 * j + 4 * (v & 1) + (v >> 1)) * 512 + tk * 16 + hh * 8;
#pragma unroll
        for (int hf = 0; hf < 2; ++hf) {
          __builtin_amdgcn_raw_buffer_store_b64(v2u{SENT4.x, SENT4.y}, rs, tsl + X1T_OFF + c * TILE_B + hf * 32768, 0, AUX_SC1);
          if (lay == 0) __builtin_amdgcn_raw_buffer_store_b64(v2u{SENT4.x, SENT4.y}, rs, tsl + XT1_OFF + c * TILE_B + hf * 32768, 0, AUX_SC1);
        }
        const int hb = RFL(v < 7 ? HT_OFF + c * HT_C + (28 * j + 4 * v) * 512 : OOB);
#pragma unroll
        for (int u = 0; u < 4; ++u) st_sc1(SENT4, rs, vo, hb + (u >> 1) * 114688 + 2 * (u & 1) * 512);
        if (lay == 0) st_sc1(SENT4, rs, vo, OT1_OFF + c * TILE_B + (v >> 2) * 32768 + (2 * (4 * j + (v & 3))) * 512);
      }
      SB();
#pragma unroll
      for (int i = 0; i < 4; ++i) {
        *(LAS v4u*)(L + L_KV + ((4 * (v >> 2) + i) * 4 + (v & 3)) * 2048 + vo) = kvn[2 * i];
        *(LAS v4u*)(L + L_KV + ((4 * (v >> 2) + i) * 4 + (v & 3)) * 2048 + 1024 + vo) = kvn[2 * i + 1];
      }
      __syncthreads();
    }
    STAMP(1 + lay * 8);
    {
      FRESH();
      const int hq = v & 3, r = v >> 2, tq = tk - 4 * hh;
      const int kb = L_KV + (r * 4 + hq) * 2048;
      const v4u Q = LDS4(L_Q + (hq * 2 + r) * 1024 + vo);
      v4u Kf[9];
#pragma unroll
      for (int i = 0; i < 9; ++i) Kf[i] = LDS4(kb + i * 8192 + vo);
      const bool ones_lane = (lane & 16) != 0;
      const unsigned vsel = ones_lane ? 0u : 1u, vbase = ones_lane ? (unsigned)L_ONES : (unsigned)(kb + 1024 + (32 * hh + (lane & 15)) * 16);
      auto vop = [&](int i, int s) -> v4u { return LDS4(vbase + vsel * (unsigned)(i * 8192 + s * 256)); };
      const int first_ok = 8 - r - 2 * c;
      const int w1b = RFL(v < 7 ? W1_OFF + (j * 224 + v * 32) * KB : OOB);
      auto tmax = [&](const f32x16& x, float m) {
        const float a = max3f(x[0], x[1], x[2]), b2_ = max3f(x[3], x[4], x[5]), c2 = max3f(x[6], x[7], x[8]), d = max3f(x[9], x[10], x[11]), e = max3f(x[12], x[13], x[14]);
        return max3f(max3f(a, b2_, x[15]), max3f(c2, d, e), m);
      };
      f32x16 Sm, S[2];
      {
        f32x16 Sa = mfma32(Kf[0], Q, Z);
        const f32x16 Sb = mfma32(Kf[8], Q, Z);
        S[1] = mfma32(Kf[1], Q, Z);
        if (first_ok > 0) {
#pragma unroll
          for (int q = 0; q < 16; ++q) Sa[q] = -1e30f;
        }
#pragma unroll
        for (int q = 0; q < 16; ++q) { const int row = (q & 3) + 8 * (q >> 2); Sm[q] = row <= tq ? Sb[q] : Sa[q]; }
      }
      float mx = tmax(Sm, -1e30f);
      SB();
#pragma unroll
      for (int i = 1; i < 8; ++i) {
        if (i + 1 < 8) S[(i + 1) & 1] = mfma32(Kf[i + 1], Q, Z);
        if (i >= first_ok) mx = tmax(S[i & 1], mx);
        if (i < 6) w1[4 + (i - 1)] = ldw(rs, vo, w1b, 4 + (i - 1));
        SB();
      }
      { const f32x2 rr = both_halves(mx); mx = max3f(rr.x, rr.y, rr.y); }
      f32x16 Cm;
#pragma unroll
      for (int q = 0; q < 16; ++q) Cm[q] = -mx;
      S[1] = mfma32(Kf[1], Q, Cm);
      f32x16 O;
      {
        const v4u vn0 = vop(8, 0), vn1 = vop(8, 1), vo0 = vop(0, 0), vo1 = vop(0, 1);
        unsigned pp[8];
#pragma unroll
        for (int q = 0; q < 8; ++q) pp[q] = pk(__builtin_amdgcn_exp2f(Sm[2 * q] - mx), __builtin_amdgcn_exp2f(Sm[2 * q + 1] - mx));
        v4u pn[2], po[2];
#pragma unroll
        for (int q = 0; q < 8; ++q) {
          const int row0 = ((2 * q) & 3) + 8 * ((2 * q) >> 2);
          const unsigned mk = (row0 <= tq ? 0xFFFFu : 0u) | (row0 + 1 <= tq ? 0xFFFF0000u : 0u);
          pn[q >> 2][q & 3] = pp[q] & mk; po[q >> 2][q & 3] = pp[q] & ~mk;
        }
        O = mfma32(vn0, pn[0], Z); O = mfma32(vn1, pn[1], O);
        O = mfma32(vo0, po[0], O); O = mfma32(vo1, po[1], O);
        SB();
      }
#pragma unroll
      for (int i = 1; i < 8; ++i) {
        if (i + 1 < 8) S[(i + 1) & 1] = mfma32(Kf[i + 1], Q, Cm);
        const v4u va0 = vop(i, 0), va1 = vop(i, 1);
        if (i >= first_ok) {
          const f32x16& cur = S[i & 1];
          v4u pp[2];
#pragma unroll
          for (int q = 0; q < 8; ++q) pp[q >> 2][q & 3] = pk(__builtin_amdgcn_exp2f(cur[2 * q]), __builtin_amdgcn_exp2f(cur[2 * q + 1]));
          O = mfma32(va0, pp[0], O); O = mfma32(va1, pp[1], O);
        }
        w1[9 + (i - 1)] = ldw(rs, vo, w1b, 9 + (i - 1));
        SB();
      }
      KEEP(O);
      const float rl = __builtin_amdgcn_rcpf(O[8]);
      asm volatile("s_waitcnt vmcnt(12)" ::: "memory");
      st_sc1(nosent2(swap_T8(pk8(O, 0, rl))), rs, vo, OT_OFF + c * TILE_B + r * 32768 + (2 * (4 * j + hq)) * 512);
      if (v < 4) WAITV0();
    }
    STAMP(2 + lay * 8);
    {
      FRESH();
      const int otb = RFL(OT_OFF + c * TILE_B + v * 4096);
      const int w1b = RFL(v < 7 ? W1_OFF + (j * 224 + v * 32) * KB : OOB);
      auto oa_issue = [&]() {
#pragma unroll
        for (int i = 0; i < 4; ++i) stA[i] = ld_seq(rs, vo, otb, i);
      };
      auto ob_issue = [&]() {
#pragma unroll
        for (int i = 0; i < 4; ++i) stB[i] = ld_seq(rs, vo, otb + 32768, i);
      };
      f32x16 a;
      auto pass = [&](int hf) {
        const int x0 = L_TILE + hf * 32768 + (v >> 1) * 8192 + vo;
        v4u xs[8];
#pragma unroll
        for (int s = 0; s < 8; ++s) xs[s] = LDS4(x0 + s * 1024);
        a = Z;
#pragma unroll
        for (int s = 0; s < 8; ++s) a = mfma32(wo[s], xs[s], a);
        KEEP(a);
#pragma unroll
        for (int u = 1; u < 4; ++u) *(LAS f32x4*)(L + L_XCH + ((hf * 8 + (v ^ (2 * u))) * 3 + (u - 1)) * 1024 + vo) = q4(a, u);
      };
      auto epi = [&](int hf) {
        f32x4 o = q4(a, 0);
#pragma unroll
        for (int k = 0; k < 3; ++k) o += LDSF4(L_XCH + ((hf * 8 + v) * 3 + k) * 1024 + vo);
        xr[hf] = pk4(unpk4(xr[hf]) + o);
        __builtin_amdgcn_raw_buffer_store_b64(v2u{nosent(xr[hf].x), xr[hf].y}, rs, (8 * j + 4 * (v & 1) + (v >> 1)) * 512 + tk * 16 + hh * 8 + X1T_OFF + c * TILE_B + hf * 32768, 0, AUX_SC1);
      };
      oa_issue();
#pragma unroll
      for (int i = 16; i < 24; ++i) w1[i] = ldw(rs, vo, w1b, i);
      SB();
      verify<4>(stA, oa_issue, tmo, 0x30, lane);
      STAMP(16 + lay * 24 + 8);
      __syncthreads();
      fill_half<false>(L, 0, v, lane, stA);
      ob_issue();
      __syncthreads();
      STAMP(16 + lay * 24 + 9);
      pass(0);
      STAMP(16 + lay * 24 + 21);
      verify<4>(stB, ob_issue, tmo, 0x31, lane);
      fill_half<false>(L, 1, v, lane, stB);
      __syncthreads();
      epi(0);
      STAMP(16 + lay * 24 + 23);
      pass(1);
#pragma unroll
      for (int i = 24; i < 32; ++i) w1[i] = ldw(rs, vo, w1b, i);
      __syncthreads();
#pragma unroll
      for (int i = 0; i < 4; ++i) stA[i] = ld_seq(rs, vo, RFL(X1T_OFF + c * TILE_B + v * 4096), i);
      SB();
      epi(1);
    }
    STAMP(3 + lay * 8);
    v4u hA[14];
    {
      FRESH();
      const int x1b = RFL(X1T_OFF + c * TILE_B + v * 4096);
      const int w2b = RFL(W2_OFF + (j * 224 + v * 28) * KB);
      const int hb = RFL(v < 7 ? HT_OFF + c * HT_C + (28 * j + 4 * v) * 512 : OOB);
      auto xa_issue = [&]() {
#pragma unroll
        for (int i = 0; i < 4; ++i) stA[i] = ld_seq(rs, vo, x1b, i);
      };
      auto xb_issue = [&]() {
#pragma unroll
        for (int i = 0; i < 4; ++i) stB[i] = ld_seq(rs, vo, x1b + 32768, i);
      };
      f32x16 a;
      auto pass = [&](int hf) {
        v4u xs[3];
        const int x0 = L_TILE + hf * 32768 + vo;
#pragma unroll
        for (int s = 0; s < 2; ++s) xs[s] = LDS4(x0 + s * 1024);
        a = Z;
        PRIO(1);
#pragma unroll
        for (int s = 0; s < 32; ++s) {
          if (s + 2 < 32) xs[(s + 2) % 3] = LDS4(x0 + (s + 2) * 1024);
          SB();
          a = mfma32(w1[s], xs[s % 3], a);
          if (hf == 0 && s == 8) xb_issue();
          if (hf == 0 && s >= 22) w2[s - 22] = ldw(rs, vo, w2b, s - 22);
          if (hf == 1 && s >= 2 && s < 20) w2[10 + (s - 2)] = ldw(rs, vo, w2b, 10 + (s - 2));
          SB();
        }
        KEEP(a);
        PRIO(0);
      };
      v4u ho[2];
      auto gelu = [&](int hf) {
        const f32x2 ab = final_stats<true>(L, lane, hf);
        const float c1 = -2.0f * 0.7978845608028654f * 1.4426950408889634f, c2 = c1 * 0.044715f;
#pragma unroll
        for (int u2 = 0; u2 < 2; ++u2) {
          f32x16 hv;
#pragma unroll
          for (int u = 2 * u2; u < 2 * u2 + 2; ++u) {
            const f32x4 csn = LDSF4(L_C1 + (32 * v + 8 * u + 4 * hh) * 4), bn = LDSF4(L_C1 + 1024 + (32 * v + 8 * u + 4 * hh) * 4);
#pragma unroll
            for (int i = 0; i < 4; ++i) {
              const float z = a[4 * u + i] * ab.x + (csn[i] * ab.y + bn[i]);
              const float e = __builtin_amdgcn_exp2f(z * (z * z * c2 + c1));
              hv[4 * u + i] = z * __builtin_amdgcn_rcpf(1.0f + e);
            }
          }
          ho[u2] = nosent2(swap_T8(pk8(hv, 8 * u2)));
          if (hf == 0) st_sc1(ho[u2], rs, vo, hb + 2 * u2 * 512);
        }
      };
      verify<4>(stA, xa_issue, tmo, 0x40, lane);
      STAMP(16 + lay * 24 + 10);
      fill_half<true>(L, 0, v, lane, stA);
      __syncthreads();
      STAMP(16 + lay * 24 + 11);
      pass(0);
      STAMP(16 + lay * 24 + 12);
      gelu(0);
      STAMP(16 + lay * 24 + 13);
      verify<4>(stB, xb_issue, tmo, 0x41, lane);
      fill_half<true>(L, 1, v, lane, stB);
      __syncthreads();
      STAMP(16 + lay * 24 + 14);
      pass(1);
      STAMP(16 + lay * 24 + 15);
      gelu(1);
#pragma unroll
      for (int s = 0; s < 14; ++s) hA[s] = ld_seq(rs, vo, RFL(HT_OFF + c * HT_C + 28 * v * 512), s);
      SB();
#pragma unroll
      for (int u2 = 0; u2 < 2; ++u2) st_sc1(ho[u2], rs, vo, hb + 114688 + 2 * u2 * 512);
    }
    STAMP(4 + lay * 8);
    {
      FRESH();
      const int htb = RFL(HT_OFF + c * HT_C + 28 * v * 512);
      const int wqb = RFL(lay == 0 ? WQ_OFF + (j * 192 + (v < 4 ? v * 32 : 128 + (v - 4) * 16)) * KB : OOB), wqb2 = RFL(v < 4 ? wqb : OOB);
      const int xnb = RFL(lay == 0 ? XT1_OFF + c * TILE_B + v * 4096 : OOB);
#pragma unroll
      for (int hf = 0; hf < 2; ++hf) {
        verify<14>(hA, [&]() {
#pragma unroll
          for (int s = 0; s < 14; ++s) hA[s] = ld_seq(rs, vo, htb + hf * 114688, s);
        }, tmo, 0x50 + hf, lane);
        if (hf == 0) __syncthreads();
        STAMP(16 + lay * 24 + 17 + 2 * hf);
        f32x16 ac[2] = {Z, Z};
        PRIO(1);
#pragma unroll
        for (int s = 0; s < 14; ++s) {
          ac[0] = mfma32(w2[2 * s], hA[s], ac[0]);
          ac[1] = mfma32(w2[2 * s + 1], hA[s], ac[1]);
          if (hf == 1) wq[s] = ldw(rs, vo, wqb, s);
          else hA[s] = ld_seq(rs, vo, htb + 114688, s);
          SB();
        }
        KEEP(ac[0]); KEEP(ac[1]);
        PRIO(0);
        STAMP(16 + lay * 24 + 18 + 2 * hf);
        LAS unsigned char* R = L + (hf ? L_TILE : L_RB);
#pragma unroll
        for (int nt = 0; nt < 2; ++nt)
#pragma unroll
          for (int u = 0; u < 4; ++u) *(LAS f32x4*)(R + ((nt + 2 * u) * 8 + v) * 1024 + vo) = q4(ac[nt], u);
        if (hf == 1) { wq[14] = ldw(rs, vo, wqb, 14); wq[15] = ldw(rs, vo, wqb, 15); }
        __syncthreads();
        f32x4 s0 = {0, 0, 0, 0};
#pragma unroll
        for (int k = 0; k < 8; ++k) s0 += *(LAS f32x4*)(R + (v * 8 + k) * 1024 + vo);
        const f32x4 b0 = LDSF4(L_CQ + (192 + 32 * (v & 1) + 8 * (v >> 1) + 4 * hh) * 4);
        xr[hf] = pk4(unpk4(xr[hf]) + s0 + b0);
        if (hf == 1) {
#pragma unroll
          for (int i = 0; i < 4; ++i) stA[i] = ld_seq(rs, vo, xnb, i);
          SB();
        }
        if (lay == 0) {
          __builtin_amdgcn_raw_buffer_store_b64(v2u{nosent(xr[hf].x), xr[hf].y}, rs, (8 * j + 4 * (v & 1) + (v >> 1)) * 512 + tk * 16 + hh * 8 + XT1_OFF + c * TILE_B + hf * 32768, 0, AUX_SC1);
        } else {
          *(v2u*)(p.out + (size_t)(64 * c + 32 * hf + tk) * DM + 64 * j + 32 * (v & 1) + 8 * (v >> 1) + 4 * hh) = xr[hf];
        }
      }
      __syncthreads();
#pragma unroll
      for (int i = 16; i < 32; ++i) wq[i] = ldw(rs, vo, wqb2, i);
      SB();
    }
    STAMP(5 + lay * 8);
  }
  STAMP_FLUSH();
}
}

extern "C" void kernel_launch(void* const* d_in, const int* in_sizes, int n_in, void* d_out, int out_size, void* d_ws, size_t ws_size,
                              hipStream_t stream) {
  using namespace fk;
  Params p;
  p.x = (const u16*)d_in[0]; p.wqkv = (const u16*)d_in[1]; p.wo = (const u16*)d_in[2];
  p.w1 = (const u16*)d_in[4]; p.b1 = (const u16*)d_in[5]; p.w2 = (const u16*)d_in[6]; p.b2 = (const u16*)d_in[7];
  p.out = (u16*)d_out; p.ws = (unsigned char*)d_ws;
  static bool attr_set = false;
  if (!attr_set) { (void)hipFuncSetAttribute((const void*)k_main, hipFuncAttributeMaxDynamicSharedMemorySize, L_BYTES); attr_set = true; }
  k_prep<<<PREP_BLOCKS, 256, 0, stream>>>(p);
  k_main<<<256, 512, L_BYTES, stream>>>(p);
}
```

```cpp
#include <hip/hip_runtime.h>
#include <stdint.h>

namespace fk {
typedef unsigned short u16;
typedef unsigned v4u __attribute__((ext_vector_type(4)));
typedef unsigned v2u __attribute__((ext_vector_type(2)));
typedef float f32x4 __attribute__((ext_vector_type(4)));
typedef float f32x2 __attribute__((ext_vector_type(2)));
typedef float f32x16 __attribute__((ext_vector_type(16)));
typedef __bf16 bf8v __attribute__((ext_vector_type(8)));
typedef __bf16 bf2v __attribute__((ext_vector_type(2)));
#define LAS __attribute__((address_space(3)))
#define GAS __attribute__((address_space(1)))
typedef __amdgpu_buffer_rsrc_t rsrc_t;

constexpr int SEQ = 2048, DM = 512, FF = 1792;
constexpr int KB = 1024;
constexpr int WQ_OFF = 0;
constexpr int WO_OFF = WQ_OFF + 8 * 192 * KB;
constexpr int W1_OFF = WO_OFF + 8 * 64 * KB;
constexpr int W2_OFF = W1_OFF + 8 * 224 * KB;
constexpr int CS_OFF = W2_OFF + 8 * 224 * KB;
constexpr int CTRL_OFF = CS_OFF + 16 * KB;
constexpr int TILE_B = 64 * KB;
constexpr int KV_B = 32 * 64 * 2 * KB;
constexpr int HT_C = 2 * 224 * 512;
constexpr int KV0_OFF = CTRL_OFF + 16 * KB;
constexpr int OT0_OFF = KV0_OFF + KV_B;
constexpr int KV1_OFF = OT0_OFF + 32 * TILE_B;
constexpr int OT1_OFF = KV1_OFF + KV_B;
constexpr int X1T0_OFF = OT1_OFF + 32 * TILE_B;
constexpr int X1T1_OFF = X1T0_OFF + 32 * TILE_B;
constexpr int XT1_OFF = X1T1_OFF + 32 * TILE_B;
constexpr int HT0_OFF = XT1_OFF + 32 * TILE_B;
constexpr int HT1_OFF = HT0_OFF + 32 * HT_C;
constexpr int STAMP_OFF = HT1_OFF + 32 * HT_C;
constexpr int WS_END = STAMP_OFF + 256 * 8 * 128 * 8;
constexpr unsigned SENT = 0xFFFFFFFFu;
constexpr int AUX_SC1 = 16;
constexpr unsigned SPIN_CAP = 1u << 14;

constexpr int L_TILE = 0;
constexpr int L_KV = 0;
constexpr int L_RB = 64 * KB;
constexpr int L_Q = 80 * KB;
constexpr int L_XCH = 88 * KB;
constexpr int L_PST = 136 * KB;
constexpr int L_FST = 144 * KB;
constexpr int L_C1 = 145 * KB;
constexpr int L_CQ = 147 * KB;
constexpr int L_ONES = 148 * KB;
constexpr int L_FLAG = 148 * KB + 64;
constexpr int L_BYTES = 148 * KB + 128;

__device__ __forceinline__ float bf2f(unsigned b) { return __builtin_bit_cast(float, b << 16); }
__device__ __forceinline__ float bflo(unsigned d) { return __builtin_bit_cast(float, d << 16); }
__device__ __forceinline__ float bfhi(unsigned d) { return __builtin_bit_cast(float, d & 0xFFFF0000u); }
__device__ __forceinline__ unsigned pk(float lo, float hi) {
  bf2v r = {(__bf16)lo, (__bf16)hi};
  return __builtin_bit_cast(unsigned, r);
}
__device__ __forceinline__ f32x4 mfma16(v4u a, v4u b, f32x4 c) {
  return __builtin_amdgcn_mfma_f32_16x16x32_bf16(__builtin_bit_cast(bf8v, a), __builtin_bit_cast(bf8v, b), c, 0, 0, 0);
}
__device__ __forceinline__ unsigned nosent(unsigned d) { return d < 0xFFFFFFFEu ? d : 0xFFFFFFFEu; }
__device__ __forceinline__ v4u nosent4(v4u v) { return v4u{nosent(v.x), nosent(v.y), nosent(v.z), nosent(v.w)}; }
__device__ __forceinline__ v4u pack_T(f32x4 a, f32x4 b) {
  unsigned x0 = pk(a[0], a[1]), x1 = pk(a[2], a[3]), y0 = pk(b[0], b[1]), y1 = pk(b[2], b[3]);
  auto r0 = __builtin_amdgcn_permlane16_swap(x0, y0, false, false);
  auto r1 = __builtin_amdgcn_permlane16_swap(x1, y1, false, false);
  return v4u{r0[0], r1[0], r0[1], r1[1]};
}
__device__ __forceinline__ v2u pk4(f32x4 a) { return v2u{pk(a[0], a[1]), pk(a[2], a[3])}; }
__device__ __forceinline__ f32x4 unpk4(v2u v) { return f32x4{bflo(v.x), bfhi(v.x), bflo(v.y), bfhi(v.y)}; }
__device__ __forceinline__ v4u swap_T(v2u x, v2u y) {
  auto r0 = __builtin_amdgcn_permlane16_swap(x.x, y.x, false, false);
  auto r1 = __builtin_amdgcn_permlane16_swap(x.y, y.y, false, false);
  return v4u{r0[0], r1[0], r0[1], r1[1]};
}
#define KEEP(x) asm volatile("" : "+v"(x))
#define WAITV0() asm volatile("s_waitcnt vmcnt(0)" ::: "memory")
#define PRIO(x) __builtin_amdgcn_s_setprio(x)
#define SB() __builtin_amdgcn_sched_barrier(0)

__device__ __forceinline__ v4u ldw(rsrc_t rs, int voff, int sbase, int i) {
  return __builtin_amdgcn_raw_buffer_load_b128(rs, voff + (i & 3) * 1024, sbase + (i >> 2) * 4096, 0);
}
__device__ __forceinline__ v4u ld_sc1(rsrc_t rs, int voff, int soff) {
  return __builtin_amdgcn_raw_buffer_load_b128(rs, voff, soff, AUX_SC1); }
__device__ __forceinline__ v4u ld_seq(rsrc_t rs, int voff, int sbase, int i) { return ld_sc1(rs, voff + (i & 3) * 1024, sbase + (i >> 2) * 4096); }
#define RFL(x) __builtin_amdgcn_readfirstlane(x)
__device__ __forceinline__ void st_sc1(v4u v, rsrc_t rs, int voff, int soff) {
  __builtin_amdgcn_raw_buffer_store_b128(v, rs, voff + soff, 0, AUX_SC1); }
__device__ __forceinline__ unsigned max4(unsigned m, v4u d) {
  m = __builtin_elementwise_max(__builtin_elementwise_max(m, d.x), d.y);
  return __builtin_elementwise_max(__builtin_elementwise_max(m, d.z), d.w);
}
__device__ __forceinline__ bool spin_fail(unsigned& spins, GAS unsigned* tmo, unsigned code, int lane) {
  ++spins;
  if (spins > SPIN_CAP) {
    if (lane == 0) __hip_atomic_store(tmo, code, __ATOMIC_RELAXED, __HIP_MEMORY_SCOPE_AGENT);
    return true;
  }
  if ((spins & 63u) == 0u && __hip_atomic_load(tmo, __ATOMIC_RELAXED, __HIP_MEMORY_SCOPE_AGENT) != 0u) return true;
  return false;
}

struct Params {
  const u16* x;
  const u16* wqkv;
  const u16* wo;
  const u16* w1;
  const u16* b1;
  const u16* w2;
  const u16* b2;
  u16* out;
  unsigned char* ws;
};

constexpr int NFRAG = 8 * 704;
constexpr int PB_TILE = NFRAG * 16 / 256;
constexpr int PB_CS = (1536 + 1792) / 64;
constexpr int CLEAN_B = KV_B + 32 * TILE_B;
constexpr int PB_CLEAN = CLEAN_B / (256 * 64);
constexpr int PREP_BLOCKS = PB_TILE + PB_CS + PB_CLEAN;

__global__ void __launch_bounds__(256) k_prep(Params p) {
  const int b = blockIdx.x, tid = threadIdx.x;
  if (b < PB_TILE) {
    const int F = b * 16 + (tid >> 4), t16 = tid & 15, cq = t16 & 7, h = t16 >> 3;
    const u16* W; int ldw_, k0, col;
    if (F < 8 * 192) {
      const int j = F / 192, i = F % 192;
      W = p.wqkv; ldw_ = 1536;
      if (i < 128) { const int head = 4 * j + (i >> 5); k0 = 16 * (i & 31); col = cq < 4 ? head * 16 + 4 * cq : 512 + head * 16 + 4 * (cq - 4); }
      else { const int u = i - 128, wv = u >> 4; k0 = 16 * (16 * (wv & 1) + (u & 15)); col = 1024 + (4 * j + 2 * (wv >> 1)) * 16 + 4 * cq; }
    } else if (F < 8 * 256) {
      const int f = F - 8 * 192, j = f >> 6, u = f & 63, v = u >> 3;
      W = p.wo; ldw_ = 512; k0 = 16 * (8 * (v >> 1) + (u & 7)); col = 64 * j + 32 * (v & 1) + 4 * (cq ^ (2 * (v >> 1)));
    } else if (F < 8 * 480) {
      const int f = F - 8 * 256, j = f / 224, u = f % 224;
      W = p.w1; ldw_ = 1792; k0 = 16 * (u & 31); col = 224 * j + 32 * (u >> 5) + 4 * cq;
    } else {
      const int f = F - 8 * 480, j = f / 224, u = f % 224, v = u / 28, q = u % 28;
      W = p.w2; ldw_ = 512; k0 = 224 * v + 16 * (q >> 1); col = 64 * j + 32 * (q & 1) + 4 * cq;
    }
    const u16* src = W + (size_t)(k0 + 8 * h) * ldw_ + col;
    v2u r[8];
#pragma unroll
    for (int e = 0; e < 8; ++e) r[e] = *(const v2u*)(src + (size_t)e * ldw_);
    v4u o[4];
#pragma unroll
    for (int e = 0; e < 4; ++e) {
      const unsigned a0 = r[2 * e].x, a1 = r[2 * e + 1].x, c0 = r[2 * e].y, c1 = r[2 * e + 1].y;
      o[0][e] = (a0 & 0xFFFFu) | (a1 << 16);
      o[1][e] = (a0 >> 16) | (a1 & 0xFFFF0000u);
      o[2][e] = (c0 & 0xFFFFu) | (c1 << 16);
      o[3][e] = (c0 >> 16) | (c1 & 0xFFFF0000u);
    }
    v4u* dst = (v4u*)(p.ws + (size_t)F * 1024 + (h * 32 + 4 * cq) * 16);
#pragma unroll
    for (int i = 0; i < 4; ++i) dst[i] = o[i];
  } else if (b < PB_TILE + PB_CS) {
    __shared__ float red[16][64];
    const int cb = b - PB_TILE;
    const u16* W; int ldw_, col;
    if (cb < 24) { W = p.wqkv; ldw_ = 1536; col = cb * 64; } else { W = p.w1; ldw_ = 1792; col = (cb - 24) * 64; }
    const int cq = tid & 15, rg = tid >> 4;
    float s0 = 0, s1 = 0, s2 = 0, s3 = 0;
    const u16* src = W + (size_t)(rg * 32) * ldw_ + col + 4 * cq;
#pragma unroll
    for (int e = 0; e < 32; ++e) {
      const v2u v = *(const v2u*)(src + (size_t)e * ldw_);
      s0 += bflo(v.x); s1 += bfhi(v.x); s2 += bflo(v.y); s3 += bfhi(v.y);
    }
    red[rg][4 * cq + 0] = s0; red[rg][4 * cq + 1] = s1; red[rg][4 * cq + 2] = s2; red[rg][4 * cq + 3] = s3;
    __syncthreads();
    if (tid < 64) {
      float s = 0;
#pragma unroll
      for (int r = 0; r < 16; ++r) s += red[r][tid];
      ((float*)(p.ws + CS_OFF))[cb * 64 + tid] = s;
    }
    if (cb == 0 && tid == 0) *(unsigned*)(p.ws + CTRL_OFF) = 0u;
  } else {
    const int cb = b - PB_TILE - PB_CS;
    v4u* dst = (v4u*)(p.ws + KV0_OFF + (size_t)cb * (256 * 64)) + tid;
    const v4u s = {SENT, SENT, SENT, SENT};
#pragma unroll
    for (int i = 0; i < 4; ++i) dst[i * 256] = s;
  }
}

#define STAMP(i) do {} while (0)
#define STAMP_FLUSH() do {} while (0)

__device__ __forceinline__ float max3f(float a, float b, float c) { float r; asm("v_max3_f32 %0, %1, %2, %3" : "=v"(r) : "v"(a), "v"(b), "v"(c)); return r; }
__device__ __forceinline__ unsigned max3u(unsigned a, unsigned b, unsigned c) { unsigned r; asm("v_max3_u32 %0, %1, %2, %3" : "=v"(r) : "v"(a), "v"(b), "v"(c)); return r; }
__device__ __forceinline__ f32x16 mfma32(v4u a, v4u b, f32x16 c) {
  return __builtin_amdgcn_mfma_f32_32x32x16_bf16(__builtin_bit_cast(bf8v, a), __builtin_bit_cast(bf8v, b), c, 0, 0, 0);
}
template <int N> __device__ __forceinline__ bool any_sent(const v4u (&d)[N]) {
  unsigned m0 = 0, m1 = 0;
#pragma unroll
  for (int i = 0; i < N; ++i) { if (i & 1) m1 = max3u(m1, d[i].x, d[i].z); else m0 = max3u(m0, d[i].x, d[i].z); }
  return __any(max3u(m0, m1, 0) == SENT);
}
template <int N> __device__ __forceinline__ void issue(v4u (&d)[N], rsrc_t rs, int voff, int soff, int stride) {
#pragma unroll
  for (int i = 0; i < N; ++i) d[i] = ld_sc1(rs, voff, soff + i * stride);
}
template <int N, class F> __device__ __forceinline__ void verify(v4u (&d)[N], F load_i, GAS unsigned* tmo, unsigned code, int lane) {
  if (__builtin_expect(any_sent<N>(d), 0)) {
    unsigned spins = 0;
    do {
      __builtin_amdgcn_s_sleep(1);
      asm volatile("" ::: "memory");
#pragma unroll
      for (int i = 0; i < N; ++i)
        if (__any(max3u(d[i].x, d[i].z, 0u) == SENT)) d[i] = load_i(i);
    } while (any_sent<N>(d) && !spin_fail(spins, tmo, code, lane));
  }
}
__device__ __forceinline__ f32x2 both_halves(float v) {
  const unsigned a = __builtin_bit_cast(unsigned, v);
  auto r = __builtin_amdgcn_permlane32_swap(a, a, false, false);
  const unsigned r0 = r[0], r1 = r[1];
  return f32x2{__builtin_bit_cast(float, r0), __builtin_bit_cast(float, r1)};
}
__device__ __forceinline__ float dot2(unsigned a, unsigned b, float c) {
  return __builtin_amdgcn_fdot2_f32_bf16(__builtin_bit_cast(bf2v, a), __builtin_bit_cast(bf2v, b), c, false);
}
__device__ __forceinline__ v4u nosent2(v4u v) { return v4u{nosent(v.x), v.y, nosent(v.z), v.w}; }
__device__ __forceinline__ f32x4 round4(f32x4 s) {
  const unsigned u0 = pk(s[0], s[1]), u1 = pk(s[2], s[3]);
  return f32x4{bflo(u0), bfhi(u0), bflo(u1), bfhi(u1)};
}

__device__ __forceinline__ v4u pk8(const f32x16& a, int r0, float sc = 1.0f) {
  return v4u{pk(a[r0] * sc, a[r0 + 1] * sc), pk(a[r0 + 2] * sc, a[r0 + 3] * sc), pk(a[r0 + 4] * sc, a[r0 + 5] * sc), pk(a[r0 + 6] * sc, a[r0 + 7] * sc)};
}
__device__ __forceinline__ v4u swap_T8(v4u p) {
  auto r0 = __builtin_amdgcn_permlane32_swap(p[0], p[2], false, false);
  auto r1 = __builtin_amdgcn_permlane32_swap(p[1], p[3], false, false);
  return v4u{r0[0], r1[0], r0[1], r1[1]};
}
__device__ __forceinline__ f32x4 q4(const f32x16& a, int u) { return f32x4{a[4 * u], a[4 * u + 1], a[4 * u + 2], a[4 * u + 3]}; }
#define FRESH() int lane = lane_, c = c_, j = j_, v = v_; asm volatile("" : "+v"(lane), "+s"(c), "+s"(j), "+s"(v)); \
  const int vo = lane * 16, tk = lane & 31, hh = lane >> 5; (void)vo; (void)tk; (void)hh
#define LDS4(off) (*(const LAS v4u*)(L + (off)))
#define LDSF4(off) (*(const LAS f32x4*)(L + (off)))
constexpr int OOB = 0x7FF00000;

template <bool STATS>
__device__ __forceinline__ void fill_half(LAS unsigned char* L, int hf, int v, int lane, const v4u (&d)[4]) {
#pragma unroll
  for (int i = 0; i < 4; ++i) *(LAS v4u*)(L + L_TILE + hf * 32768 + v * 4096 + i * 1024 + lane * 16) = d[i];
  if (STATS) {
    float s0 = 0.f, s1 = 0.f, q0 = 0.f, q1 = 0.f;
#pragma unroll
    for (int i = 0; i < 4; ++i) {
      const v4u x = d[i];
      s0 = dot2(x[0], 0x3F803F80u, s0); q0 = dot2(x[0], x[0], q0);
      s1 = dot2(x[1], 0x3F803F80u, s1); q1 = dot2(x[1], x[1], q1);
      s0 = dot2(x[2], 0x3F803F80u, s0); q0 = dot2(x[2], x[2], q0);
      s1 = dot2(x[3], 0x3F803F80u, s1); q1 = dot2(x[3], x[3], q1);
    }
    *(LAS f32x2*)(L + L_PST + (((hf * 8 + v) * 2 + (lane >> 5)) * 32 + (lane & 31)) * 8) = f32x2{s0 + s1, q0 + q1};
  }
}
template <bool DOUBLE_LN>
__device__ __forceinline__ f32x2 final_stats(LAS unsigned char* L, int lane, int hf) {
  const int t = lane & 31, hh = lane >> 5;
  f32x2 pv[8];
#pragma unroll
  for (int k = 0; k < 8; ++k) pv[k] = *(LAS f32x2*)(L + L_PST + ((hf * 16 + hh * 8 + k) * 32 + t) * 8);
  SB();
  const f32x2 s = ((pv[0] + pv[1]) + (pv[2] + pv[3])) + ((pv[4] + pv[5]) + (pv[6] + pv[7]));
  const f32x2 rs_ = both_halves(s.x), rq = both_halves(s.y);
  const float mean = (rs_.x + rs_.y) * (1.0f / DM);
  float var = (rq.x + rq.y) * (1.0f / DM) - mean * mean;
  var = var > 0.f ? var : 0.f;
  float r = __builtin_amdgcn_rsqf(var + 1e-5f);
  if (DOUBLE_LN) {
    const float v2 = var * r * r;
    r *= __builtin_amdgcn_rsqf(v2 + 1e-5f);
  }
  const f32x2 ab = {r, -mean * r};
  *(LAS f32x2*)(L + L_FST + (hf * 32 + t) * 8) = ab;
  return ab;
}

__global__ void __launch_bounds__(512, 2) k_main(Params p) {
  extern __shared__ __attribute__((aligned(16))) unsigned char smem[];
  LAS unsigned char* L = (LAS unsigned char*)smem;
  const int b = blockIdx.x, tid = threadIdx.x, lane_ = tid & 63, v_ = __builtin_amdgcn_readfirstlane(tid >> 6);
#define MAPPING 1
  int c_, j_;
  if (MAPPING == 0) { c_ = (b & 7) * 4 + (b >> 6); j_ = (b >> 3) & 7; }
  else { c_ = ((b & 7) >> 1) * 8 + (b >> 5); j_ = (b & 1) * 4 + ((b >> 3) & 3); }
  const rsrc_t rs = __builtin_amdgcn_make_buffer_rsrc((void*)p.ws, 0, WS_END, 0x00020000);
  const rsrc_t rx = __builtin_amdgcn_make_buffer_rsrc((void*)p.x, 0, SEQ * DM * 2, 0x00020000);
  GAS unsigned* tmo = (GAS unsigned*)(p.ws + CTRL_OFF);
  STAMP(0);
  v2u xr[2]; v4u wq[32], stA[4];
  f32x16 Z;
#pragma unroll
  for (int q = 0; q < 16; ++q) Z[q] = 0.f;
  {
    FRESH();
    const float* cs = (const float*)(p.ws + CS_OFF);
    float k0 = 0.f, k1 = 0.f;
    if (tid < 224) { k0 = cs[1536 + 224 * j + tid]; k1 = bf2f(p.b1[224 * j + tid]); }
    else if (tid >= 256) { const int t = tid - 256; k0 = t < 192 ? cs[(t >> 6) * 512 + 64 * j + (t & 63)] : bf2f(p.b2[64 * j + t - 192]); }
#pragma unroll
    for (int hf = 0; hf < 2; ++hf) xr[hf] = *(const v2u*)(p.x + (size_t)(64 * c + 32 * hf + tk) * DM + 64 * j + 32 * (v & 1) + 8 * (v >> 1) + 4 * hh);
#pragma unroll
    for (int i = 0; i < 4; ++i) stA[i] = __builtin_amdgcn_raw_buffer_load_b128(rx, tk * 1024 + hh * 16 + i * 32, (64 * c) * 1024 + v * 128, 0);
    SB();
    __builtin_amdgcn_s_barrier();
    SB();
    const int wqb = RFL(WQ_OFF + (j * 192 + (v < 4 ? v * 32 : 128 + (v - 4) * 16)) * KB), wqb2 = RFL(v < 4 ? wqb : OOB);
#pragma unroll
    for (int i = 0; i < 16; ++i) wq[i] = ldw(rs, vo, wqb, i);
    SB();
    if (tid < 224) { *(LAS float*)(L + L_C1 + tid * 4) = k0; *(LAS float*)(L + L_C1 + 1024 + tid * 4) = k1; }
    else if (tid >= 256) *(LAS float*)(L + L_CQ + (tid - 256) * 4) = k0;
    else if (tid < 228) *(LAS unsigned*)(L + L_ONES + (tid - 224) * 4) = 0x3F803F80u;
    v4u SENT4 = {SENT, SENT, SENT, SENT};
    asm volatile("" : "+v"(SENT4));
#pragma unroll
    for (int hf = 0; hf < 2; ++hf) {
      if (v < 4) st_sc1(SENT4, rs, vo, KV1_OFF + ((4 * j + v) * 64 + 2 * c + hf) * 2048);
      else st_sc1(SENT4, rs, ((4 * j + 2 * ((v - 4) >> 1) + ((lane >> 4) & 1)) * 64) * 2048 + 1024 + (32 * hh + 16 * (v & 1) + (lane & 15)) * 16, KV1_OFF + (2 * c + hf) * 2048);
    }
    SB();
  }

  for (int lay = 0; lay < 2; ++lay) {
    const int KV_OFF = lay ? KV1_OFF : KV0_OFF, OT_OFF = lay ? OT1_OFF : OT0_OFF, X1T_OFF = lay ? X1T1_OFF : X1T0_OFF, HT_OFF = lay ? HT1_OFF : HT0_OFF;
    v4u wo[8], w1[32], w2[28], stB[4];
    {
      FRESH();
      const int xtb = RFL(XT1_OFF + c * TILE_B + v * 4096);
      const int wob = RFL(WO_OFF + (j * 64 + v * 8) * KB);
      f32x16 acc[2];
      auto xb_issue = [&]() {
        if (lay) {
#pragma unroll
          for (int i = 0; i < 4; ++i) stB[i] = ld_seq(rs, vo, xtb + 32768, i);
        } else {
#pragma unroll
          for (int i = 0; i < 4; ++i) stB[i] = __builtin_amdgcn_raw_buffer_load_b128(rx, tk * 1024 + hh * 16 + i * 32, (64 * c + 32) * 1024 + v * 128, 0);
        }
      };
      auto pass_qk = [&](int hf) {
        v4u xs[3];
        const int x0 = L_TILE + hf * 32768 + vo;
#pragma unroll
        for (int s = 0; s < 2; ++s) xs[s] = LDS4(x0 + s * 1024);
        acc[hf] = Z;
        PRIO(1);
#pragma unroll
        for (int s = 0; s < 32; ++s) {
          if (s + 2 < 32) xs[(s + 2) % 3] = LDS4(x0 + (s + 2) * 1024);
          SB();
          acc[hf] = mfma32(wq[s], xs[s % 3], acc[hf]);
          if (hf == 0 && s == 12) xb_issue();
          if (hf == 1 && s >= 24) wo[s - 24] = ldw(rs, vo, wob, s - 24);
          SB();
        }
        KEEP(acc[hf]);
        PRIO(0);
      };
      auto pass_v = [&](int hf) {
        const int kh = v & 1;
        v4u xs[3];
        const int x0 = L_TILE + hf * 32768 + kh * 16384 + (vo ^ (kh * 256));
#pragma unroll
        for (int s = 0; s < 2; ++s) xs[s] = LDS4(x0 + s * 1024);
        acc[hf] = Z;
        PRIO(1);
#pragma unroll
        for (int s = 0; s < 16; ++s) {
          if (s + 2 < 16) xs[(s + 2) % 3] = LDS4(x0 + (s + 2) * 1024);
          SB();
          acc[hf] = mfma32(xs[s % 3], wq[s], acc[hf]);
          if (hf == 0 && s == 12) xb_issue();
          if (hf == 1 && s >= 8) wo[s - 8] = ldw(rs, vo, wob, s - 8);
          SB();
        }
        KEEP(acc[hf]);
        PRIO(0);
#pragma unroll
        for (int u = 0; u < 2; ++u) *(LAS f32x4*)(L + L_XCH + ((hf * 8 + v) * 2 + u) * 1024 + vo) = q4(acc[hf], 2 + u);
      };
      auto epi_qk = [&](int hf) {
        const f32x2 ab = final_stats<false>(L, lane, hf);
        f32x4 csn[4];
#pragma unroll
        for (int u = 0; u < 4; ++u) csn[u] = LDSF4(L_CQ + ((u >> 1) * 64 + 16 * v + 8 * (u & 1) + 4 * hh) * 4);
        const float cexp = 0.25f * 1.4426950408889634f;
        f32x16 val;
#pragma unroll
        for (int q = 0; q < 16; ++q) { const float sc = q < 8 ? cexp : 1.0f; val[q] = acc[hf][q] * (ab.x * sc) + csn[q >> 2][q & 3] * (ab.y * sc); }
        const v4u K = nosent2(pk8(val, 8));
        st_sc1(K, rs, vo, KV_OFF + ((4 * j + v) * 64 + 2 * c + hf) * 2048);
        *(LAS v4u*)(L + L_KV + ((8 + hf) * 4 + v) * 2048 + vo) = K;
        *(LAS v4u*)(L + L_Q + (v * 2 + hf) * 1024 + vo) = pk8(val, 0);
      };
      auto epi_v = [&](int hf) {
        const int kh = v & 1, pp = (v - 4) >> 1;
        (void)final_stats<false>(L, lane, hf);
        const float csv = *(LAS float*)(L + L_CQ + (128 + 32 * pp + tk) * 4);
        f32x16 val;
#pragma unroll
        for (int u = 0; u < 2; ++u) {
          const f32x4 o = LDSF4(L_XCH + ((hf * 8 + (v ^ 1)) * 2 + u) * 1024 + vo);
          const f32x4 t0 = LDSF4(L_FST + (hf * 32 + 16 * kh + 8 * u + 4 * hh) * 8), t1 = LDSF4(L_FST + (hf * 32 + 16 * kh + 8 * u + 4 * hh + 2) * 8);
          val[4 * u + 0] = (acc[hf][4 * u + 0] + o[0]) * t0[0] + csv * t0[1];
          val[4 * u + 1] = (acc[hf][4 * u + 1] + o[1]) * t0[2] + csv * t0[3];
          val[4 * u + 2] = (acc[hf][4 * u + 2] + o[2]) * t1[0] + csv * t1[1];
          val[4 * u + 3] = (acc[hf][4 * u + 3] + o[3]) * t1[2] + csv * t1[3];
        }
        const int hb = (lane >> 4) & 1, d = lane & 15;
        const v4u V = nosent2(pk8(val, 0));
        st_sc1(V, rs, ((4 * j + 2 * pp + hb) * 64) * 2048 + 1024 + (32 * hh + 16 * kh + d) * 16, KV_OFF + (2 * c + hf) * 2048);
        *(LAS v4u*)(L + L_KV + ((8 + hf) * 4 + 2 * pp + hb) * 2048 + 1024 + (32 * hh + 16 * kh + d) * 16) = V;
      };
      if (lay) verify<4>(stA, [&](int i) { return ld_seq(rs, vo, xtb, i); }, tmo, 0x10, lane);
      STAMP(16 + lay * 24 + 0);
      fill_half<true>(L, 0, v, lane, stA);
      {
        const int wq2b = RFL(v < 4 ? WQ_OFF + (j * 192 + v * 32) * KB : OOB);
#pragma unroll
        for (int i = 16; i < 32; ++i) wq[i] = ldw(rs, vo, wq2b, i);
        SB();
      }
      __syncthreads();
      STAMP(16 + lay * 24 + 1);
      if (v < 4) pass_qk(0); else pass_v(0);
      STAMP(16 + lay * 24 + 2);
      if (lay) verify<4>(stB, [&](int i) { return ld_seq(rs, vo, xtb + 32768, i); }, tmo, 0x11, lane);
      fill_half<true>(L, 1, v, lane, stB);
      __syncthreads();
      STAMP(16 + lay * 24 + 3);
      if (v < 4) { epi_qk(0); pass_qk(1); } else { pass_v(1); epi_v(0); }
      __syncthreads();
      if (v < 4) epi_qk(1); else epi_v(1);
      STAMP(16 + lay * 24 + 4);
      v4u kvn[8];
      auto kv_load = [&](int i) {
        const int kt = 2 * c - 8 + 4 * (v >> 2) + (i >> 1);
        return ld_sc1(rs, vo + (i & 1) * 1024, RFL(kt >= 0 ? KV_OFF + ((4 * j + (v & 3)) * 64 + kt) * 2048 : OOB));
      };
      auto kv_issue = [&]() {
#pragma unroll
        for (int i = 0; i < 8; ++i) kvn[i] = kv_load(i);
      };
      WAITV0();
      __syncthreads();
      STAMP(16 + lay * 24 + 6);
      kv_issue();
      const int w1b = RFL(v < 7 ? W1_OFF + (j * 224 + v * 32) * KB : OOB);
#pragma unroll
      for (int i = 0; i < 4; ++i) w1[i] = ldw(rs, vo, w1b, i);
      SB();
      verify<8>(kvn, kv_load, tmo, 0x20, lane);
      STAMP(16 + lay * 24 + 7);
      {
        v4u SENT4 = {SENT, SENT, SENT, SENT};
        asm volatile("" : "+v"(SENT4));
        const int tsl = (8 * j + 4 * (v & 1) + (v >> 1)) * 512 + tk * 16 + hh * 8;
#pragma unroll
        for (int hf = 0; hf < 2; ++hf) {
          __builtin_amdgcn_raw_buffer_store_b64(v2u{SENT4.x, SENT4.y}, rs, tsl + X1T_OFF + c * TILE_B + hf * 32768, 0, AUX_SC1);
          if (lay == 0) __builtin_amdgcn_raw_buffer_store_b64(v2u{SENT4.x, SENT4.y}, rs, tsl + XT1_OFF + c * TILE_B + hf * 32768, 0, AUX_SC1);
        }
        const int hb = RFL(v < 7 ? HT_OFF + c * HT_C + (28 * j + 4 * v) * 512 : OOB);
#pragma unroll
        for (int u = 0; u < 4; ++u) st_sc1(SENT4, rs, vo, hb + (u >> 1) * 114688 + 2 * (u & 1) * 512);
        if (lay == 0) st_sc1(SENT4, rs, vo, OT1_OFF + c * TILE_B + (v >> 2) * 32768 + (2 * (4 * j + (v & 3))) * 512);
      }
      SB();
#pragma unroll
      for (int i = 0; i < 4; ++i) {
        *(LAS v4u*)(L + L_KV + ((4 * (v >> 2) + i) * 4 + (v & 3)) * 2048 + vo) = kvn[2 * i];
        *(LAS v4u*)(L + L_KV + ((4 * (v >> 2) + i) * 4 + (v & 3)) * 2048 + 1024 + vo) = kvn[2 * i + 1];
      }
      __syncthreads();
    }
    STAMP(1 + lay * 8);
    {
      FRESH();
      const int hq = v & 3, r = v >> 2, tq = tk - 4 * hh;
      const int kb = L_KV + (r * 4 + hq) * 2048;
      const v4u Q = LDS4(L_Q + (hq * 2 + r) * 1024 + vo);
      v4u Kf[9];
#pragma unroll
      for (int i = 0; i < 9; ++i) Kf[i] = LDS4(kb + i * 8192 + vo);
      const bool ones_lane = (lane & 16) != 0;
      const unsigned vsel = ones_lane ? 0u : 1u, vbase = ones_lane ? (unsigned)L_ONES : (unsigned)(kb + 1024 + (32 * hh + (lane & 15)) * 16);
      auto vop = [&](int i, int s) -> v4u { return LDS4(vbase + vsel * (unsigned)(i * 8192 + s * 256)); };
      const int first_ok = 8 - r - 2 * c;
      const int w1b = RFL(v < 7 ? W1_OFF + (j * 224 + v * 32) * KB : OOB);
      auto tmax = [&](const f32x16& x, float m) {
        const float a = max3f(x[0], x[1], x[2]), b2_ = max3f(x[3], x[4], x[5]), c2 = max3f(x[6], x[7], x[8]), d = max3f(x[9], x[10], x[11]), e = max3f(x[12], x[13], x[14]);
        return max3f(max3f(a, b2_, x[15]), max3f(c2, d, e), m);
      };
      f32x16 Sm, S[2];
      {
        f32x16 Sa = mfma32(Kf[0], Q, Z);
        const f32x16 Sb = mfma32(Kf[8], Q, Z);
        S[1] = mfma32(Kf[1], Q, Z);
        if (first_ok > 0) {
#pragma unroll
          for (int q = 0; q < 16; ++q) Sa[q] = -1e30f;
        }
#pragma unroll
        for (int q = 0; q < 16; ++q) { const int row = (q & 3) + 8 * (q >> 2); Sm[q] = row <= tq ? Sb[q] : Sa[q]; }
      }
      float mx = tmax(Sm, -1e30f);
      SB();
#pragma unroll
      for (int i = 1; i < 8; ++i) {
        if (i + 1 < 8) S[(i + 1) & 1] = mfma32(Kf[i + 1], Q, Z);
        if (i >= first_ok) mx = tmax(S[i & 1], mx);
        if (i < 6) w1[4 + (i - 1)] = ldw(rs, vo, w1b, 4 + (i - 1));
        SB();
      }
      { const f32x2 rr = both_halves(mx); mx = max3f(rr.x, rr.y, rr.y); }
      f32x16 Cm;
#pragma unroll
      for (int q = 0; q < 16; ++q) Cm[q] = -mx;
      S[1] = mfma32(Kf[1], Q, Cm);
      f32x16 O;
      {
        const v4u vn0 = vop(8, 0), vn1 = vop(8, 1), vo0 = vop(0, 0), vo1 = vop(0, 1);
        unsigned pp[8];
#pragma unroll
        for (int q = 0; q < 8; ++q) pp[q] = pk(__builtin_amdgcn_exp2f(Sm[2 * q] - mx), __builtin_amdgcn_exp2f(Sm[2 * q + 1] - mx));
        v4u pn[2], po[2];
#pragma unroll
        for (int q = 0; q < 8; ++q) {
          const int row0 = ((2 * q) & 3) + 8 * ((2 * q) >> 2);
          const unsigned mk = (row0 <= tq ? 0xFFFFu : 0u) | (row0 + 1 <= tq ? 0xFFFF0000u : 0u);
          pn[q >> 2][q & 3] = pp[q] & mk; po[q >> 2][q & 3] = pp[q] & ~mk;
        }
        O = mfma32(vn0, pn[0], Z); O = mfma32(vn1, pn[1], O);
        O = mfma32(vo0, po[0], O); O = mfma32(vo1, po[1], O);
        SB();
      }
#pragma unroll
      for (int i = 1; i < 8; ++i) {
        if (i + 1 < 8) S[(i + 1) & 1] = mfma32(Kf[i + 1], Q, Cm);
        const v4u va0 = vop(i, 0), va1 = vop(i, 1);
        if (i >= first_ok) {
          const f32x16& cur = S[i & 1];
          v4u pp[2];
#pragma unroll
          for (int q = 0; q < 8; ++q) pp[q >> 2][q & 3] = pk(__builtin_amdgcn_exp2f(cur[2 * q]), __builtin_amdgcn_exp2f(cur[2 * q + 1]));
          O = mfma32(va0, pp[0], O); O = mfma32(va1, pp[1], O);
        }
        w1[9 + (i - 1)] = ldw(rs, vo, w1b, 9 + (i - 1));
        SB();
      }
      KEEP(O);
      const float rl = __builtin_amdgcn_rcpf(O[8]);
      asm volatile("s_waitcnt vmcnt(12)" ::: "memory");
      st_sc1(nosent2(swap_T8(pk8(O, 0, rl))), rs, vo, OT_OFF + c * TILE_B + r * 32768 + (2 * (4 * j + hq)) * 512);
      if (v < 4) WAITV0();
    }
    STAMP(2 + lay * 8);
    {
      FRESH();
      const int otb = RFL(OT_OFF + c * TILE_B + v * 4096);
      const int w1b = RFL(v < 7 ? W1_OFF + (j * 224 + v * 32) * KB : OOB);
      auto oa_issue = [&]() {
#pragma unroll
        for (int i = 0; i < 4; ++i) stA[i] = ld_seq(rs, vo, otb, i);
      };
      auto ob_issue = [&]() {
#pragma unroll
        for (int i = 0; i < 4; ++i) stB[i] = ld_seq(rs, vo, otb + 32768, i);
      };
      f32x16 a;
      auto pass = [&](int hf) {
        const int x0 = L_TILE + hf * 32768 + (v >> 1) * 8192 + vo;
        v4u xs[8];
#pragma unroll
        for (int s = 0; s < 8; ++s) xs[s] = LDS4(x0 + s * 1024);
        a = Z;
#pragma unroll
        for (int s = 0; s < 8; ++s) a = mfma32(wo[s], xs[s], a);
        KEEP(a);
#pragma unroll
        for (int u = 1; u < 4; ++u) *(LAS f32x4*)(L + L_XCH + ((hf * 8 + (v ^ (2 * u))) * 3 + (u - 1)) * 1024 + vo) = q4(a, u);
      };
      auto epi = [&](int hf) {
        f32x4 o = q4(a, 0);
#pragma unroll
        for (int k = 0; k < 3; ++k) o += LDSF4(L_XCH + ((hf * 8 + v) * 3 + k) * 1024 + vo);
        xr[hf] = pk4(unpk4(xr[hf]) + o);
        __builtin_amdgcn_raw_buffer_store_b64(v2u{nosent(xr[hf].x), xr[hf].y}, rs, (8 * j + 4 * (v & 1) + (v >> 1)) * 512 + tk * 16 + hh * 8 + X1T_OFF + c * TILE_B + hf * 32768, 0, AUX_SC1);
      };
      oa_issue();
#pragma unroll
      for (int i = 16; i < 24; ++i) w1[i] = ldw(rs, vo, w1b, i);
      SB();
      verify<4>(stA, [&](int i) { return ld_seq(rs, vo, otb, i); }, tmo, 0x30, lane);
      STAMP(16 + lay * 24 + 8);
      __syncthreads();
      fill_half<false>(L, 0, v, lane, stA);
      ob_issue();
      __syncthreads();
      STAMP(16 + lay * 24 + 9);
      pass(0);
      STAMP(16 + lay * 24 + 21);
      verify<4>(stB, [&](int i) { return ld_seq(rs, vo, otb + 32768, i); }, tmo, 0x31, lane);
      fill_half<false>(L, 1, v, lane, stB);
      __syncthreads();
      epi(0);
      STAMP(16 + lay * 24 + 23);
      pass(1);
#pragma unroll
      for (int i = 24; i < 32; ++i) w1[i] = ldw(rs, vo, w1b, i);
      __syncthreads();
#pragma unroll
      for (int i = 0; i < 4; ++i) stA[i] = ld_seq(rs, vo, RFL(X1T_OFF + c * TILE_B + v * 4096), i);
      SB();
      epi(1);
    }
    STAMP(3 + lay * 8);
    v4u hA[14];
    {
      FRESH();
      const int x1b = RFL(X1T_OFF + c * TILE_B + v * 4096);
      const int w2b = RFL(W2_OFF + (j * 224 + v * 28) * KB);
      const int hb = RFL(v < 7 ? HT_OFF + c * HT_C + (28 * j + 4 * v) * 512 : OOB);
      auto xb_issue = [&]() {
#pragma unroll
        for (int i = 0; i < 4; ++i) stB[i] = ld_seq(rs, vo, x1b + 32768, i);
      };
      f32x16 a;
      auto pass = [&](int hf) {
        v4u xs[3];
        const int x0 = L_TILE + hf * 32768 + vo;
#pragma unroll
        for (int s = 0; s < 2; ++s) xs[s] = LDS4(x0 + s * 1024);
        a = Z;
        PRIO(1);
#pragma unroll
        for (int s = 0; s < 32; ++s) {
          if (s + 2 < 32) xs[(s + 2) % 3] = LDS4(x0 + (s + 2) * 1024);
          SB();
          a = mfma32(w1[s], xs[s % 3], a);
          if (hf == 0 && s == 8) xb_issue();
          if (hf == 0 && s >= 22) w2[s - 22] = ldw(rs, vo, w2b, s - 22);
          if (hf == 1 && s >= 2 && s < 20) w2[10 + (s - 2)] = ldw(rs, vo, w2b, 10 + (s - 2));
          SB();
        }
        KEEP(a);
        PRIO(0);
      };
      v4u ho[2];
      auto gelu = [&](int hf) {
        const f32x2 ab = final_stats<true>(L, lane, hf);
        const float c1 = -2.0f * 0.7978845608028654f * 1.4426950408889634f, c2 = c1 * 0.044715f;
#pragma unroll
        for (int u2 = 0; u2 < 2; ++u2) {
          f32x16 hv;
#pragma unroll
          for (int u = 2 * u2; u < 2 * u2 + 2; ++u) {
            const f32x4 csn = LDSF4(L_C1 + (32 * v + 8 * u + 4 * hh) * 4), bn = LDSF4(L_C1 + 1024 + (32 * v + 8 * u + 4 * hh) * 4);
#pragma unroll
            for (int i = 0; i < 4; ++i) {
              const float z = a[4 * u + i] * ab.x + (csn[i] * ab.y + bn[i]);
              const float e = __builtin_amdgcn_exp2f(z * (z * z * c2 + c1));
              hv[4 * u + i] = z * __builtin_amdgcn_rcpf(1.0f + e);
            }
          }
          ho[u2] = nosent2(swap_T8(pk8(hv, 8 * u2)));
          if (hf == 0) st_sc1(ho[u2], rs, vo, hb + 2 * u2 * 512);
        }
      };
      verify<4>(stA, [&](int i) { return ld_seq(rs, vo, x1b, i); }, tmo, 0x40, lane);
      STAMP(16 + lay * 24 + 10);
      fill_half<true>(L, 0, v, lane, stA);
      __syncthreads();
      STAMP(16 + lay * 24 + 11);
      pass(0);
      STAMP(16 + lay * 24 + 12);
      gelu(0);
      STAMP(16 + lay * 24 + 13);
      verify<4>(stB, [&](int i) { return ld_seq(rs, vo, x1b + 32768, i); }, tmo, 0x41, lane);
      fill_half<true>(L, 1, v, lane, stB);
      __syncthreads();
      STAMP(16 + lay * 24 + 14);
      pass(1);
      STAMP(16 + lay * 24 + 15);
#pragma unroll
      for (int s = 0; s < 14; ++s) hA[s] = ld_seq(rs, vo, RFL(HT_OFF + c * HT_C + 28 * v * 512), s);
      SB();
      gelu(1);
#pragma unroll
      for (int u2 = 0; u2 < 2; ++u2) st_sc1(ho[u2], rs, vo, hb + 114688 + 2 * u2 * 512);
    }
    STAMP(4 + lay * 8);
    {
      FRESH();
      const int htb = RFL(HT_OFF + c * HT_C + 28 * v * 512);
      const int wqb = RFL(lay == 0 ? WQ_OFF + (j * 192 + (v < 4 ? v * 32 : 128 + (v - 4) * 16)) * KB : OOB), wqb2 = RFL(v < 4 ? wqb : OOB);
      const int xnb = RFL(lay == 0 ? XT1_OFF + c * TILE_B + v * 4096 : OOB);
#pragma unroll
      for (int hf = 0; hf < 2; ++hf) {
        verify<14>(hA, [&](int i) { return ld_seq(rs, vo, htb + hf * 114688, i); }, tmo, 0x50 + hf, lane);
        if (hf == 0) __syncthreads();
        STAMP(16 + lay * 24 + 17 + 2 * hf);
        f32x16 ac[2] = {Z, Z};
        PRIO(1);
#pragma unroll
        for (int s = 0; s < 14; ++s) {
          ac[0] = mfma32(w2[2 * s], hA[s], ac[0]);
          ac[1] = mfma32(w2[2 * s + 1], hA[s], ac[1]);
          if (hf == 1) wq[s] = ldw(rs, vo, wqb, s);
          else hA[s] = ld_seq(rs, vo, htb + 114688, s);
          SB();
        }
        KEEP(ac[0]); KEEP(ac[1]);
        PRIO(0);
        STAMP(16 + lay * 24 + 18 + 2 * hf);
        LAS unsigned char* R = L + (hf ? L_TILE : L_RB);
#pragma unroll
        for (int nt = 0; nt < 2; ++nt)
#pragma unroll
          for (int u = 0; u < 4; ++u) *(LAS f32x4*)(R + ((nt + 2 * u) * 8 + v) * 1024 + vo) = q4(ac[nt], u);
        if (hf == 1) { wq[14] = ldw(rs, vo, wqb, 14); wq[15] = ldw(rs, vo, wqb, 15); }
        __syncthreads();
        f32x4 s0 = {0, 0, 0, 0};
#pragma unroll
        for (int k = 0; k < 8; ++k) s0 += *(LAS f32x4*)(R + (v * 8 + k) * 1024 + vo);
        const f32x4 b0 = LDSF4(L_CQ + (192 + 32 * (v & 1) + 8 * (v >> 1) + 4 * hh) * 4);
        xr[hf] = pk4(unpk4(xr[hf]) + s0 + b0);
        if (hf == 1) {
#pragma unroll
          for (int i = 0; i < 4; ++i) stA[i] = ld_seq(rs, vo, xnb, i);
          SB();
        }
        if (lay == 0) {
          __builtin_amdgcn_raw_buffer_store_b64(v2u{nosent(xr[hf].x), xr[hf].y}, rs, (8 * j + 4 * (v & 1) + (v >> 1)) * 512 + tk * 16 + hh * 8 + XT1_OFF + c * TILE_B + hf * 32768, 0, AUX_SC1);
        } else {
          *(v2u*)(p.out + (size_t)(64 * c + 32 * hf + tk) * DM + 64 * j + 32 * (v & 1) + 8 * (v >> 1) + 4 * hh) = xr[hf];
        }
      }
      __syncthreads();
    }
    STAMP(5 + lay * 8);
  }
  STAMP_FLUSH();
}
}

extern "C" void kernel_launch(void* const* d_in, const int* in_sizes, int n_in, void* d_out, int out_size, void* d_ws, size_t ws_size,
                              hipStream_t stream) {
  using namespace fk;
  Params p;
  p.x = (const u16*)d_in[0]; p.wqkv = (const u16*)d_in[1]; p.wo = (const u16*)d_in[2];
  p.w1 = (const u16*)d_in[4]; p.b1 = (const u16*)d_in[5]; p.w2 = (const u16*)d_in[6]; p.b2 = (const u16*)d_in[7];
  p.out = (u16*)d_out; p.ws = (unsigned char*)d_ws;
  (void)hipFuncSetAttribute((const void*)k_main, hipFuncAttributeMaxDynamicSharedMemorySize, L_BYTES);
  k_prep<<<PREP_BLOCKS, 256, 0, stream>>>(p);
  k_main<<<256, 512, L_BYTES, stream>>>(p);
}
```

```cpp
#include <hip/hip_runtime.h>
#include <stdint.h>

namespace fk {
typedef unsigned short u16;
typedef unsigned v4u __attribute__((ext_vector_type(4)));
typedef unsigned v2u __attribute__((ext_vector_type(2)));
typedef float f32x4 __attribute__((ext_vector_type(4)));
typedef float f32x2 __attribute__((ext_vector_type(2)));
typedef float f32x16 __attribute__((ext_vector_type(16)));
typedef __bf16 bf8v __attribute__((ext_vector_type(8)));
typedef __bf16 bf2v __attribute__((ext_vector_type(2)));
#define LAS __attribute__((address_space(3)))
#define GAS __attribute__((address_space(1)))
typedef __amdgpu_buffer_rsrc_t rsrc_t;

constexpr int SEQ = 2048, DM = 512, FF = 1792;
constexpr int KB = 1024;
constexpr int WQ_OFF = 0;
constexpr int WO_OFF = WQ_OFF + 8 * 192 * KB;
constexpr int W1_OFF = WO_OFF + 8 * 64 * KB;
constexpr int W2_OFF = W1_OFF + 8 * 224 * KB;
constexpr int CS_OFF = W2_OFF + 8 * 224 * KB;
constexpr int CTRL_OFF = CS_OFF + 16 * KB;
constexpr int TILE_B = 64 * KB;
constexpr int KV_B = 32 * 64 * 2 * KB;
constexpr int HT_C = 2 * 224 * 512;
constexpr int KV0_OFF = CTRL_OFF + 16 * KB;
constexpr int OT0_OFF = KV0_OFF + KV_B;
constexpr int KV1_OFF = OT0_OFF + 32 * TILE_B;
constexpr int OT1_OFF = KV1_OFF + KV_B;
constexpr int X1T0_OFF = OT1_OFF + 32 * TILE_B;
constexpr int X1T1_OFF = X1T0_OFF + 32 * TILE_B;
constexpr int XT1_OFF = X1T1_OFF + 32 * TILE_B;
constexpr int HT0_OFF = XT1_OFF + 32 * TILE_B;
constexpr int HT1_OFF = HT0_OFF + 32 * HT_C;
constexpr int WS_END = HT1_OFF + 32 * HT_C;
constexpr unsigned SENT = 0xFFFFFFFFu;
constexpr int AUX_SC1 = 16;
constexpr unsigned SPIN_CAP = 1u << 14;

constexpr int L_TILE = 0;
constexpr int L_KV = 0;
constexpr int L_RB = 64 * KB;
constexpr int L_Q = 80 * KB;
constexpr int L_XCH = 88 * KB;
constexpr int L_PST = 136 * KB;
constexpr int L_FST = 144 * KB;
constexpr int L_C1 = 145 * KB;
constexpr int L_CQ = 147 * KB;
constexpr int L_ONES = 148 * KB;
constexpr int L_BYTES = 148 * KB + 64;

__device__ __forceinline__ float bf2f(unsigned b) { return __builtin_bit_cast(float, b << 16); }
__device__ __forceinline__ float bflo(unsigned d) { return __builtin_bit_cast(float, d << 16); }
__device__ __forceinline__ float bfhi(unsigned d) { return __builtin_bit_cast(float, d & 0xFFFF0000u); }
__device__ __forceinline__ unsigned pk(float lo, float hi) {
  bf2v r = {(__bf16)lo, (__bf16)hi};
  return __builtin_bit_cast(unsigned, r);
}
__device__ __forceinline__ unsigned nosent(unsigned d) { return d < 0xFFFFFFFEu ? d : 0xFFFFFFFEu; }
__device__ __forceinline__ v2u pk4(f32x4 a) { return v2u{pk(a[0], a[1]), pk(a[2], a[3])}; }
__device__ __forceinline__ f32x4 unpk4(v2u v) { return f32x4{bflo(v.x), bfhi(v.x), bflo(v.y), bfhi(v.y)}; }
#define KEEP(x) asm volatile("" : "+v"(x))
#define WAITV0() asm volatile("s_waitcnt vmcnt(0)" ::: "memory")
#define PRIO(x) __builtin_amdgcn_s_setprio(x)
#define SB() __builtin_amdgcn_sched_barrier(0)

__device__ __forceinline__ v4u ldw(rsrc_t rs, int voff, int sbase, int i) {
  return __builtin_amdgcn_raw_buffer_load_b128(rs, voff + (i & 3) * 1024, sbase + (i >> 2) * 4096, 0);
}
__device__ __forceinline__ v4u ld_sc1(rsrc_t rs, int voff, int soff) {
  return __builtin_amdgcn_raw_buffer_load_b128(rs, voff, soff, AUX_SC1); }
__device__ __forceinline__ v4u ld_seq(rsrc_t rs, int voff, int sbase, int i) { return ld_sc1(rs, voff + (i & 3) * 1024, sbase + (i >> 2) * 4096); }
#define RFL(x) __builtin_amdgcn_readfirstlane(x)
__device__ __forceinline__ void st_sc1(v4u v, rsrc_t rs, int voff, int soff) {
  __builtin_amdgcn_raw_buffer_store_b128(v, rs, voff + soff, 0, AUX_SC1); }
__device__ __forceinline__ bool spin_fail(unsigned& spins, GAS unsigned* tmo, unsigned code, int lane) {
  ++spins;
  if (spins > SPIN_CAP) {
    if (lane == 0) __hip_atomic_store(tmo, code, __ATOMIC_RELAXED, __HIP_MEMORY_SCOPE_AGENT);
    return true;
  }
  if ((spins & 63u) == 0u && __hip_atomic_load(tmo, __ATOMIC_RELAXED, __HIP_MEMORY_SCOPE_AGENT) != 0u) return true;
  return false;
}

struct Params {
  const u16* x;
  const u16* wqkv;
  const u16* wo;
  const u16* w1;
  const u16* b1;
  const u16* w2;
  const u16* b2;
  u16* out;
  unsigned char* ws;
};

constexpr int NFRAG = 8 * 704;
constexpr int PB_TILE = NFRAG * 16 / 256;
constexpr int PB_CS = (1536 + 1792) / 64;
constexpr int CLEAN_B = KV_B + 32 * TILE_B;
constexpr int PB_CLEAN = CLEAN_B / (256 * 64);
constexpr int PREP_BLOCKS = PB_TILE + PB_CS + PB_CLEAN;

__global__ void __launch_bounds__(256) k_prep(Params p) {
  const int b = blockIdx.x, tid = threadIdx.x;
  if (b < PB_TILE) {
    const int F = b * 16 + (tid >> 4), t16 = tid & 15, cq = t16 & 7, h = t16 >> 3;
    const u16* W; int ldw_, k0, col;
    if (F < 8 * 192) {
      const int j = F / 192, i = F % 192;
      W = p.wqkv; ldw_ = 1536;
      if (i < 128) { const int head = 4 * j + (i >> 5); k0 = 16 * (i & 31); col = cq < 4 ? head * 16 + 4 * cq : 512 + head * 16 + 4 * (cq - 4); }
      else { const int u = i - 128, wv = u >> 4; k0 = 16 * (16 * (wv & 1) + (u & 15)); col = 1024 + (4 * j + 2 * (wv >> 1)) * 16 + 4 * cq; }
    } else if (F < 8 * 256) {
      const int f = F - 8 * 192, j = f >> 6, u = f & 63, v = u >> 3;
      W = p.wo; ldw_ = 512; k0 = 16 * (8 * (v >> 1) + (u & 7)); col = 64 * j + 32 * (v & 1) + 4 * (cq ^ (2 * (v >> 1)));
    } else if (F < 8 * 480) {
      const int f = F - 8 * 256, j = f / 224, u = f % 224;
      W = p.w1; ldw_ = 1792; k0 = 16 * (u & 31); col = 224 * j + 32 * (u >> 5) + 4 * cq;
    } else {
      const int f = F - 8 * 480, j = f / 224, u = f % 224, v = u / 28, q = u % 28;
      W = p.w2; ldw_ = 512; k0 = 224 * v + 16 * (q >> 1); col = 64 * j + 32 * (q & 1) + 4 * cq;
    }
    const u16* src = W + (size_t)(k0 + 8 * h) * ldw_ + col;
    v2u r[8];
#pragma unroll
    for (int e = 0; e < 8; ++e) r[e] = *(const v2u*)(src + (size_t)e * ldw_);
    v4u o[4];
#pragma unroll
    for (int e = 0; e < 4; ++e) {
      const unsigned a0 = r[2 * e].x, a1 = r[2 * e + 1].x, c0 = r[2 * e].y, c1 = r[2 * e + 1].y;
      o[0][e] = (a0 & 0xFFFFu) | (a1 << 16);
      o[1][e] = (a0 >> 16) | (a1 & 0xFFFF0000u);
      o[2][e] = (c0 & 0xFFFFu) | (c1 << 16);
      o[3][e] = (c0 >> 16) | (c1 & 0xFFFF0000u);
    }
    v4u* dst = (v4u*)(p.ws + (size_t)F * 1024 + (h * 32 + 4 * cq) * 16);
#pragma unroll
    for (int i = 0; i < 4; ++i) dst[i] = o[i];
  } else if (b < PB_TILE + PB_CS) {
    __shared__ float red[16][64];
    const int cb = b - PB_TILE;
    const u16* W; int ldw_, col;
    if (cb < 24) { W = p.wqkv; ldw_ = 1536; col = cb * 64; } else { W = p.w1; ldw_ = 1792; col = (cb - 24) * 64; }
    const int cq = tid & 15, rg = tid >> 4;
    float s0 = 0, s1 = 0, s2 = 0, s3 = 0;
    const u16* src = W + (size_t)(rg * 32) * ldw_ + col + 4 * cq;
#pragma unroll
    for (int e = 0; e < 32; ++e) {
      const v2u v = *(const v2u*)(src + (size_t)e * ldw_);
      s0 += bflo(v.x); s1 += bfhi(v.x); s2 += bflo(v.y); s3 += bfhi(v.y);
    }
    red[rg][4 * cq + 0] = s0; red[rg][4 * cq + 1] = s1; red[rg][4 * cq + 2] = s2; red[rg][4 * cq + 3] = s3;
    __syncthreads();
    if (tid < 64) {
      float s = 0;
#pragma unroll
      for (int r = 0; r < 16; ++r) s += red[r][tid];
      ((float*)(p.ws + CS_OFF))[cb * 64 + tid] = s;
    }
    if (cb == 0 && tid == 0) *(unsigned*)(p.ws + CTRL_OFF) = 0u;
  } else {
    const int cb = b - PB_TILE - PB_CS;
    v4u* dst = (v4u*)(p.ws + KV0_OFF + (size_t)cb * (256 * 64)) + tid;
    const v4u s = {SENT, SENT, SENT, SENT};
#pragma unroll
    for (int i = 0; i < 4; ++i) dst[i * 256] = s;
  }
}


__device__ __forceinline__ float max3f(float a, float b, float c) { float r; asm("v_max3_f32 %0, %1, %2, %3" : "=v"(r) : "v"(a), "v"(b), "v"(c)); return r; }
__device__ __forceinline__ unsigned max3u(unsigned a, unsigned b, unsigned c) { unsigned r; asm("v_max3_u32 %0, %1, %2, %3" : "=v"(r) : "v"(a), "v"(b), "v"(c)); return r; }
__device__ __forceinline__ f32x16 mfma32(v4u a, v4u b, f32x16 c) {
  return __builtin_amdgcn_mfma_f32_32x32x16_bf16(__builtin_bit_cast(bf8v, a), __builtin_bit_cast(bf8v, b), c, 0, 0, 0);
}
template <int N> __device__ __forceinline__ bool any_sent(const v4u (&d)[N]) {
  unsigned m0 = 0, m1 = 0;
#pragma unroll
  for (int i = 0; i < N; ++i) { if (i & 1) m1 = max3u(m1, d[i].x, d[i].z); else m0 = max3u(m0, d[i].x, d[i].z); }
  return __any(max3u(m0, m1, 0) == SENT);
}
template <int N, class F> __device__ __forceinline__ void verify(v4u (&d)[N], F load_i, GAS unsigned* tmo, unsigned code, int lane) {
  if (__builtin_expect(any_sent<N>(d), 0)) {
    unsigned spins = 0;
    do {
      __builtin_amdgcn_s_sleep(1);
      asm volatile("" ::: "memory");
#pragma unroll
      for (int i = 0; i < N; ++i)
        if (__any(max3u(d[i].x, d[i].z, 0u) == SENT)) d[i] = load_i(i);
    } while (any_sent<N>(d) && !spin_fail(spins, tmo, code, lane));
  }
}
__device__ __forceinline__ f32x2 both_halves(float v) {
  const unsigned a = __builtin_bit_cast(unsigned, v);
  auto r = __builtin_amdgcn_permlane32_swap(a, a, false, false);
  const unsigned r0 = r[0], r1 = r[1];
  return f32x2{__builtin_bit_cast(float, r0), __builtin_bit_cast(float, r1)};
}
__device__ __forceinline__ float dot2(unsigned a, unsigned b, float c) {
  return __builtin_amdgcn_fdot2_f32_bf16(__builtin_bit_cast(bf2v, a), __builtin_bit_cast(bf2v, b), c, false);
}
__device__ __forceinline__ v4u nosent2(v4u v) { return v4u{nosent(v.x), v.y, nosent(v.z), v.w}; }

__device__ __forceinline__ v4u pk8(const f32x16& a, int r0, float sc = 1.0f) {
  return v4u{pk(a[r0] * sc, a[r0 + 1] * sc), pk(a[r0 + 2] * sc, a[r0 + 3] * sc), pk(a[r0 + 4] * sc, a[r0 + 5] * sc), pk(a[r0 + 6] * sc, a[r0 + 7] * sc)};
}
__device__ __forceinline__ v4u swap_T8(v4u p) {
  auto r0 = __builtin_amdgcn_permlane32_swap(p[0], p[2], false, false);
  auto r1 = __builtin_amdgcn_permlane32_swap(p[1], p[3], false, false);
  return v4u{r0[0], r1[0], r0[1], r1[1]};
}
__device__ __forceinline__ f32x4 q4(const f32x16& a, int u) { return f32x4{a[4 * u], a[4 * u + 1], a[4 * u + 2], a[4 * u + 3]}; }
#define FRESH() int lane = lane_, c = c_, j = j_, v = v_; asm volatile("" : "+v"(lane), "+s"(c), "+s"(j), "+s"(v)); \
  const int vo = lane * 16, tk = lane & 31, hh = lane >> 5; (void)vo; (void)tk; (void)hh
#define LDS4(off) (*(const LAS v4u*)(L + (off)))
#define LDSF4(off) (*(const LAS f32x4*)(L + (off)))
constexpr int OOB = 0x7FF00000;

template <bool STATS>
__device__ __forceinline__ void fill_half(LAS unsigned char* L, int hf, int v, int lane, const v4u (&d)[4]) {
#pragma unroll
  for (int i = 0; i < 4; ++i) *(LAS v4u*)(L + L_TILE + hf * 32768 + v * 4096 + i * 1024 + lane * 16) = d[i];
  if (STATS) {
    float s0 = 0.f, s1 = 0.f, q0 = 0.f, q1 = 0.f;
#pragma unroll
    for (int i = 0; i < 4; ++i) {
      const v4u x = d[i];
      s0 = dot2(x[0], 0x3F803F80u, s0); q0 = dot2(x[0], x[0], q0);
      s1 = dot2(x[1], 0x3F803F80u, s1); q1 = dot2(x[1], x[1], q1);
      s0 = dot2(x[2], 0x3F803F80u, s0); q0 = dot2(x[2], x[2], q0);
      s1 = dot2(x[3], 0x3F803F80u, s1); q1 = dot2(x[3], x[3], q1);
    }
    *(LAS f32x2*)(L + L_PST + (((hf * 8 + v) * 2 + (lane >> 5)) * 32 + (lane & 31)) * 8) = f32x2{s0 + s1, q0 + q1};
  }
}
template <bool DOUBLE_LN>
__device__ __forceinline__ f32x2 final_stats(LAS unsigned char* L, int lane, int hf) {
  const int t = lane & 31, hh = lane >> 5;
  f32x2 pv[8];
#pragma unroll
  for (int k = 0; k < 8; ++k) pv[k] = *(LAS f32x2*)(L + L_PST + ((hf * 16 + hh * 8 + k) * 32 + t) * 8);
  SB();
  const f32x2 s = ((pv[0] + pv[1]) + (pv[2] + pv[3])) + ((pv[4] + pv[5]) + (pv[6] + pv[7]));
  const f32x2 rs_ = both_halves(s.x), rq = both_halves(s.y);
  const float mean = (rs_.x + rs_.y) * (1.0f / DM);
  float var = (rq.x + rq.y) * (1.0f / DM) - mean * mean;
  var = var > 0.f ? var : 0.f;
  float r = __builtin_amdgcn_rsqf(var + 1e-5f);
  if (DOUBLE_LN) {
    const float v2 = var * r * r;
    r *= __builtin_amdgcn_rsqf(v2 + 1e-5f);
  }
  const f32x2 ab = {r, -mean * r};
  *(LAS f32x2*)(L + L_FST + (hf * 32 + t) * 8) = ab;
  return ab;
}

__global__ void __launch_bounds__(512, 2) k_main(Params p) {
  extern __shared__ __attribute__((aligned(16))) unsigned char smem[];
  LAS unsigned char* L = (LAS unsigned char*)smem;
  const int b = blockIdx.x, tid = threadIdx.x, lane_ = tid & 63, v_ = __builtin_amdgcn_readfirstlane(tid >> 6);
  int c_, j_;
  c_ = ((b & 7) >> 1) * 8 + (b >> 5); j_ = (b & 1) * 4 + ((b >> 3) & 3);
  const rsrc_t rs = __builtin_amdgcn_make_buffer_rsrc((void*)p.ws, 0, WS_END, 0x00020000);
  const rsrc_t rx = __builtin_amdgcn_make_buffer_rsrc((void*)p.x, 0, SEQ * DM * 2, 0x00020000);
  GAS unsigned* tmo = (GAS unsigned*)(p.ws + CTRL_OFF);
  v2u xr[2]; v4u wq[32], stA[4];
  f32x16 Z;
#pragma unroll
  for (int q = 0; q < 16; ++q) Z[q] = 0.f;
  {
    FRESH();
    const float* cs = (const float*)(p.ws + CS_OFF);
    float k0 = 0.f, k1 = 0.f;
    if (tid < 224) { k0 = cs[1536 + 224 * j + tid]; k1 = bf2f(p.b1[224 * j + tid]); }
    else if (tid >= 256) { const int t = tid - 256; k0 = t < 192 ? cs[(t >> 6) * 512 + 64 * j + (t & 63)] : bf2f(p.b2[64 * j + t - 192]); }
#pragma unroll
    for (int hf = 0; hf < 2; ++hf) xr[hf] = *(const v2u*)(p.x + (size_t)(64 * c + 32 * hf + tk) * DM + 64 * j + 32 * (v & 1) + 8 * (v >> 1) + 4 * hh);
#pragma unroll
    for (int i = 0; i < 4; ++i) stA[i] = __builtin_amdgcn_raw_buffer_load_b128(rx, tk * 1024 + hh * 16 + i * 32, (64 * c) * 1024 + v * 128, 0);
    SB();
    __builtin_amdgcn_s_barrier();
    SB();
    const int wqb = RFL(WQ_OFF + (j * 192 + (v < 4 ? v * 32 : 128 + (v - 4) * 16)) * KB), wqb2 = RFL(v < 4 ? wqb : OOB);
#pragma unroll
    for (int i = 0; i < 16; ++i) wq[i] = ldw(rs, vo, wqb, i);
    SB();
    if (tid < 224) { *(LAS float*)(L + L_C1 + tid * 4) = k0; *(LAS float*)(L + L_C1 + 1024 + tid * 4) = k1; }
    else if (tid >= 256) *(LAS float*)(L + L_CQ + (tid - 256) * 4) = k0;
    else if (tid < 228) *(LAS unsigned*)(L + L_ONES + (tid - 224) * 4) = 0x3F803F80u;
    v4u SENT4 = {SENT, SENT, SENT, SENT};
    asm volatile("" : "+v"(SENT4));
#pragma unroll
    for (int hf = 0; hf < 2; ++hf) {
      if (v < 4) st_sc1(SENT4, rs, vo, KV1_OFF + ((4 * j + v) * 64 + 2 * c + hf) * 2048);
      else st_sc1(SENT4, rs, ((4 * j + 2 * ((v - 4) >> 1) + ((lane >> 4) & 1)) * 64) * 2048 + 1024 + (32 * hh + 16 * (v & 1) + (lane & 15)) * 16, KV1_OFF + (2 * c + hf) * 2048);
    }
    SB();
  }

  for (int lay = 0; lay < 2; ++lay) {
    const int KV_OFF = lay ? KV1_OFF : KV0_OFF, OT_OFF = lay ? OT1_OFF : OT0_OFF, X1T_OFF = lay ? X1T1_OFF : X1T0_OFF, HT_OFF = lay ? HT1_OFF : HT0_OFF;
    v4u wo[8], w1[32], w2[28], stB[4];
    {
      FRESH();
      const int xtb = RFL(XT1_OFF + c * TILE_B + v * 4096);
      const int wob = RFL(WO_OFF + (j * 64 + v * 8) * KB);
      f32x16 acc[2];
      auto xb_issue = [&]() {
        if (lay) {
#pragma unroll
          for (int i = 0; i < 4; ++i) stB[i] = ld_seq(rs, vo, xtb + 32768, i);
        } else {
#pragma unroll
          for (int i = 0; i < 4; ++i) stB[i] = __builtin_amdgcn_raw_buffer_load_b128(rx, tk * 1024 + hh * 16 + i * 32, (64 * c + 32) * 1024 + v * 128, 0);
        }
      };
      auto pass_qk = [&](int hf) {
        v4u xs[3];
        const int x0 = L_TILE + hf * 32768 + vo;
#pragma unroll
        for (int s = 0; s < 2; ++s) xs[s] = LDS4(x0 + s * 1024);
        acc[hf] = Z;
        PRIO(1);
#pragma unroll
        for (int s = 0; s < 32; ++s) {
          if (s + 2 < 32) xs[(s + 2) % 3] = LDS4(x0 + (s + 2) * 1024);
          SB();
          acc[hf] = mfma32(wq[s], xs[s % 3], acc[hf]);
          if (hf == 0 && s == 12) xb_issue();
          if (hf == 1 && s >= 24) wo[s - 24] = ldw(rs, vo, wob, s - 24);
          SB();
        }
        KEEP(acc[hf]);
        PRIO(0);
      };
      auto pass_v = [&](int hf) {
        const int kh = v & 1;
        v4u xs[3];
        const int x0 = L_TILE + hf * 32768 + kh * 16384 + (vo ^ (kh * 256));
#pragma unroll
        for (int s = 0; s < 2; ++s) xs[s] = LDS4(x0 + s * 1024);
        acc[hf] = Z;
        PRIO(1);
#pragma unroll
        for (int s = 0; s < 16; ++s) {
          if (s + 2 < 16) xs[(s + 2) % 3] = LDS4(x0 + (s + 2) * 1024);
          SB();
          acc[hf] = mfma32(xs[s % 3], wq[s], acc[hf]);
          if (hf == 0 && s == 12) xb_issue();
          if (hf == 1 && s >= 8) wo[s - 8] = ldw(rs, vo, wob, s - 8);
          SB();
        }
        KEEP(acc[hf]);
        PRIO(0);
#pragma unroll
        for (int u = 0; u < 2; ++u) *(LAS f32x4*)(L + L_XCH + ((hf * 8 + v) * 2 + u) * 1024 + vo) = q4(acc[hf], 2 + u);
      };
      auto epi_qk = [&](int hf) {
        const f32x2 ab = final_stats<false>(L, lane, hf);
        f32x4 csn[4];
#pragma unroll
        for (int u = 0; u < 4; ++u) csn[u] = LDSF4(L_CQ + ((u >> 1) * 64 + 16 * v + 8 * (u & 1) + 4 * hh) * 4);
        const float cexp = 0.25f * 1.4426950408889634f;
        f32x16 val;
#pragma unroll
        for (int q = 0; q < 16; ++q) { const float sc = q < 8 ? cexp : 1.0f; val[q] = acc[hf][q] * (ab.x * sc) + csn[q >> 2][q & 3] * (ab.y * sc); }
        const v4u K = nosent2(pk8(val, 8));
        st_sc1(K, rs, vo, KV_OFF + ((4 * j + v) * 64 + 2 * c + hf) * 2048);
        *(LAS v4u*)(L + L_KV + ((8 + hf) * 4 + v) * 2048 + vo) = K;
        *(LAS v4u*)(L + L_Q + (v * 2 + hf) * 1024 + vo) = pk8(val, 0);
      };
      auto epi_v = [&](int hf) {
        const int kh = v & 1, pp = (v - 4) >> 1;
        (void)final_stats<false>(L, lane, hf);
        const float csv = *(LAS float*)(L + L_CQ + (128 + 32 * pp + tk) * 4);
        f32x16 val;
#pragma unroll
        for (int u = 0; u < 2; ++u) {
          const f32x4 o = LDSF4(L_XCH + ((hf * 8 + (v ^ 1)) * 2 + u) * 1024 + vo);
          const f32x4 t0 = LDSF4(L_FST + (hf * 32 + 16 * kh + 8 * u + 4 * hh) * 8), t1 = LDSF4(L_FST + (hf * 32 + 16 * kh + 8 * u + 4 * hh + 2) * 8);
          val[4 * u + 0] = (acc[hf][4 * u + 0] + o[0]) * t0[0] + csv * t0[1];
          val[4 * u + 1] = (acc[hf][4 * u + 1] + o[1]) * t0[2] + csv * t0[3];
          val[4 * u + 2] = (acc[hf][4 * u + 2] + o[2]) * t1[0] + csv * t1[1];
          val[4 * u + 3] = (acc[hf][4 * u + 3] + o[3]) * t1[2] + csv * t1[3];
        }
        const int hb = (lane >> 4) & 1, d = lane & 15;
        const v4u V = nosent2(pk8(val, 0));
        st_sc1(V, rs, ((4 * j + 2 * pp + hb) * 64) * 2048 + 1024 + (32 * hh + 16 * kh + d) * 16, KV_OFF + (2 * c + hf) * 2048);
        *(LAS v4u*)(L + L_KV + ((8 + hf) * 4 + 2 * pp + hb) * 2048 + 1024 + (32 * hh + 16 * kh + d) * 16) = V;
      };
      if (lay) verify<4>(stA, [&](int i) { return ld_seq(rs, vo, xtb, i); }, tmo, 0x10, lane);
      fill_half<true>(L, 0, v, lane, stA);
      {
        const int wq2b = RFL(v < 4 ? WQ_OFF + (j * 192 + v * 32) * KB : OOB);
#pragma unroll
        for (int i = 16; i < 32; ++i) wq[i] = ldw(rs, vo, wq2b, i);
        SB();
      }
      __syncthreads();
      if (v < 4) pass_qk(0); else pass_v(0);
      if (lay) verify<4>(stB, [&](int i) { return ld_seq(rs, vo, xtb + 32768, i); }, tmo, 0x11, lane);
      fill_half<true>(L, 1, v, lane, stB);
      __syncthreads();
      if (v < 4) { epi_qk(0); pass_qk(1); } else { pass_v(1); epi_v(0); }
      __syncthreads();
      if (v < 4) epi_qk(1); else epi_v(1);
      v4u kvn[8];
      auto kv_load = [&](int i) {
        const int kt = 2 * c - 8 + 4 * (v >> 2) + (i >> 1);
        return ld_sc1(rs, vo + (i & 1) * 1024, RFL(kt >= 0 ? KV_OFF + ((4 * j + (v & 3)) * 64 + kt) * 2048 : OOB));
      };
      auto kv_issue = [&]() {
#pragma unroll
        for (int i = 0; i < 8; ++i) kvn[i] = kv_load(i);
      };
      WAITV0();
      __syncthreads();
      kv_issue();
      const int w1b = RFL(v < 7 ? W1_OFF + (j * 224 + v * 32) * KB : OOB);
#pragma unroll
      for (int i = 0; i < 4; ++i) w1[i] = ldw(rs, vo, w1b, i);
      SB();
      verify<8>(kvn, kv_load, tmo, 0x20, lane);
      {
        v4u SENT4 = {SENT, SENT, SENT, SENT};
        asm volatile("" : "+v"(SENT4));
        const int tsl = (8 * j + 4 * (v & 1) + (v >> 1)) * 512 + tk * 16 + hh * 8;
#pragma unroll
        for (int hf = 0; hf < 2; ++hf) {
          __builtin_amdgcn_raw_buffer_store_b64(v2u{SENT4.x, SENT4.y}, rs, tsl + X1T_OFF + c * TILE_B + hf * 32768, 0, AUX_SC1);
          if (lay == 0) __builtin_amdgcn_raw_buffer_store_b64(v2u{SENT4.x, SENT4.y}, rs, tsl + XT1_OFF + c * TILE_B + hf * 32768, 0, AUX_SC1);
        }
        const int hb = RFL(v < 7 ? HT_OFF + c * HT_C + (28 * j + 4 * v) * 512 : OOB);
#pragma unroll
        for (int u = 0; u < 4; ++u) st_sc1(SENT4, rs, vo, hb + (u >> 1) * 114688 + 2 * (u & 1) * 512);
        if (lay == 0) st_sc1(SENT4, rs, vo, OT1_OFF + c * TILE_B + (v >> 2) * 32768 + (2 * (4 * j + (v & 3))) * 512);
      }
      SB();
#pragma unroll
      for (int i = 0; i < 4; ++i) {
        *(LAS v4u*)(L + L_KV + ((4 * (v >> 2) + i) * 4 + (v & 3)) * 2048 + vo) = kvn[2 * i];
        *(LAS v4u*)(L + L_KV + ((4 * (v >> 2) + i) * 4 + (v & 3)) * 2048 + 1024 + vo) = kvn[2 * i + 1];
      }
      __syncthreads();
    }
    {
      FRESH();
      const int hq = v & 3, r = v >> 2, tq = tk - 4 * hh;
      const int kb = L_KV + (r * 4 + hq) * 2048;
      const v4u Q = LDS4(L_Q + (hq * 2 + r) * 1024 + vo);
      v4u Kf[9];
#pragma unroll
      for (int i = 0; i < 9; ++i) Kf[i] = LDS4(kb + i * 8192 + vo);
      const bool ones_lane = (lane & 16) != 0;
      const unsigned vsel = ones_lane ? 0u : 1u, vbase = ones_lane ? (unsigned)L_ONES : (unsigned)(kb + 1024 + (32 * hh + (lane & 15)) * 16);
      auto vop = [&](int i, int s) -> v4u { return LDS4(vbase + vsel * (unsigned)(i * 8192 + s * 256)); };
      const int first_ok = 8 - r - 2 * c;
      const int w1b = RFL(v < 7 ? W1_OFF + (j * 224 + v * 32) * KB : OOB);
      auto tmax = [&](const f32x16& x, float m) {
        const float a = max3f(x[0], x[1], x[2]), b2_ = max3f(x[3], x[4], x[5]), c2 = max3f(x[6], x[7], x[8]), d = max3f(x[9], x[10], x[11]), e = max3f(x[12], x[13], x[14]);
        return max3f(max3f(a, b2_, x[15]), max3f(c2, d, e), m);
      };
      f32x16 Sm, S[2];
      {
        f32x16 Sa = mfma32(Kf[0], Q, Z);
        const f32x16 Sb = mfma32(Kf[8], Q, Z);
        S[1] = mfma32(Kf[1], Q, Z);
        if (first_ok > 0) {
#pragma unroll
          for (int q = 0; q < 16; ++q) Sa[q] = -1e30f;
        }
#pragma unroll
        for (int q = 0; q < 16; ++q) { const int row = (q & 3) + 8 * (q >> 2); Sm[q] = row <= tq ? Sb[q] : Sa[q]; }
      }
      float mx = tmax(Sm, -1e30f);
      SB();
#pragma unroll
      for (int i = 1; i < 8; ++i) {
        if (i + 1 < 8) S[(i + 1) & 1] = mfma32(Kf[i + 1], Q, Z);
        if (i >= first_ok) mx = tmax(S[i & 1], mx);
        if (i < 6) w1[4 + (i - 1)] = ldw(rs, vo, w1b, 4 + (i - 1));
        SB();
      }
      { const f32x2 rr = both_halves(mx); mx = max3f(rr.x, rr.y, rr.y); }
      f32x16 Cm;
#pragma unroll
      for (int q = 0; q < 16; ++q) Cm[q] = -mx;
      S[1] = mfma32(Kf[1], Q, Cm);
      f32x16 O;
      {
        const v4u vn0 = vop(8, 0), vn1 = vop(8, 1), vo0 = vop(0, 0), vo1 = vop(0, 1);
        unsigned pp[8];
#pragma unroll
        for (int q = 0; q < 8; ++q) pp[q] = pk(__builtin_amdgcn_exp2f(Sm[2 * q] - mx), __builtin_amdgcn_exp2f(Sm[2 * q + 1] - mx));
        v4u pn[2], po[2];
#pragma unroll
        for (int q = 0; q < 8; ++q) {
          const int row0 = ((2 * q) & 3) + 8 * ((2 * q) >> 2);
          const unsigned mk = (row0 <= tq ? 0xFFFFu : 0u) | (row0 + 1 <= tq ? 0xFFFF0000u : 0u);
          pn[q >> 2][q & 3] = pp[q] & mk; po[q >> 2][q & 3] = pp[q] & ~mk;
        }
        O = mfma32(vn0, pn[0], Z); O = mfma32(vn1, pn[1], O);
        O = mfma32(vo0, po[0], O); O = mfma32(vo1, po[1], O);
        SB();
      }
#pragma unroll
      for (int i = 1; i < 8; ++i) {
        if (i + 1 < 8) S[(i + 1) & 1] = mfma32(Kf[i + 1], Q, Cm);
        const v4u va0 = vop(i, 0), va1 = vop(i, 1);
        if (i >= first_ok) {
          const f32x16& cur = S[i & 1];
          v4u pp[2];
#pragma unroll
          for (int q = 0; q < 8; ++q) pp[q >> 2][q & 3] = pk(__builtin_amdgcn_exp2f(cur[2 * q]), __builtin_amdgcn_exp2f(cur[2 * q + 1]));
          O = mfma32(va0, pp[0], O); O = mfma32(va1, pp[1], O);
        }
        w1[9 + (i - 1)] = ldw(rs, vo, w1b, 9 + (i - 1));
        SB();
      }
      KEEP(O);
      const float rl = __builtin_amdgcn_rcpf(O[8]);
      asm volatile("s_waitcnt vmcnt(12)" ::: "memory");
      st_sc1(nosent2(swap_T8(pk8(O, 0, rl))), rs, vo, OT_OFF + c * TILE_B + r * 32768 + (2 * (4 * j + hq)) * 512);
      if (v < 4) WAITV0();
    }
    {
      FRESH();
      const int otb = RFL(OT_OFF + c * TILE_B + v * 4096);
      const int w1b = RFL(v < 7 ? W1_OFF + (j * 224 + v * 32) * KB : OOB);
      auto oa_issue = [&]() {
#pragma unroll
        for (int i = 0; i < 4; ++i) stA[i] = ld_seq(rs, vo, otb, i);
      };
      auto ob_issue = [&]() {
#pragma unroll
        for (int i = 0; i < 4; ++i) stB[i] = ld_seq(rs, vo, otb + 32768, i);
      };
      f32x16 a;
      auto pass = [&](int hf) {
        const int x0 = L_TILE + hf * 32768 + (v >> 1) * 8192 + vo;
        v4u xs[8];
#pragma unroll
        for (int s = 0; s < 8; ++s) xs[s] = LDS4(x0 + s * 1024);
        a = Z;
#pragma unroll
        for (int s = 0; s < 8; ++s) a = mfma32(wo[s], xs[s], a);
        KEEP(a);
#pragma unroll
        for (int u = 1; u < 4; ++u) *(LAS f32x4*)(L + L_XCH + ((hf * 8 + (v ^ (2 * u))) * 3 + (u - 1)) * 1024 + vo) = q4(a, u);
      };
      auto epi = [&](int hf) {
        f32x4 o = q4(a, 0);
#pragma unroll
        for (int k = 0; k < 3; ++k) o += LDSF4(L_XCH + ((hf * 8 + v) * 3 + k) * 1024 + vo);
        xr[hf] = pk4(unpk4(xr[hf]) + o);
        __builtin_amdgcn_raw_buffer_store_b64(v2u{nosent(xr[hf].x), xr[hf].y}, rs, (8 * j + 4 * (v & 1) + (v >> 1)) * 512 + tk * 16 + hh * 8 + X1T_OFF + c * TILE_B + hf * 32768, 0, AUX_SC1);
      };
      oa_issue();
#pragma unroll
      for (int i = 16; i < 24; ++i) w1[i] = ldw(rs, vo, w1b, i);
      SB();
      verify<4>(stA, [&](int i) { return ld_seq(rs, vo, otb, i); }, tmo, 0x30, lane);
      __syncthreads();
      fill_half<false>(L, 0, v, lane, stA);
      ob_issue();
      __syncthreads();
      pass(0);
      verify<4>(stB, [&](int i) { return ld_seq(rs, vo, otb + 32768, i); }, tmo, 0x31, lane);
      fill_half<false>(L, 1, v, lane, stB);
      __syncthreads();
      epi(0);
      pass(1);
#pragma unroll
      for (int i = 24; i < 32; ++i) w1[i] = ldw(rs, vo, w1b, i);
      __syncthreads();
#pragma unroll
      for (int i = 0; i < 4; ++i) stA[i] = ld_seq(rs, vo, RFL(X1T_OFF + c * TILE_B + v * 4096), i);
      SB();
      epi(1);
    }
    v4u hA[14];
    {
      FRESH();
      const int x1b = RFL(X1T_OFF + c * TILE_B + v * 4096);
      const int w2b = RFL(W2_OFF + (j * 224 + v * 28) * KB);
      const int hb = RFL(v < 7 ? HT_OFF + c * HT_C + (28 * j + 4 * v) * 512 : OOB);
      auto xb_issue = [&]() {
#pragma unroll
        for (int i = 0; i < 4; ++i) stB[i] = ld_seq(rs, vo, x1b + 32768, i);
      };
      f32x16 a;
      auto pass = [&](int hf) {
        v4u xs[3];
        const int x0 = L_TILE + hf * 32768 + vo;
#pragma unroll
        for (int s = 0; s < 2; ++s) xs[s] = LDS4(x0 + s * 1024);
        a = Z;
        PRIO(1);
#pragma unroll
        for (int s = 0; s < 32; ++s) {
          if (s + 2 < 32) xs[(s + 2) % 3] = LDS4(x0 + (s + 2) * 1024);
          SB();
          a = mfma32(w1[s], xs[s % 3], a);
          if (hf == 0 && s == 8) xb_issue();
          if (hf == 0 && s >= 22) w2[s - 22] = ldw(rs, vo, w2b, s - 22);
          if (hf == 1 && s >= 2 && s < 20) w2[10 + (s - 2)] = ldw(rs, vo, w2b, 10 + (s - 2));
          SB();
        }
        KEEP(a);
        PRIO(0);
      };
      v4u ho[2];
      auto gelu = [&](int hf) {
        const f32x2 ab = final_stats<true>(L, lane, hf);
        const float c1 = -2.0f * 0.7978845608028654f * 1.4426950408889634f, c2 = c1 * 0.044715f;
#pragma unroll
        for (int u2 = 0; u2 < 2; ++u2) {
          f32x16 hv;
#pragma unroll
          for (int u = 2 * u2; u < 2 * u2 + 2; ++u) {
            const f32x4 csn = LDSF4(L_C1 + (32 * v + 8 * u + 4 * hh) * 4), bn = LDSF4(L_C1 + 1024 + (32 * v + 8 * u + 4 * hh) * 4);
#pragma unroll
            for (int i = 0; i < 4; ++i) {
              const float z = a[4 * u + i] * ab.x + (csn[i] * ab.y + bn[i]);
              const float e = __builtin_amdgcn_exp2f(z * (z * z * c2 + c1));
              hv[4 * u + i] = z * __builtin_amdgcn_rcpf(1.0f + e);
            }
          }
          ho[u2] = nosent2(swap_T8(pk8(hv, 8 * u2)));
          if (hf == 0) st_sc1(ho[u2], rs, vo, hb + 2 * u2 * 512);
        }
      };
      verify<4>(stA, [&](int i) { return ld_seq(rs, vo, x1b, i); }, tmo, 0x40, lane);
      fill_half<true>(L, 0, v, lane, stA);
      __syncthreads();
      pass(0);
      gelu(0);
      verify<4>(stB, [&](int i) { return ld_seq(rs, vo, x1b + 32768, i); }, tmo, 0x41, lane);
      fill_half<true>(L, 1, v, lane, stB);
      __syncthreads();
      pass(1);
#pragma unroll
      for (int s = 0; s < 14; ++s) hA[s] = ld_seq(rs, vo, RFL(HT_OFF + c * HT_C + 28 * v * 512), s);
      SB();
      gelu(1);
#pragma unroll
      for (int u2 = 0; u2 < 2; ++u2) st_sc1(ho[u2], rs, vo, hb + 114688 + 2 * u2 * 512);
    }
    {
      FRESH();
      const int htb = RFL(HT_OFF + c * HT_C + 28 * v * 512);
      const int wqb = RFL(lay == 0 ? WQ_OFF + (j * 192 + (v < 4 ? v * 32 : 128 + (v - 4) * 16)) * KB : OOB), wqb2 = RFL(v < 4 ? wqb : OOB);
      const int xnb = RFL(lay == 0 ? XT1_OFF + c * TILE_B + v * 4096 : OOB);
#pragma unroll
      for (int hf = 0; hf < 2; ++hf) {
        verify<14>(hA, [&](int i) { return ld_seq(rs, vo, htb + hf * 114688, i); }, tmo, 0x50 + hf, lane);
        if (hf == 0) __syncthreads();
        f32x16 ac[2] = {Z, Z};
        PRIO(1);
#pragma unroll
        for (int s = 0; s < 14; ++s) {
          ac[0] = mfma32(w2[2 * s], hA[s], ac[0]);
          ac[1] = mfma32(w2[2 * s + 1], hA[s], ac[1]);
          if (hf == 1) wq[s] = ldw(rs, vo, wqb, s);
          else hA[s] = ld_seq(rs, vo, htb + 114688, s);
          SB();
        }
        KEEP(ac[0]); KEEP(ac[1]);
        PRIO(0);
        LAS unsigned char* R = L + (hf ? L_TILE : L_RB);
#pragma unroll
        for (int nt = 0; nt < 2; ++nt)
#pragma unroll
          for (int u = 0; u < 4; ++u) *(LAS f32x4*)(R + ((nt + 2 * u) * 8 + v) * 1024 + vo) = q4(ac[nt], u);
        if (hf == 1) { wq[14] = ldw(rs, vo, wqb, 14); wq[15] = ldw(rs, vo, wqb, 15); }
        __syncthreads();
        f32x4 s0 = {0, 0, 0, 0};
#pragma unroll
        for (int k = 0; k < 8; ++k) s0 += *(LAS f32x4*)(R + (v * 8 + k) * 1024 + vo);
        const f32x4 b0 = LDSF4(L_CQ + (192 + 32 * (v & 1) + 8 * (v >> 1) + 4 * hh) * 4);
        xr[hf] = pk4(unpk4(xr[hf]) + s0 + b0);
        if (hf == 1) {
#pragma unroll
          for (int i = 0; i < 4; ++i) stA[i] = ld_seq(rs, vo, xnb, i);
          SB();
        }
        if (lay == 0) {
          __builtin_amdgcn_raw_buffer_store_b64(v2u{nosent(xr[hf].x), xr[hf].y}, rs, (8 * j + 4 * (v & 1) + (v >> 1)) * 512 + tk * 16 + hh * 8 + XT1_OFF + c * TILE_B + hf * 32768, 0, AUX_SC1);
        } else {
          *(v2u*)(p.out + (size_t)(64 * c + 32 * hf + tk) * DM + 64 * j + 32 * (v & 1) + 8 * (v >> 1) + 4 * hh) = xr[hf];
        }
      }
      __syncthreads();
    }
  }
}
}

extern "C" void kernel_launch(void* const* d_in, const int* in_sizes, int n_in, void* d_out, int out_size, void* d_ws, size_t ws_size,
                              hipStream_t stream) {
  using namespace fk;
  Params p;
  p.x = (const u16*)d_in[0]; p.wqkv = (const u16*)d_in[1]; p.wo = (const u16*)d_in[2];
  p.w1 = (const u16*)d_in[4]; p.b1 = (const u16*)d_in[5]; p.w2 = (const u16*)d_in[6]; p.b2 = (const u16*)d_in[7];
  p.out = (u16*)d_out; p.ws = (unsigned char*)d_ws;
  (void)hipFuncSetAttribute((const void*)k_main, hipFuncAttributeMaxDynamicSharedMemorySize, L_BYTES);
  k_prep<<<PREP_BLOCKS, 256, 0, stream>>>(p);
  k_main<<<256, 512, L_BYTES, stream>>>(p);
}
```

```cpp
#include <hip/hip_runtime.h>
#include <stdint.h>

namespace fk {
typedef unsigned short u16;
typedef unsigned v4u __attribute__((ext_vector_type(4)));
typedef unsigned v2u __attribute__((ext_vector_type(2)));
typedef float f32x4 __attribute__((ext_vector_type(4)));
typedef float f32x2 __attribute__((ext_vector_type(2)));
typedef float f32x16 __attribute__((ext_vector_type(16)));
typedef __bf16 bf8v __attribute__((ext_vector_type(8)));
typedef __bf16 bf2v __attribute__((ext_vector_type(2)));
#define LAS __attribute__((address_space(3)))
#define GAS __attribute__((address_space(1)))
typedef __amdgpu_buffer_rsrc_t rsrc_t;

constexpr int SEQ = 2048, DM = 512, FF = 1792;
constexpr int KB = 1024;
constexpr int WQ_OFF = 0;
constexpr int WO_OFF = WQ_OFF + 8 * 192 * KB;
constexpr int W1_OFF = WO_OFF + 8 * 64 * KB;
constexpr int W2_OFF = W1_OFF + 8 * 224 * KB;
constexpr int CS_OFF = W2_OFF + 8 * 224 * KB;
constexpr int CTRL_OFF = CS_OFF + 16 * KB;
constexpr int TILE_B = 64 * KB;
constexpr int KV_B = 32 * 64 * 2 * KB;
constexpr int HT_C = 2 * 224 * 512;
constexpr int KV0_OFF = CTRL_OFF + 16 * KB;
constexpr int OT0_OFF = KV0_OFF + KV_B;
constexpr int KV1_OFF = OT0_OFF + 32 * TILE_B;
constexpr int OT1_OFF = KV1_OFF + KV_B;
constexpr int X1T0_OFF = OT1_OFF + 32 * TILE_B;
constexpr int X1T1_OFF = X1T0_OFF + 32 * TILE_B;
constexpr int XT1_OFF = X1T1_OFF + 32 * TILE_B;
constexpr int HT0_OFF = XT1_OFF + 32 * TILE_B;
constexpr int HT1_OFF = HT0_OFF + 32 * HT_C;
constexpr int STAMP_OFF = HT1_OFF + 32 * HT_C;
constexpr int WS_END = STAMP_OFF + 256 * 8 * 128 * 8;
constexpr unsigned SENT = 0xFFFFFFFFu;
constexpr int AUX_SC1 = 16;
constexpr unsigned SPIN_CAP = 1u << 14;

constexpr int L_TILE = 0;
constexpr int L_KV = 0;
constexpr int L_RB = 64 * KB;
constexpr int L_Q = 80 * KB;
constexpr int L_XK1 = 88 * KB;
constexpr int L_XCH = 104 * KB;
constexpr int L_PST = 152 * KB;
constexpr int L_FST = 156 * KB;
constexpr int L_C1 = 156 * KB + 512;
constexpr int L_CQ = 158 * KB + 512;
constexpr int L_ONES = 159 * KB + 512;
constexpr int L_BYTES = 159 * KB + 576;

__device__ __forceinline__ float bf2f(unsigned b) { return __builtin_bit_cast(float, b << 16); }
__device__ __forceinline__ float bflo(unsigned d) { return __builtin_bit_cast(float, d << 16); }
__device__ __forceinline__ float bfhi(unsigned d) { return __builtin_bit_cast(float, d & 0xFFFF0000u); }
__device__ __forceinline__ unsigned pk(float lo, float hi) {
  bf2v r = {(__bf16)lo, (__bf16)hi};
  return __builtin_bit_cast(unsigned, r);
}
__device__ __forceinline__ f32x4 mfma16(v4u a, v4u b, f32x4 c) {
  return __builtin_amdgcn_mfma_f32_16x16x32_bf16(__builtin_bit_cast(bf8v, a), __builtin_bit_cast(bf8v, b), c, 0, 0, 0);
}
__device__ __forceinline__ unsigned nosent(unsigned d) { return d < 0xFFFFFFFEu ? d : 0xFFFFFFFEu; }
__device__ __forceinline__ v4u nosent4(v4u v) { return v4u{nosent(v.x), nosent(v.y), nosent(v.z), nosent(v.w)}; }
__device__ __forceinline__ v4u pack_T(f32x4 a, f32x4 b) {
  unsigned x0 = pk(a[0], a[1]), x1 = pk(a[2], a[3]), y0 = pk(b[0], b[1]), y1 = pk(b[2], b[3]);
  auto r0 = __builtin_amdgcn_permlane16_swap(x0, y0, false, false);
  auto r1 = __builtin_amdgcn_permlane16_swap(x1, y1, false, false);
  return v4u{r0[0], r1[0], r0[1], r1[1]};
}
__device__ __forceinline__ v2u pk4(f32x4 a) { return v2u{pk(a[0], a[1]), pk(a[2], a[3])}; }
__device__ __forceinline__ f32x4 unpk4(v2u v) { return f32x4{bflo(v.x), bfhi(v.x), bflo(v.y), bfhi(v.y)}; }
__device__ __forceinline__ v4u swap_T(v2u x, v2u y) {
  auto r0 = __builtin_amdgcn_permlane16_swap(x.x, y.x, false, false);
  auto r1 = __builtin_amdgcn_permlane16_swap(x.y, y.y, false, false);
  return v4u{r0[0], r1[0], r0[1], r1[1]};
}
#define KEEP(x) asm volatile("" : "+v"(x))
#define WAITV0() asm volatile("s_waitcnt vmcnt(0)" ::: "memory")
#define PRIO(x) __builtin_amdgcn_s_setprio(x)
#define SB() __builtin_amdgcn_sched_barrier(0)

__device__ __forceinline__ v4u ldw(rsrc_t rs, int voff, int sbase, int i) {
  return __builtin_amdgcn_raw_buffer_load_b128(rs, voff + (i & 3) * 1024, sbase + (i >> 2) * 4096, 0);
}
__device__ __forceinline__ v4u ld_sc1(rsrc_t rs, int voff, int soff) {
  return __builtin_amdgcn_raw_buffer_load_b128(rs, voff, soff, AUX_SC1); }
__device__ __forceinline__ v4u ld_seq(rsrc_t rs, int voff, int sbase, int i) { return ld_sc1(rs, voff + (i & 3) * 1024, sbase + (i >> 2) * 4096); }
#define RFL(x) __builtin_amdgcn_readfirstlane(x)
__device__ __forceinline__ void st_sc1(v4u v, rsrc_t rs, int voff, int soff) {
  __builtin_amdgcn_raw_buffer_store_b128(v, rs, voff + soff, 0, AUX_SC1); }
__device__ __forceinline__ unsigned max4(unsigned m, v4u d) {
  m = __builtin_elementwise_max(__builtin_elementwise_max(m, d.x), d.y);
  return __builtin_elementwise_max(__builtin_elementwise_max(m, d.z), d.w);
}
__device__ __forceinline__ bool spin_fail(unsigned& spins, GAS unsigned* tmo, unsigned code, int lane) {
  ++spins;
  if (spins > SPIN_CAP) {
    if (lane == 0) __hip_atomic_store(tmo, code, __ATOMIC_RELAXED, __HIP_MEMORY_SCOPE_AGENT);
    return true;
  }
  if ((spins & 63u) == 0u && __hip_atomic_load(tmo, __ATOMIC_RELAXED, __HIP_MEMORY_SCOPE_AGENT) != 0u) return true;
  return false;
}

struct Params {
  const u16* x;
  const u16* wqkv;
  const u16* wo;
  const u16* w1;
  const u16* b1;
  const u16* w2;
  const u16* b2;
  u16* out;
  unsigned char* ws;
};

constexpr int NFRAG = 8 * 704;
constexpr int PB_TILE = NFRAG * 16 / 256;
constexpr int PB_CS = (1536 + 1792) / 64;
constexpr int CLEAN_B = KV_B + 32 * TILE_B;
constexpr int PB_CLEAN = CLEAN_B / (256 * 64);
constexpr int PREP_BLOCKS = PB_TILE + PB_CS + PB_CLEAN;

__global__ void __launch_bounds__(256) k_prep(Params p) {
  const int b = blockIdx.x, tid = threadIdx.x;
  if (b < PB_TILE) {
    const int F = b * 16 + (tid >> 4), t16 = tid & 15, cq = t16 & 7, h = t16 >> 3;
    const u16* W; int ldw_, k0, col;
    if (F < 8 * 192) {
      const int j = F / 192, v = (F % 192) / 24, i = F % 24;
      W = p.wqkv; ldw_ = 1536;
      if (i < 16) {
        k0 = 16 * (16 * (v & 1) + i);
        col = v < 4 ? 512 + (4 * j + 2 * (v >> 1)) * 16 + 4 * (cq ^ (4 * (v & 1))) : 1024 + (4 * j + 2 * ((v - 4) >> 1)) * 16 + 4 * cq;
      } else {
        k0 = 16 * (8 * (v >> 1) + i - 16);
        col = (4 * j + 2 * (v & 1)) * 16 + 4 * (cq ^ (4 * (v >> 2)));
      }
    } else if (F < 8 * 256) {
      const int f = F - 8 * 192, j = f >> 6, u = f & 63, v = u >> 3;
      W = p.wo; ldw_ = 512; k0 = 16 * (8 * (v >> 1) + (u & 7)); col = 64 * j + 32 * (v & 1) + 4 * (cq ^ (2 * (v >> 1)));
    } else if (F < 8 * 480) {
      const int f = F - 8 * 256, j = f / 224, u = f % 224;
      W = p.w1; ldw_ = 1792; k0 = 16 * (u & 31); col = 224 * j + 32 * (u >> 5) + 4 * cq;
    } else {
      const int f = F - 8 * 480, j = f / 224, u = f % 224, v = u / 28, q = u % 28;
      W = p.w2; ldw_ = 512; k0 = 224 * v + 16 * (q >> 1); col = 64 * j + 32 * (q & 1) + 4 * cq;
    }
    const u16* src = W + (size_t)(k0 + 8 * h) * ldw_ + col;
    v2u r[8];
#pragma unroll
    for (int e = 0; e < 8; ++e) r[e] = *(const v2u*)(src + (size_t)e * ldw_);
    v4u o[4];
#pragma unroll
    for (int e = 0; e < 4; ++e) {
      const unsigned a0 = r[2 * e].x, a1 = r[2 * e + 1].x, c0 = r[2 * e].y, c1 = r[2 * e + 1].y;
      o[0][e] = (a0 & 0xFFFFu) | (a1 << 16);
      o[1][e] = (a0 >> 16) | (a1 & 0xFFFF0000u);
      o[2][e] = (c0 & 0xFFFFu) | (c1 << 16);
      o[3][e] = (c0 >> 16) | (c1 & 0xFFFF0000u);
    }
    v4u* dst = (v4u*)(p.ws + (size_t)F * 1024 + (h * 32 + 4 * cq) * 16);
#pragma unroll
    for (int i = 0; i < 4; ++i) dst[i] = o[i];
  } else if (b < PB_TILE + PB_CS) {
    __shared__ float red[16][64];
    const int cb = b - PB_TILE;
    const u16* W; int ldw_, col;
    if (cb < 24) { W = p.wqkv; ldw_ = 1536; col = cb * 64; } else { W = p.w1; ldw_ = 1792; col = (cb - 24) * 64; }
    const int cq = tid & 15, rg = tid >> 4;
    float s0 = 0, s1 = 0, s2 = 0, s3 = 0;
    const u16* src = W + (size_t)(rg * 32) * ldw_ + col + 4 * cq;
#pragma unroll
    for (int e = 0; e < 32; ++e) {
      const v2u v = *(const v2u*)(src + (size_t)e * ldw_);
      s0 += bflo(v.x); s1 += bfhi(v.x); s2 += bflo(v.y); s3 += bfhi(v.y);
    }
    red[rg][4 * cq + 0] = s0; red[rg][4 * cq + 1] = s1; red[rg][4 * cq + 2] = s2; red[rg][4 * cq + 3] = s3;
    __syncthreads();
    if (tid < 64) {
      float s = 0;
#pragma unroll
      for (int r = 0; r < 16; ++r) s += red[r][tid];
      ((float*)(p.ws + CS_OFF))[cb * 64 + tid] = s;
    }
    if (cb == 0 && tid == 0) *(unsigned*)(p.ws + CTRL_OFF) = 0u;
  } else {
    const int cb = b - PB_TILE - PB_CS;
    v4u* dst = (v4u*)(p.ws + KV0_OFF + (size_t)cb * (256 * 64)) + tid;
    const v4u s = {SENT, SENT, SENT, SENT};
#pragma unroll
    for (int i = 0; i < 4; ++i) dst[i * 256] = s;
  }
}

#define STAMP(i) do {} while (0)
#define STAMP_FLUSH() do {} while (0)

__device__ __forceinline__ float max3f(float a, float b, float c) { float r; asm("v_max3_f32 %0, %1, %2, %3" : "=v"(r) : "v"(a), "v"(b), "v"(c)); return r; }
__device__ __forceinline__ unsigned max3u(unsigned a, unsigned b, unsigned c) { unsigned r; asm("v_max3_u32 %0, %1, %2, %3" : "=v"(r) : "v"(a), "v"(b), "v"(c)); return r; }
__device__ __forceinline__ f32x16 mfma32(v4u a, v4u b, f32x16 c) {
  return __builtin_amdgcn_mfma_f32_32x32x16_bf16(__builtin_bit_cast(bf8v, a), __builtin_bit_cast(bf8v, b), c, 0, 0, 0);
}
template <int N> __device__ __forceinline__ bool any_sent(const v4u (&d)[N]) {
  unsigned m0 = 0, m1 = 0;
#pragma unroll
  for (int i = 0; i < N; ++i) { if (i & 1) m1 = max3u(m1, d[i].x, d[i].z); else m0 = max3u(m0, d[i].x, d[i].z); }
  return __any(max3u(m0, m1, 0) == SENT);
}
template <int N> __device__ __forceinline__ void issue(v4u (&d)[N], rsrc_t rs, int voff, int soff, int stride) {
#pragma unroll
  for (int i = 0; i < N; ++i) d[i] = ld_sc1(rs, voff, soff + i * stride);
}
template <int N, class F> __device__ __forceinline__ void verify(v4u (&d)[N], F load_i, GAS unsigned* tmo, unsigned code, int lane) {
  if (__builtin_expect(any_sent<N>(d), 0)) {
    unsigned spins = 0;
    do {
      __builtin_amdgcn_s_sleep(1);
      asm volatile("" ::: "memory");
#pragma unroll
      for (int i = 0; i < N; ++i)
        if (__any(max3u(d[i].x, d[i].z, 0u) == SENT)) d[i] = load_i(i);
    } while (any_sent<N>(d) && !spin_fail(spins, tmo, code, lane));
  }
}
__device__ __forceinline__ f32x2 both_halves(float v) {
  const unsigned a = __builtin_bit_cast(unsigned, v);
  auto r = __builtin_amdgcn_permlane32_swap(a, a, false, false);
  const unsigned r0 = r[0], r1 = r[1];
  return f32x2{__builtin_bit_cast(float, r0), __builtin_bit_cast(float, r1)};
}
__device__ __forceinline__ float dot2(unsigned a, unsigned b, float c) {
  return __builtin_amdgcn_fdot2_f32_bf16(__builtin_bit_cast(bf2v, a), __builtin_bit_cast(bf2v, b), c, false);
}
__device__ __forceinline__ v4u nosent2(v4u v) { return v4u{nosent(v.x), v.y, nosent(v.z), v.w}; }
__device__ __forceinline__ f32x4 round4(f32x4 s) {
  const unsigned u0 = pk(s[0], s[1]), u1 = pk(s[2], s[3]);
  return f32x4{bflo(u0), bfhi(u0), bflo(u1), bfhi(u1)};
}

__device__ __forceinline__ v4u pk8(const f32x16& a, int r0, float sc = 1.0f) {
  return v4u{pk(a[r0] * sc, a[r0 + 1] * sc), pk(a[r0 + 2] * sc, a[r0 + 3] * sc), pk(a[r0 + 4] * sc, a[r0 + 5] * sc), pk(a[r0 + 6] * sc, a[r0 + 7] * sc)};
}
__device__ __forceinline__ v4u swap_T8(v4u p) {
  auto r0 = __builtin_amdgcn_permlane32_swap(p[0], p[2], false, false);
  auto r1 = __builtin_amdgcn_permlane32_swap(p[1], p[3], false, false);
  return v4u{r0[0], r1[0], r0[1], r1[1]};
}
__device__ __forceinline__ f32x4 q4(const f32x16& a, int u) { return f32x4{a[4 * u], a[4 * u + 1], a[4 * u + 2], a[4 * u + 3]}; }
#define FRESH() int lane = lane_, c = c_, j = j_, v = v_; asm volatile("" : "+v"(lane), "+s"(c), "+s"(j), "+s"(v)); \
  const int vo = lane * 16, tk = lane & 31, hh = lane >> 5; (void)vo; (void)tk; (void)hh
#define LDS4(off) (*(const LAS v4u*)(L + (off)))
#define LDSF4(off) (*(const LAS f32x4*)(L + (off)))
constexpr int OOB = 0x7FF00000;

template <bool STATS>
__device__ __forceinline__ void fill_half(LAS unsigned char* L, int hf, int v, int lane, const v4u (&d)[4]) {
#pragma unroll
  for (int i = 0; i < 4; ++i) *(LAS v4u*)(L + L_TILE + hf * 32768 + v * 4096 + i * 1024 + lane * 16) = d[i];
  if (STATS) {
    float s0 = 0.f, s1 = 0.f, q0 = 0.f, q1 = 0.f;
#pragma unroll
    for (int i = 0; i < 4; ++i) {
      const v4u x = d[i];
      s0 = dot2(x[0], 0x3F803F80u, s0); q0 = dot2(x[0], x[0], q0);
      s1 = dot2(x[1], 0x3F803F80u, s1); q1 = dot2(x[1], x[1], q1);
      s0 = dot2(x[2], 0x3F803F80u, s0); q0 = dot2(x[2], x[2], q0);
      s1 = dot2(x[3], 0x3F803F80u, s1); q1 = dot2(x[3], x[3], q1);
    }
    const f32x2 ss = both_halves(s0 + s1), qq = both_halves(q0 + q1);
    if (lane < 32) *(LAS f32x2*)(L + L_PST + ((hf * 8 + v) * 32 + lane) * 8) = f32x2{ss.x + ss.y, qq.x + qq.y};
  }
}
template <bool DOUBLE_LN>
__device__ __forceinline__ f32x2 final_stats(LAS unsigned char* L, int lane, int hf) {
  const int t = lane & 31, hh = lane >> 5;
  f32x2 pv[4];
#pragma unroll
  for (int k = 0; k < 4; ++k) pv[k] = *(LAS f32x2*)(L + L_PST + ((hf * 8 + hh * 4 + k) * 32 + t) * 8);
  SB();
  const f32x2 s = (pv[0] + pv[1]) + (pv[2] + pv[3]);
  const f32x2 rs_ = both_halves(s.x), rq = both_halves(s.y);
  const float mean = (rs_.x + rs_.y) * (1.0f / DM);
  float var = (rq.x + rq.y) * (1.0f / DM) - mean * mean;
  var = var > 0.f ? var : 0.f;
  float r = __builtin_amdgcn_rsqf(var + 1e-5f);
  if (DOUBLE_LN) {
    const float v2 = var * r * r;
    r *= __builtin_amdgcn_rsqf(v2 + 1e-5f);
  }
  const f32x2 ab = {r, -mean * r};
  *(LAS f32x2*)(L + L_FST + (hf * 32 + t) * 8) = ab;
  return ab;
}

__global__ void __launch_bounds__(512, 2) k_main(Params p) {
  extern __shared__ __attribute__((aligned(16))) unsigned char smem[];
  LAS unsigned char* L = (LAS unsigned char*)smem;
  const int b = blockIdx.x, tid = threadIdx.x, lane_ = tid & 63, v_ = __builtin_amdgcn_readfirstlane(tid >> 6);
#define MAPPING 1
  int c_, j_;
  if (MAPPING == 0) { c_ = (b & 7) * 4 + (b >> 6); j_ = (b >> 3) & 7; }
  else { c_ = ((b & 7) >> 1) * 8 + (b >> 5); j_ = (b & 1) * 4 + ((b >> 3) & 3); }
  const rsrc_t rs = __builtin_amdgcn_make_buffer_rsrc((void*)p.ws, 0, WS_END, 0x00020000);
  const rsrc_t rx = __builtin_amdgcn_make_buffer_rsrc((void*)p.x, 0, SEQ * DM * 2, 0x00020000);
  GAS unsigned* tmo = (GAS unsigned*)(p.ws + CTRL_OFF);
  STAMP(0);
  v2u xr[2]; v4u wq[24], stA[4];
  f32x16 Z;
#pragma unroll
  for (int q = 0; q < 16; ++q) Z[q] = 0.f;
  {
    FRESH();
    const float* cs = (const float*)(p.ws + CS_OFF);
    float k0 = 0.f, k1 = 0.f;
    if (tid < 224) { k0 = cs[1536 + 224 * j + tid]; k1 = bf2f(p.b1[224 * j + tid]); }
    else if (tid >= 256) { const int t = tid - 256; k0 = t < 192 ? cs[(t >> 6) * 512 + 64 * j + (t & 63)] : bf2f(p.b2[64 * j + t - 192]); }
#pragma unroll
    for (int hf = 0; hf < 2; ++hf) xr[hf] = *(const v2u*)(p.x + (size_t)(64 * c + 32 * hf + tk) * DM + 64 * j + 32 * (v & 1) + 8 * (v >> 1) + 4 * hh);
#pragma unroll
    for (int i = 0; i < 4; ++i) stA[i] = __builtin_amdgcn_raw_buffer_load_b128(rx, tk * 1024 + hh * 16 + i * 32, (64 * c) * 1024 + v * 128, 0);
    SB();
    __builtin_amdgcn_s_barrier();
    SB();
    const int wqb = RFL(WQ_OFF + (j * 192 + v * 24) * KB);
#pragma unroll
    for (int i = 0; i < 16; ++i) wq[i] = ldw(rs, vo, wqb, i);
    SB();
    if (tid < 224) { *(LAS float*)(L + L_C1 + tid * 4) = k0; *(LAS float*)(L + L_C1 + 1024 + tid * 4) = k1; }
    else if (tid >= 256) *(LAS float*)(L + L_CQ + (tid - 256) * 4) = k0;
    else if (tid < 228) *(LAS unsigned*)(L + L_ONES + (tid - 224) * 4) = 0x3F803F80u;
    v4u SENT4 = {SENT, SENT, SENT, SENT};
    asm volatile("" : "+v"(SENT4));
#pragma unroll
    for (int hf = 0; hf < 2; ++hf) {
      if (v < 4) st_sc1(SENT4, rs, vo, KV1_OFF + ((4 * j + v) * 64 + 2 * c + hf) * 2048);
      else st_sc1(SENT4, rs, ((4 * j + 2 * ((v - 4) >> 1) + ((lane >> 4) & 1)) * 64) * 2048 + 1024 + (32 * hh + 16 * (v & 1) + (lane & 15)) * 16, KV1_OFF + (2 * c + hf) * 2048);
    }
    SB();
  }

  for (int lay = 0; lay < 2; ++lay) {
    const int KV_OFF = lay ? KV1_OFF : KV0_OFF, OT_OFF = lay ? OT1_OFF : OT0_OFF, X1T_OFF = lay ? X1T1_OFF : X1T0_OFF, HT_OFF = lay ? HT1_OFF : HT0_OFF;
    v4u wo[8], w1[32], w2[28], stB[4];
    {
      FRESH();
      const int xtb = RFL(XT1_OFF + c * TILE_B + v * 4096);
      const int wob = RFL(WO_OFF + (j * 64 + v * 8) * KB);
      const int kh = v & 1;
      f32x16 acc[2];
      auto xb_issue = [&]() {
        if (lay) {
#pragma unroll
          for (int i = 0; i < 4; ++i) stB[i] = ld_seq(rs, vo, xtb + 32768, i);
        } else {
#pragma unroll
          for (int i = 0; i < 4; ++i) stB[i] = __builtin_amdgcn_raw_buffer_load_b128(rx, tk * 1024 + hh * 16 + i * 32, (64 * c + 32) * 1024 + v * 128, 0);
        }
      };
      auto pass_k = [&](int hf) {
        v4u xs[3];
        const int x0 = L_TILE + hf * 32768 + kh * 16384 + vo;
#pragma unroll
        for (int s = 0; s < 2; ++s) xs[s] = LDS4(x0 + s * 1024);
        acc[hf] = Z;
        PRIO(1);
#pragma unroll
        for (int s = 0; s < 16; ++s) {
          if (s + 2 < 16) xs[(s + 2) % 3] = LDS4(x0 + (s + 2) * 1024);
          SB();
          acc[hf] = mfma32(wq[s], xs[s % 3], acc[hf]);
          if (hf == 0 && s == 8) xb_issue();
          SB();
        }
        KEEP(acc[hf]);
        PRIO(0);
      };
      auto pass_v = [&](int hf) {
        v4u xs[3];
        const int x0 = L_TILE + hf * 32768 + kh * 16384 + (vo ^ (kh * 256));
#pragma unroll
        for (int s = 0; s < 2; ++s) xs[s] = LDS4(x0 + s * 1024);
        acc[hf] = Z;
        PRIO(1);
#pragma unroll
        for (int s = 0; s < 16; ++s) {
          if (s + 2 < 16) xs[(s + 2) % 3] = LDS4(x0 + (s + 2) * 1024);
          SB();
          acc[hf] = mfma32(xs[s % 3], wq[s], acc[hf]);
          if (hf == 0 && s == 8) xb_issue();
          SB();
        }
        KEEP(acc[hf]);
        PRIO(0);
      };
      auto send = [&](int hf) {
#pragma unroll
        for (int u = 0; u < 2; ++u) *(LAS f32x4*)(L + (hf ? L_XK1 : L_XCH) + (v * 2 + u) * 1024 + vo) = q4(acc[hf], 2 + u);
      };
      auto epi_k = [&](int hf) {
        const f32x2 ab = final_stats<false>(L, lane, hf);
        f32x16 val;
#pragma unroll
        for (int u = 0; u < 2; ++u) {
          const f32x4 o = LDSF4((hf ? L_XK1 : L_XCH) + ((v ^ 1) * 2 + u) * 1024 + vo);
          const f32x4 csn = LDSF4(L_CQ + (64 + 16 * v + 8 * u + 4 * hh) * 4);
#pragma unroll
          for (int i = 0; i < 4; ++i) val[4 * u + i] = (acc[hf][4 * u + i] + o[i]) * ab.x + csn[i] * ab.y;
        }
        const v4u K = nosent2(pk8(val, 0));
        st_sc1(K, rs, vo, KV_OFF + ((4 * j + v) * 64 + 2 * c + hf) * 2048);
        *(LAS v4u*)(L + L_KV + ((8 + hf) * 4 + v) * 2048 + vo) = K;
      };
      auto epi_v = [&](int hf) {
        const int pp = (v - 4) >> 1;
        (void)final_stats<false>(L, lane, hf);
        const float csv = *(LAS float*)(L + L_CQ + (128 + 32 * pp + tk) * 4);
        f32x16 val;
#pragma unroll
        for (int u = 0; u < 2; ++u) {
          const f32x4 o = LDSF4((hf ? L_XK1 : L_XCH) + ((v ^ 1) * 2 + u) * 1024 + vo);
          const f32x4 t0 = LDSF4(L_FST + (hf * 32 + 16 * kh + 8 * u + 4 * hh) * 8), t1 = LDSF4(L_FST + (hf * 32 + 16 * kh + 8 * u + 4 * hh + 2) * 8);
          val[4 * u + 0] = (acc[hf][4 * u + 0] + o[0]) * t0[0] + csv * t0[1];
          val[4 * u + 1] = (acc[hf][4 * u + 1] + o[1]) * t0[2] + csv * t0[3];
          val[4 * u + 2] = (acc[hf][4 * u + 2] + o[2]) * t1[0] + csv * t1[1];
          val[4 * u + 3] = (acc[hf][4 * u + 3] + o[3]) * t1[2] + csv * t1[3];
        }
        const int hb = (lane >> 4) & 1, d = lane & 15;
        const v4u V = nosent2(pk8(val, 0));
        st_sc1(V, rs, ((4 * j + 2 * pp + hb) * 64) * 2048 + 1024 + (32 * hh + 16 * kh + d) * 16, KV_OFF + (2 * c + hf) * 2048);
        *(LAS v4u*)(L + L_KV + ((8 + hf) * 4 + 2 * pp + hb) * 2048 + 1024 + (32 * hh + 16 * kh + d) * 16) = V;
      };
      if (lay) verify<4>(stA, [&](int i) { return ld_seq(rs, vo, xtb, i); }, tmo, 0x10, lane);
      fill_half<true>(L, 0, v, lane, stA);
      {
        const int wqb = RFL(WQ_OFF + (j * 192 + v * 24) * KB);
#pragma unroll
        for (int i = 16; i < 24; ++i) wq[i] = ldw(rs, vo, wqb, i);
        SB();
      }
      __syncthreads();
      if (v < 4) pass_k(0); else pass_v(0);
      send(0);
      if (lay) verify<4>(stB, [&](int i) { return ld_seq(rs, vo, xtb + 32768, i); }, tmo, 0x11, lane);
      fill_half<true>(L, 1, v, lane, stB);
      __syncthreads();
      if (v < 4) { epi_k(0); pass_k(1); } else { pass_v(1); epi_v(0); }
      send(1);
      __syncthreads();
      if (v < 4) epi_k(1); else epi_v(1);
      {
        const int ho = (v >> 1) & 1;
        const int x0 = L_TILE + ho * 32768 + (v >> 1) * 8192 + vo, x1 = L_TILE + (1 - ho) * 32768 + (v >> 1) * 8192 + vo;
        v4u xs0[3], xs1[3];
#pragma unroll
        for (int s = 0; s < 2; ++s) { xs0[s] = LDS4(x0 + s * 1024); xs1[s] = LDS4(x1 + s * 1024); }
        acc[0] = Z; acc[1] = Z;
        PRIO(1);
#pragma unroll
        for (int s = 0; s < 8; ++s) {
          if (s + 2 < 8) { xs0[(s + 2) % 3] = LDS4(x0 + (s + 2) * 1024); xs1[(s + 2) % 3] = LDS4(x1 + (s + 2) * 1024); }
          SB();
          acc[0] = mfma32(wq[16 + s], xs0[s % 3], acc[0]); acc[1] = mfma32(wq[16 + s], xs1[s % 3], acc[1]);
          wo[s] = ldw(rs, vo, wob, s);
          SB();
        }
        KEEP(acc[0]); KEEP(acc[1]);
        PRIO(0);
#pragma unroll
        for (int u = 0; u < 2; ++u) {
          *(LAS f32x4*)(L + L_XCH + (((v ^ 4) * 3 + 0) * 2 + u) * 1024 + vo) = q4(acc[0], 2 + u);
          *(LAS f32x4*)(L + L_XCH + (((v ^ 2) * 3 + 1) * 2 + u) * 1024 + vo) = q4(acc[1], u);
          *(LAS f32x4*)(L + L_XCH + (((v ^ 6) * 3 + 2) * 2 + u) * 1024 + vo) = q4(acc[1], 2 + u);
        }
      }
      v4u kvn[8];
      auto kv_load = [&](int i) {
        const int kt = 2 * c - 8 + 4 * (v >> 2) + (i >> 1);
        return ld_sc1(rs, vo + (i & 1) * 1024, RFL(kt >= 0 ? KV_OFF + ((4 * j + (v & 3)) * 64 + kt) * 2048 : OOB));
      };
      auto kv_issue = [&]() {
#pragma unroll
        for (int i = 0; i < 8; ++i) kvn[i] = kv_load(i);
      };
      WAITV0();
      __syncthreads();
      STAMP(16 + lay * 24 + 6);
      kv_issue();
      {
        const int ho = (v >> 1) & 1, hl = 2 * (v & 1) + (v >> 2);
        const f32x2 ab = *(LAS f32x2*)(L + L_FST + (ho * 32 + tk) * 8);
        const float cexp = 0.25f * 1.4426950408889634f;
        f32x16 val;
#pragma unroll
        for (int u = 0; u < 2; ++u) {
          f32x4 o = q4(acc[0], u);
#pragma unroll
          for (int k = 0; k < 3; ++k) o += LDSF4(L_XCH + ((v * 3 + k) * 2 + u) * 1024 + vo);
          const f32x4 csn = LDSF4(L_CQ + (16 * hl + 8 * u + 4 * hh) * 4);
#pragma unroll
          for (int i = 0; i < 4; ++i) val[4 * u + i] = o[i] * (ab.x * cexp) + csn[i] * (ab.y * cexp);
        }
        *(LAS v4u*)(L + L_Q + (hl * 2 + ho) * 1024 + vo) = pk8(val, 0);
      }
      const int w1b = RFL(v < 7 ? W1_OFF + (j * 224 + v * 32) * KB : OOB);
#pragma unroll
      for (int i = 0; i < 4; ++i) w1[i] = ldw(rs, vo, w1b, i);
      SB();
      verify<8>(kvn, kv_load, tmo, 0x20, lane);
      STAMP(16 + lay * 24 + 7);
      {
        v4u SENT4 = {SENT, SENT, SENT, SENT};
        asm volatile("" : "+v"(SENT4));
        const int tsl = (8 * j + 4 * (v & 1) + (v >> 1)) * 512 + tk * 16 + hh * 8;
#pragma unroll
        for (int hf = 0; hf < 2; ++hf) {
          __builtin_amdgcn_raw_buffer_store_b64(v2u{SENT4.x, SENT4.y}, rs, tsl + X1T_OFF + c * TILE_B + hf * 32768, 0, AUX_SC1);
          if (lay == 0) __builtin_amdgcn_raw_buffer_store_b64(v2u{SENT4.x, SENT4.y}, rs, tsl + XT1_OFF + c * TILE_B + hf * 32768, 0, AUX_SC1);
        }
        const int hb = RFL(v < 7 ? HT_OFF + c * HT_C + (28 * j + 4 * v) * 512 : OOB);
#pragma unroll
        for (int u = 0; u < 4; ++u) st_sc1(SENT4, rs, vo, hb + (u >> 1) * 114688 + 2 * (u & 1) * 512);
        if (lay == 0) st_sc1(SENT4, rs, vo, OT1_OFF + c * TILE_B + (v >> 2) * 32768 + (2 * (4 * j + (v & 3))) * 512);
      }
      SB();
#pragma unroll
      for (int i = 0; i < 4; ++i) {
        *(LAS v4u*)(L + L_KV + ((4 * (v >> 2) + i) * 4 + (v & 3)) * 2048 + vo) = kvn[2 * i];
        *(LAS v4u*)(L + L_KV + ((4 * (v >> 2) + i) * 4 + (v & 3)) * 2048 + 1024 + vo) = kvn[2 * i + 1];
      }
      __syncthreads();
    }
    STAMP(1 + lay * 8);
    {
      FRESH();
      const int hq = v & 3, r = v >> 2, tq = tk - 4 * hh;
      const int kb = L_KV + (r * 4 + hq) * 2048;
      const v4u Q = LDS4(L_Q + (hq * 2 + r) * 1024 + vo);
      v4u Kf[9];
#pragma unroll
      for (int i = 0; i < 9; ++i) Kf[i] = LDS4(kb + i * 8192 + vo);
      const bool ones_lane = (lane & 16) != 0;
      const unsigned vsel = ones_lane ? 0u : 1u, vbase = ones_lane ? (unsigned)L_ONES : (unsigned)(kb + 1024 + (32 * hh + (lane & 15)) * 16);
      auto vop = [&](int i, int s) -> v4u { return LDS4(vbase + vsel * (unsigned)(i * 8192 + s * 256)); };
      const int first_ok = 8 - r - 2 * c;
      const int w1b = RFL(v < 7 ? W1_OFF + (j * 224 + v * 32) * KB : OOB);
      auto tmax = [&](const f32x16& x, float m) {
        const float a = max3f(x[0], x[1], x[2]), b2_ = max3f(x[3], x[4], x[5]), c2 = max3f(x[6], x[7], x[8]), d = max3f(x[9], x[10], x[11]), e = max3f(x[12], x[13], x[14]);
        return max3f(max3f(a, b2_, x[15]), max3f(c2, d, e), m);
      };
      f32x16 Sm, S[2];
      {
        f32x16 Sa = mfma32(Kf[0], Q, Z);
        const f32x16 Sb = mfma32(Kf[8], Q, Z);
        S[1] = mfma32(Kf[1], Q, Z);
        if (first_ok > 0) {
#pragma unroll
          for (int q = 0; q < 16; ++q) Sa[q] = -1e30f;
        }
#pragma unroll
        for (int q = 0; q < 16; ++q) { const int row = (q & 3) + 8 * (q >> 2); Sm[q] = row <= tq ? Sb[q] : Sa[q]; }
      }
      float mx = tmax(Sm, -1e30f);
      SB();
#pragma unroll
      for (int i = 1; i < 8; ++i) {
        if (i + 1 < 8) S[(i + 1) & 1] = mfma32(Kf[i + 1], Q, Z);
        if (i >= first_ok) mx = tmax(S[i & 1], mx);
        if (i < 6) w1[4 + (i - 1)] = ldw(rs, vo, w1b, 4 + (i - 1));
        SB();
      }
      { const f32x2 rr = both_halves(mx); mx = max3f(rr.x, rr.y, rr.y); }
      f32x16 Cm;
#pragma unroll
      for (int q = 0; q < 16; ++q) Cm[q] = -mx;
      S[1] = mfma32(Kf[1], Q, Cm);
      f32x16 O;
      {
        const v4u vn0 = vop(8, 0), vn1 = vop(8, 1), vo0 = vop(0, 0), vo1 = vop(0, 1);
        unsigned pp[8];
#pragma unroll
        for (int q = 0; q < 8; ++q) pp[q] = pk(__builtin_amdgcn_exp2f(Sm[2 * q] - mx), __builtin_amdgcn_exp2f(Sm[2 * q + 1] - mx));
        v4u pn[2], po[2];
#pragma unroll
        for (int q = 0; q < 8; ++q) {
          const int row0 = ((2 * q) & 3) + 8 * ((2 * q) >> 2);
          const unsigned mk = (row0 <= tq ? 0xFFFFu : 0u) | (row0 + 1 <= tq ? 0xFFFF0000u : 0u);
          pn[q >> 2][q & 3] = pp[q] & mk; po[q >> 2][q & 3] = pp[q] & ~mk;
        }
        O = mfma32(vn0, pn[0], Z); O = mfma32(vn1, pn[1], O);
        O = mfma32(vo0, po[0], O); O = mfma32(vo1, po[1], O);
        SB();
      }
#pragma unroll
      for (int i = 1; i < 8; ++i) {
        if (i + 1 < 8) S[(i + 1) & 1] = mfma32(Kf[i + 1], Q, Cm);
        const v4u va0 = vop(i, 0), va1 = vop(i, 1);
        if (i >= first_ok) {
          const f32x16& cur = S[i & 1];
          v4u pp[2];
#pragma unroll
          for (int q = 0; q < 8; ++q) pp[q >> 2][q & 3] = pk(__builtin_amdgcn_exp2f(cur[2 * q]), __builtin_amdgcn_exp2f(cur[2 * q + 1]));
          O = mfma32(va0, pp[0], O); O = mfma32(va1, pp[1], O);
        }
        w1[9 + (i - 1)] = ldw(rs, vo, w1b, 9 + (i - 1));
        SB();
      }
      KEEP(O);
      const float rl = __builtin_amdgcn_rcpf(O[8]);
      asm volatile("s_waitcnt vmcnt(12)" ::: "memory");
      st_sc1(nosent2(swap_T8(pk8(O, 0, rl))), rs, vo, OT_OFF + c * TILE_B + r * 32768 + (2 * (4 * j + hq)) * 512);
      if (v < 4) WAITV0();
    }
    STAMP(2 + lay * 8);
    {
      FRESH();
      const int otb = RFL(OT_OFF + c * TILE_B + v * 4096);
      const int w1b = RFL(v < 7 ? W1_OFF + (j * 224 + v * 32) * KB : OOB);
      auto oa_issue = [&]() {
#pragma unroll
        for (int i = 0; i < 4; ++i) stA[i] = ld_seq(rs, vo, otb, i);
      };
      auto ob_issue = [&]() {
#pragma unroll
        for (int i = 0; i < 4; ++i) stB[i] = ld_seq(rs, vo, otb + 32768, i);
      };
      f32x16 a;
      auto pass = [&](int hf) {
        const int x0 = L_TILE + hf * 32768 + (v >> 1) * 8192 + vo;
        v4u xs[8];
#pragma unroll
        for (int s = 0; s < 8; ++s) xs[s] = LDS4(x0 + s * 1024);
        a = Z;
#pragma unroll
        for (int s = 0; s < 8; ++s) a = mfma32(wo[s], xs[s], a);
        KEEP(a);
#pragma unroll
        for (int u = 1; u < 4; ++u) *(LAS f32x4*)(L + L_XCH + ((hf * 8 + (v ^ (2 * u))) * 3 + (u - 1)) * 1024 + vo) = q4(a, u);
      };
      auto epi = [&](int hf) {
        f32x4 o = q4(a, 0);
#pragma unroll
        for (int k = 0; k < 3; ++k) o += LDSF4(L_XCH + ((hf * 8 + v) * 3 + k) * 1024 + vo);
        xr[hf] = pk4(unpk4(xr[hf]) + o);
        __builtin_amdgcn_raw_buffer_store_b64(v2u{nosent(xr[hf].x), xr[hf].y}, rs, (8 * j + 4 * (v & 1) + (v >> 1)) * 512 + tk * 16 + hh * 8 + X1T_OFF + c * TILE_B + hf * 32768, 0, AUX_SC1);
      };
      oa_issue();
#pragma unroll
      for (int i = 16; i < 24; ++i) w1[i] = ldw(rs, vo, w1b, i);
      SB();
      verify<4>(stA, [&](int i) { return ld_seq(rs, vo, otb, i); }, tmo, 0x30, lane);
      STAMP(16 + lay * 24 + 8);
      __syncthreads();
      fill_half<false>(L, 0, v, lane, stA);
      ob_issue();
      __syncthreads();
      STAMP(16 + lay * 24 + 9);
      pass(0);
      STAMP(16 + lay * 24 + 21);
      verify<4>(stB, [&](int i) { return ld_seq(rs, vo, otb + 32768, i); }, tmo, 0x31, lane);
      fill_half<false>(L, 1, v, lane, stB);
      __syncthreads();
      epi(0);
      STAMP(16 + lay * 24 + 23);
      pass(1);
#pragma unroll
      for (int i = 24; i < 32; ++i) w1[i] = ldw(rs, vo, w1b, i);
      __syncthreads();
#pragma unroll
      for (int i = 0; i < 4; ++i) stA[i] = ld_seq(rs, vo, RFL(X1T_OFF + c * TILE_B + v * 4096), i);
      SB();
      epi(1);
    }
    STAMP(3 + lay * 8);
    v4u hA[14];
    {
      FRESH();
      const int x1b = RFL(X1T_OFF + c * TILE_B + v * 4096);
      const int w2b = RFL(W2_OFF + (j * 224 + v * 28) * KB);
      const int hb = RFL(v < 7 ? HT_OFF + c * HT_C + (28 * j + 4 * v) * 512 : OOB);
      auto xb_issue = [&]() {
#pragma unroll
        for (int i = 0; i < 4; ++i) stB[i] = ld_seq(rs, vo, x1b + 32768, i);
      };
      f32x16 a;
      auto pass = [&](int hf) {
        v4u xs[3];
        const int x0 = L_TILE + hf * 32768 + vo;
#pragma unroll
        for (int s = 0; s < 2; ++s) xs[s] = LDS4(x0 + s * 1024);
        a = Z;
        PRIO(1);
#pragma unroll
        for (int s = 0; s < 32; ++s) {
          if (s + 2 < 32) xs[(s + 2) % 3] = LDS4(x0 + (s + 2) * 1024);
          SB();
          a = mfma32(w1[s], xs[s % 3], a);
          if (hf == 0 && s == 8) xb_issue();
          if (hf == 0 && s >= 22) w2[s - 22] = ldw(rs, vo, w2b, s - 22);
          if (hf == 1 && s >= 2 && s < 20) w2[10 + (s - 2)] = ldw(rs, vo, w2b, 10 + (s - 2));
          SB();
        }
        KEEP(a);
        PRIO(0);
      };
      v4u ho[2];
      auto gelu = [&](int hf) {
        const f32x2 ab = final_stats<true>(L, lane, hf);
        const float c1 = -2.0f * 0.7978845608028654f * 1.4426950408889634f, c2 = c1 * 0.044715f;
#pragma unroll
        for (int u2 = 0; u2 < 2; ++u2) {
          f32x16 hv;
#pragma unroll
          for (int u = 2 * u2; u < 2 * u2 + 2; ++u) {
            const f32x4 csn = LDSF4(L_C1 + (32 * v + 8 * u + 4 * hh) * 4), bn = LDSF4(L_C1 + 1024 + (32 * v + 8 * u + 4 * hh) * 4);
#pragma unroll
            for (int i = 0; i < 4; ++i) {
              const float z = a[4 * u + i] * ab.x + (csn[i] * ab.y + bn[i]);
              const float e = __builtin_amdgcn_exp2f(z * (z * z * c2 + c1));
              hv[4 * u + i] = z * __builtin_amdgcn_rcpf(1.0f + e);
            }
          }
          ho[u2] = nosent2(swap_T8(pk8(hv, 8 * u2)));
          if (hf == 0) st_sc1(ho[u2], rs, vo, hb + 2 * u2 * 512);
        }
      };
      verify<4>(stA, [&](int i) { return ld_seq(rs, vo, x1b, i); }, tmo, 0x40, lane);
      STAMP(16 + lay * 24 + 10);
      fill_half<true>(L, 0, v, lane, stA);
      __syncthreads();
      STAMP(16 + lay * 24 + 11);
      pass(0);
      STAMP(16 + lay * 24 + 12);
      gelu(0);
      STAMP(16 + lay * 24 + 13);
      verify<4>(stB, [&](int i) { return ld_seq(rs, vo, x1b + 32768, i); }, tmo, 0x41, lane);
      fill_half<true>(L, 1, v, lane, stB);
      __syncthreads();
      STAMP(16 + lay * 24 + 14);
      pass(1);
      STAMP(16 + lay * 24 + 15);
#pragma unroll
      for (int s = 0; s < 14; ++s) hA[s] = ld_seq(rs, vo, RFL(HT_OFF + c * HT_C + 28 * v * 512), s);
      SB();
      gelu(1);
#pragma unroll
      for (int u2 = 0; u2 < 2; ++u2) st_sc1(ho[u2], rs, vo, hb + 114688 + 2 * u2 * 512);
    }
    STAMP(4 + lay * 8);
    {
      FRESH();
      const int htb = RFL(HT_OFF + c * HT_C + 28 * v * 512);
      const int wqb = RFL(lay == 0 ? WQ_OFF + (j * 192 + v * 24) * KB : OOB);
      const int xnb = RFL(lay == 0 ? XT1_OFF + c * TILE_B + v * 4096 : OOB);
#pragma unroll
      for (int hf = 0; hf < 2; ++hf) {
        verify<14>(hA, [&](int i) { return ld_seq(rs, vo, htb + hf * 114688, i); }, tmo, 0x50 + hf, lane);
        if (hf == 0) __syncthreads();
        STAMP(16 + lay * 24 + 17 + 2 * hf);
        f32x16 ac[2] = {Z, Z};
        PRIO(1);
#pragma unroll
        for (int s = 0; s < 14; ++s) {
          ac[0] = mfma32(w2[2 * s], hA[s], ac[0]);
          ac[1] = mfma32(w2[2 * s + 1], hA[s], ac[1]);
          if (hf == 1) wq[s] = ldw(rs, vo, wqb, s);
          else hA[s] = ld_seq(rs, vo, htb + 114688, s);
          SB();
        }
        KEEP(ac[0]); KEEP(ac[1]);
        PRIO(0);
        STAMP(16 + lay * 24 + 18 + 2 * hf);
        LAS unsigned char* R = L + (hf ? L_TILE : L_RB);
#pragma unroll
        for (int nt = 0; nt < 2; ++nt)
#pragma unroll
          for (int u = 0; u < 4; ++u) *(LAS f32x4*)(R + ((nt + 2 * u) * 8 + v) * 1024 + vo) = q4(ac[nt], u);
        if (hf == 1) { wq[14] = ldw(rs, vo, wqb, 14); wq[15] = ldw(rs, vo, wqb, 15); }
        __syncthreads();
        f32x4 s0 = {0, 0, 0, 0};
#pragma unroll
        for (int k = 0; k < 8; ++k) s0 += *(LAS f32x4*)(R + (v * 8 + k) * 1024 + vo);
        const f32x4 b0 = LDSF4(L_CQ + (192 + 32 * (v & 1) + 8 * (v >> 1) + 4 * hh) * 4);
        xr[hf] = pk4(unpk4(xr[hf]) + s0 + b0);
        if (hf == 1) {
#pragma unroll
          for (int i = 0; i < 4; ++i) stA[i] = ld_seq(rs, vo, xnb, i);
          SB();
        }
        if (lay == 0) {
          __builtin_amdgcn_raw_buffer_store_b64(v2u{nosent(xr[hf].x), xr[hf].y}, rs, (8 * j + 4 * (v & 1) + (v >> 1)) * 512 + tk * 16 + hh * 8 + XT1_OFF + c * TILE_B + hf * 32768, 0, AUX_SC1);
        } else {
          *(v2u*)(p.out + (size_t)(64 * c + 32 * hf + tk) * DM + 64 * j + 32 * (v & 1) + 8 * (v >> 1) + 4 * hh) = xr[hf];
        }
      }
      __syncthreads();
    }
    STAMP(5 + lay * 8);
  }
  STAMP_FLUSH();
}
}

extern "C" void kernel_launch(void* const* d_in, const int* in_sizes, int n_in, void* d_out, int out_size, void* d_ws, size_t ws_size,
                              hipStream_t stream) {
  using namespace fk;
  Params p;
  p.x = (const u16*)d_in[0]; p.wqkv = (const u16*)d_in[1]; p.wo = (const u16*)d_in[2];
  p.w1 = (const u16*)d_in[4]; p.b1 = (const u16*)d_in[5]; p.w2 = (const u16*)d_in[6]; p.b2 = (const u16*)d_in[7];
  p.out = (u16*)d_out; p.ws = (unsigned char*)d_ws;
  (void)hipFuncSetAttribute((const void*)k_main, hipFuncAttributeMaxDynamicSharedMemorySize, L_BYTES);
  k_prep<<<PREP_BLOCKS, 256, 0, stream>>>(p);
  k_main<<<256, 512, L_BYTES, stream>>>(p);
}
```

```cpp
#include <hip/hip_runtime.h>
#include <stdint.h>

namespace fk {
typedef unsigned short u16;
typedef unsigned v4u __attribute__((ext_vector_type(4)));
typedef unsigned v2u __attribute__((ext_vector_type(2)));
typedef float f32x4 __attribute__((ext_vector_type(4)));
typedef float f32x2 __attribute__((ext_vector_type(2)));
typedef float f32x16 __attribute__((ext_vector_type(16)));
typedef __bf16 bf8v __attribute__((ext_vector_type(8)));
typedef __bf16 bf2v __attribute__((ext_vector_type(2)));
#define LAS __attribute__((address_space(3)))
#define GAS __attribute__((address_space(1)))
typedef __amdgpu_buffer_rsrc_t rsrc_t;

constexpr int SEQ = 2048, DM = 512, FF = 1792;
constexpr int KB = 1024;
constexpr int WQ_OFF = 0;
constexpr int WO_OFF = WQ_OFF + 8 * 192 * KB;
constexpr int W1_OFF = WO_OFF + 8 * 64 * KB;
constexpr int W2_OFF = W1_OFF + 8 * 224 * KB;
constexpr int CS_OFF = W2_OFF + 8 * 224 * KB;
constexpr int CTRL_OFF = CS_OFF + 16 * KB;
constexpr int TILE_B = 64 * KB;
constexpr int KV_B = 32 * 64 * 2 * KB;
constexpr int HT_C = 2 * 224 * 512;
constexpr int KV0_OFF = CTRL_OFF + 16 * KB;
constexpr int OT0_OFF = KV0_OFF + KV_B;
constexpr int KV1_OFF = OT0_OFF + 32 * TILE_B;
constexpr int OT1_OFF = KV1_OFF + KV_B;
constexpr int X1T0_OFF = OT1_OFF + 32 * TILE_B;
constexpr int X1T1_OFF = X1T0_OFF + 32 * TILE_B;
constexpr int XT1_OFF = X1T1_OFF + 32 * TILE_B;
constexpr int HT0_OFF = XT1_OFF + 32 * TILE_B;
constexpr int HT1_OFF = HT0_OFF + 32 * HT_C;
constexpr int STAMP_OFF = HT1_OFF + 32 * HT_C;
constexpr int WS_END = STAMP_OFF + 256 * 8 * 128 * 8;
constexpr unsigned SENT = 0xFFFFFFFFu;
constexpr int AUX_SC1 = 16;
constexpr unsigned SPIN_CAP = 1u << 14;

constexpr int L_TILE = 0;
constexpr int L_KV = 0;
constexpr int L_RB = 64 * KB;
constexpr int L_Q = 80 * KB;
constexpr int L_XK1 = 88 * KB;
constexpr int L_XCH = 104 * KB;
constexpr int L_PST = 152 * KB;
constexpr int L_FST = 156 * KB;
constexpr int L_C1 = 156 * KB + 512;
constexpr int L_CQ = 158 * KB + 512;
constexpr int L_ONES = 159 * KB + 512;
constexpr int L_BYTES = 159 * KB + 576;

__device__ __forceinline__ float bf2f(unsigned b) { return __builtin_bit_cast(float, b << 16); }
__device__ __forceinline__ float bflo(unsigned d) { return __builtin_bit_cast(float, d << 16); }
__device__ __forceinline__ float bfhi(unsigned d) { return __builtin_bit_cast(float, d & 0xFFFF0000u); }
__device__ __forceinline__ unsigned pk(float lo, float hi) {
  bf2v r = {(__bf16)lo, (__bf16)hi};
  return __builtin_bit_cast(unsigned, r);
}
__device__ __forceinline__ f32x4 mfma16(v4u a, v4u b, f32x4 c) {
  return __builtin_amdgcn_mfma_f32_16x16x32_bf16(__builtin_bit_cast(bf8v, a), __builtin_bit_cast(bf8v, b), c, 0, 0, 0);
}
__device__ __forceinline__ unsigned nosent(unsigned d) { return d < 0xFFFFFFFEu ? d : 0xFFFFFFFEu; }
__device__ __forceinline__ v4u nosent4(v4u v) { return v4u{nosent(v.x), nosent(v.y), nosent(v.z), nosent(v.w)}; }
__device__ __forceinline__ v4u pack_T(f32x4 a, f32x4 b) {
  unsigned x0 = pk(a[0], a[1]), x1 = pk(a[2], a[3]), y0 = pk(b[0], b[1]), y1 = pk(b[2], b[3]);
  auto r0 = __builtin_amdgcn_permlane16_swap(x0, y0, false, false);
  auto r1 = __builtin_amdgcn_permlane16_swap(x1, y1, false, false);
  return v4u{r0[0], r1[0], r0[1], r1[1]};
}
__device__ __forceinline__ v2u pk4(f32x4 a) { return v2u{pk(a[0], a[1]), pk(a[2], a[3])}; }
__device__ __forceinline__ f32x4 unpk4(v2u v) { return f32x4{bflo(v.x), bfhi(v.x), bflo(v.y), bfhi(v.y)}; }
__device__ __forceinline__ v4u swap_T(v2u x, v2u y) {
  auto r0 = __builtin_amdgcn_permlane16_swap(x.x, y.x, false, false);
  auto r1 = __builtin_amdgcn_permlane16_swap(x.y, y.y, false, false);
  return v4u{r0[0], r1[0], r0[1], r1[1]};
}
#define KEEP(x) asm volatile("" : "+v"(x))
#define WAITV0() asm volatile("s_waitcnt vmcnt(0)" ::: "memory")
#define PRIO(x) __builtin_amdgcn_s_setprio(x)
#define SB() __builtin_amdgcn_sched_barrier(0)

__device__ __forceinline__ v4u ldw(rsrc_t rs, int voff, int sbase, int i) {
  return __builtin_amdgcn_raw_buffer_load_b128(rs, voff + (i & 3) * 1024, sbase + (i >> 2) * 4096, 0);
}
__device__ __forceinline__ v4u ld_sc1(rsrc_t rs, int voff, int soff) {
  return __builtin_amdgcn_raw_buffer_load_b128(rs, voff, soff, AUX_SC1); }
__device__ __forceinline__ v4u ld_seq(rsrc_t rs, int voff, int sbase, int i) { return ld_sc1(rs, voff + (i & 3) * 1024, sbase + (i >> 2) * 4096); }
#define RFL(x) __builtin_amdgcn_readfirstlane(x)
__device__ __forceinline__ void st_sc1(v4u v, rsrc_t rs, int voff, int soff) {
  __builtin_amdgcn_raw_buffer_store_b128(v, rs, voff + soff, 0, AUX_SC1); }
__device__ __forceinline__ unsigned max4(unsigned m, v4u d) {
  m = __builtin_elementwise_max(__builtin_elementwise_max(m, d.x), d.y);
  return __builtin_elementwise_max(__builtin_elementwise_max(m, d.z), d.w);
}
__device__ __forceinline__ bool spin_fail(unsigned& spins, GAS unsigned* tmo, unsigned code, int lane) {
  ++spins;
  if (spins > SPIN_CAP) {
    if (lane == 0) __hip_atomic_store(tmo, code, __ATOMIC_RELAXED, __HIP_MEMORY_SCOPE_AGENT);
    return true;
  }
  if ((spins & 63u) == 0u && __hip_atomic_load(tmo, __ATOMIC_RELAXED, __HIP_MEMORY_SCOPE_AGENT) != 0u) return true;
  return false;
}

struct Params {
  const u16* x;
  const u16* wqkv;
  const u16* wo;
  const u16* w1;
  const u16* b1;
  const u16* w2;
  const u16* b2;
  u16* out;
  unsigned char* ws;
};

constexpr int NFRAG = 8 * 704;
constexpr int PB_TILE = NFRAG * 16 / 256;
constexpr int PB_CS = (1536 + 1792) / 64;
constexpr int CLEAN_B = KV_B + 32 * TILE_B;
constexpr int PB_CLEAN = CLEAN_B / (256 * 64);
constexpr int PREP_BLOCKS = PB_TILE + PB_CS + PB_CLEAN;

__global__ void __launch_bounds__(256) k_prep(Params p) {
  const int b = blockIdx.x, tid = threadIdx.x;
  if (b < PB_TILE) {
    const int F = b * 16 + (tid >> 4), t16 = tid & 15, cq = t16 & 7, h = t16 >> 3;
    const u16* W; int ldw_, k0, col;
    if (F < 8 * 192) {
      const int j = F / 192, v = (F % 192) / 24, i = F % 24;
      W = p.wqkv; ldw_ = 1536;
      if (i < 16) {
        k0 = 16 * (16 * (v & 1) + i);
        col = v < 4 ? 512 + (4 * j + 2 * (v >> 1)) * 16 + 4 * (cq ^ (4 * (v & 1))) : 1024 + (4 * j + 2 * ((v - 4) >> 1)) * 16 + 4 * cq;
      } else {
        k0 = 16 * (8 * (v >> 1) + i - 16);
        col = (4 * j + 2 * (v & 1)) * 16 + 4 * (cq ^ (4 * (v >> 2)));
      }
    } else if (F < 8 * 256) {
      const int f = F - 8 * 192, j = f >> 6, u = f & 63, v = u >> 3;
      W = p.wo; ldw_ = 512; k0 = 16 * (8 * (v >> 1) + (u & 7)); col = 64 * j + 32 * (v & 1) + 4 * (cq ^ (2 * (v >> 1)));
    } else if (F < 8 * 480) {
      const int f = F - 8 * 256, j = f / 224, u = f % 224;
      W = p.w1; ldw_ = 1792; k0 = 16 * (u & 31); col = 224 * j + 32 * (u >> 5) + 4 * cq;
    } else {
      const int f = F - 8 * 480, j = f / 224, u = f % 224, v = u / 28, q = u % 28;
      W = p.w2; ldw_ = 512; k0 = 224 * v + 16 * (q >> 1); col = 64 * j + 32 * (q & 1) + 4 * cq;
    }
    const u16* src = W + (size_t)(k0 + 8 * h) * ldw_ + col;
    v2u r[8];
#pragma unroll
    for (int e = 0; e < 8; ++e) r[e] = *(const v2u*)(src + (size_t)e * ldw_);
    v4u o[4];
#pragma unroll
    for (int e = 0; e < 4; ++e) {
      const unsigned a0 = r[2 * e].x, a1 = r[2 * e + 1].x, c0 = r[2 * e].y, c1 = r[2 * e + 1].y;
      o[0][e] = (a0 & 0xFFFFu) | (a1 << 16);
      o[1][e] = (a0 >> 16) | (a1 & 0xFFFF0000u);
      o[2][e] = (c0 & 0xFFFFu) | (c1 << 16);
      o[3][e] = (c0 >> 16) | (c1 & 0xFFFF0000u);
    }
    v4u* dst = (v4u*)(p.ws + (size_t)F * 1024 + (h * 32 + 4 * cq) * 16);
#pragma unroll
    for (int i = 0; i < 4; ++i) dst[i] = o[i];
  } else if (b < PB_TILE + PB_CS) {
    __shared__ float red[16][64];
    const int cb = b - PB_TILE;
    const u16* W; int ldw_, col;
    if (cb < 24) { W = p.wqkv; ldw_ = 1536; col = cb * 64; } else { W = p.w1; ldw_ = 1792; col = (cb - 24) * 64; }
    const int cq = tid & 15, rg = tid >> 4;
    float s0 = 0, s1 = 0, s2 = 0, s3 = 0;
    const u16* src = W + (size_t)(rg * 32) * ldw_ + col + 4 * cq;
#pragma unroll
    for (int e = 0; e < 32; ++e) {
      const v2u v = *(const v2u*)(src + (size_t)e * ldw_);
      s0 += bflo(v.x); s1 += bfhi(v.x); s2 += bflo(v.y); s3 += bfhi(v.y);
    }
    red[rg][4 * cq + 0] = s0; red[rg][4 * cq + 1] = s1; red[rg][4 * cq + 2] = s2; red[rg][4 * cq + 3] = s3;
    __syncthreads();
    if (tid < 64) {
      float s = 0;
#pragma unroll
      for (int r = 0; r < 16; ++r) s += red[r][tid];
      ((float*)(p.ws + CS_OFF))[cb * 64 + tid] = s;
    }
    if (cb == 0 && tid == 0) *(unsigned*)(p.ws + CTRL_OFF) = 0u;
  } else {
    const int cb = b - PB_TILE - PB_CS;
    v4u* dst = (v4u*)(p.ws + KV0_OFF + (size_t)cb * (256 * 64)) + tid;
    const v4u s = {SENT, SENT, SENT, SENT};
#pragma unroll
    for (int i = 0; i < 4; ++i) dst[i * 256] = s;
  }
}

#define STAMP(i) do {} while (0)
#define STAMP_FLUSH() do {} while (0)

__device__ __forceinline__ float max3f(float a, float b, float c) { float r; asm("v_max3_f32 %0, %1, %2, %3" : "=v"(r) : "v"(a), "v"(b), "v"(c)); return r; }
__device__ __forceinline__ unsigned max3u(unsigned a, unsigned b, unsigned c) { unsigned r; asm("v_max3_u32 %0, %1, %2, %3" : "=v"(r) : "v"(a), "v"(b), "v"(c)); return r; }
__device__ __forceinline__ f32x16 mfma32(v4u a, v4u b, f32x16 c) {
  return __builtin_amdgcn_mfma_f32_32x32x16_bf16(__builtin_bit_cast(bf8v, a), __builtin_bit_cast(bf8v, b), c, 0, 0, 0);
}
template <int N> __device__ __forceinline__ bool any_sent(const v4u (&d)[N]) {
  unsigned m0 = 0, m1 = 0;
#pragma unroll
  for (int i = 0; i < N; ++i) { if (i & 1) m1 = max3u(m1, d[i].x, d[i].z); else m0 = max3u(m0, d[i].x, d[i].z); }
  return __any(max3u(m0, m1, 0) == SENT);
}
template <int N> __device__ __forceinline__ void issue(v4u (&d)[N], rsrc_t rs, int voff, int soff, int stride) {
#pragma unroll
  for (int i = 0; i < N; ++i) d[i] = ld_sc1(rs, voff, soff + i * stride);
}
template <int N, class F> __device__ __forceinline__ void verify(v4u (&d)[N], F load_i, GAS unsigned* tmo, unsigned code, int lane) {
  if (__builtin_expect(any_sent<N>(d), 0)) {
    unsigned spins = 0;
    do {
      __builtin_amdgcn_s_sleep(1);
      asm volatile("" ::: "memory");
#pragma unroll
      for (int i = 0; i < N; ++i)
        if (__any(max3u(d[i].x, d[i].z, 0u) == SENT)) d[i] = load_i(i);
    } while (any_sent<N>(d) && !spin_fail(spins, tmo, code, lane));
  }
}
__device__ __forceinline__ f32x2 both_halves(float v) {
  const unsigned a = __builtin_bit_cast(unsigned, v);
  auto r = __builtin_amdgcn_permlane32_swap(a, a, false, false);
  const unsigned r0 = r[0], r1 = r[1];
  return f32x2{__builtin_bit_cast(float, r0), __builtin_bit_cast(float, r1)};
}
__device__ __forceinline__ float dot2(unsigned a, unsigned b, float c) {
  return __builtin_amdgcn_fdot2_f32_bf16(__builtin_bit_cast(bf2v, a), __builtin_bit_cast(bf2v, b), c, false);
}
__device__ __forceinline__ v4u nosent2(v4u v) { return v4u{nosent(v.x), v.y, nosent(v.z), v.w}; }
__device__ __forceinline__ f32x4 round4(f32x4 s) {
  const unsigned u0 = pk(s[0], s[1]), u1 = pk(s[2], s[3]);
  return f32x4{bflo(u0), bfhi(u0), bflo(u1), bfhi(u1)};
}

__device__ __forceinline__ v4u pk8(const f32x16& a, int r0, float sc = 1.0f) {
  return v4u{pk(a[r0] * sc, a[r0 + 1] * sc), pk(a[r0 + 2] * sc, a[r0 + 3] * sc), pk(a[r0 + 4] * sc, a[r0 + 5] * sc), pk(a[r0 + 6] * sc, a[r0 + 7] * sc)};
}
__device__ __forceinline__ v4u swap_T8(v4u p) {
  auto r0 = __builtin_amdgcn_permlane32_swap(p[0], p[2], false, false);
  auto r1 = __builtin_amdgcn_permlane32_swap(p[1], p[3], false, false);
  return v4u{r0[0], r1[0], r0[1], r1[1]};
}
__device__ __forceinline__ f32x4 q4(const f32x16& a, int u) { return f32x4{a[4 * u], a[4 * u + 1], a[4 * u + 2], a[4 * u + 3]}; }
#define FRESH() int lane = lane_, c = c_, j = j_, v = v_; asm volatile("" : "+v"(lane), "+s"(c), "+s"(j), "+s"(v)); \
  const int vo = lane * 16, tk = lane & 31, hh = lane >> 5; (void)vo; (void)tk; (void)hh
#define LDS4(off) (*(const LAS v4u*)(L + (off)))
#define LDSF4(off) (*(const LAS f32x4*)(L + (off)))
constexpr int OOB = 0x7FF00000;

template <bool STATS>
__device__ __forceinline__ void fill_half(LAS unsigned char* L, int hf, int v, int lane, const v4u (&d)[4]) {
#pragma unroll
  for (int i = 0; i < 4; ++i) *(LAS v4u*)(L + L_TILE + hf * 32768 + v * 4096 + i * 1024 + lane * 16) = d[i];
  if (STATS) {
    float s0 = 0.f, s1 = 0.f, q0 = 0.f, q1 = 0.f;
#pragma unroll
    for (int i = 0; i < 4; ++i) {
      const v4u x = d[i];
      s0 = dot2(x[0], 0x3F803F80u, s0); q0 = dot2(x[0], x[0], q0);
      s1 = dot2(x[1], 0x3F803F80u, s1); q1 = dot2(x[1], x[1], q1);
      s0 = dot2(x[2], 0x3F803F80u, s0); q0 = dot2(x[2], x[2], q0);
      s1 = dot2(x[3], 0x3F803F80u, s1); q1 = dot2(x[3], x[3], q1);
    }
    const f32x2 ss = both_halves(s0 + s1), qq = both_halves(q0 + q1);
    if (lane < 32) *(LAS f32x2*)(L + L_PST + ((hf * 8 + v) * 32 + lane) * 8) = f32x2{ss.x + ss.y, qq.x + qq.y};
  }
}
template <bool DOUBLE_LN>
__device__ __forceinline__ f32x2 final_stats(LAS unsigned char* L, int lane, int hf) {
  const int t = lane & 31, hh = lane >> 5;
  f32x2 pv[4];
#pragma unroll
  for (int k = 0; k < 4; ++k) pv[k] = *(LAS f32x2*)(L + L_PST + ((hf * 8 + hh * 4 + k) * 32 + t) * 8);
  SB();
  const f32x2 s = (pv[0] + pv[1]) + (pv[2] + pv[3]);
  const f32x2 rs_ = both_halves(s.x), rq = both_halves(s.y);
  const float mean = (rs_.x + rs_.y) * (1.0f / DM);
  float var = (rq.x + rq.y) * (1.0f / DM) - mean * mean;
  var = var > 0.f ? var : 0.f;
  float r = __builtin_amdgcn_rsqf(var + 1e-5f);
  if (DOUBLE_LN) {
    const float v2 = var * r * r;
    r *= __builtin_amdgcn_rsqf(v2 + 1e-5f);
  }
  const f32x2 ab = {r, -mean * r};
  *(LAS f32x2*)(L + L_FST + (hf * 32 + t) * 8) = ab;
  return ab;
}

__global__ void __launch_bounds__(512, 2) k_main(Params p) {
  extern __shared__ __attribute__((aligned(16))) unsigned char smem[];
  LAS unsigned char* L = (LAS unsigned char*)smem;
  const int b = blockIdx.x, tid = threadIdx.x, lane_ = tid & 63, v_ = __builtin_amdgcn_readfirstlane(tid >> 6);
#define MAPPING 1
  int c_, j_;
  if (MAPPING == 0) { c_ = (b & 7) * 4 + (b >> 6); j_ = (b >> 3) & 7; }
  else { c_ = ((b & 7) >> 1) * 8 + (b >> 5); j_ = (b & 1) * 4 + ((b >> 3) & 3); }
  const rsrc_t rs = __builtin_amdgcn_make_buffer_rsrc((void*)p.ws, 0, WS_END, 0x00020000);
  const rsrc_t rx = __builtin_amdgcn_make_buffer_rsrc((void*)p.x, 0, SEQ * DM * 2, 0x00020000);
  GAS unsigned* tmo = (GAS unsigned*)(p.ws + CTRL_OFF);
  STAMP(0);
  v2u xr[2]; v4u wq[24], stA[4];
  f32x16 Z;
#pragma unroll
  for (int q = 0; q < 16; ++q) Z[q] = 0.f;
  {
    FRESH();
    const float* cs = (const float*)(p.ws + CS_OFF);
    float k0 = 0.f, k1 = 0.f;
    if (tid < 224) { k0 = cs[1536 + 224 * j + tid]; k1 = bf2f(p.b1[224 * j + tid]); }
    else if (tid >= 256) { const int t = tid - 256; k0 = t < 192 ? cs[(t >> 6) * 512 + 64 * j + (t & 63)] : bf2f(p.b2[64 * j + t - 192]); }
#pragma unroll
    for (int hf = 0; hf < 2; ++hf) xr[hf] = *(const v2u*)(p.x + (size_t)(64 * c + 32 * hf + tk) * DM + 64 * j + 32 * (v & 1) + 8 * (v >> 1) + 4 * hh);
#pragma unroll
    for (int i = 0; i < 4; ++i) stA[i] = __builtin_amdgcn_raw_buffer_load_b128(rx, tk * 1024 + hh * 16 + i * 32, (64 * c) * 1024 + v * 128, 0);
    SB();
    __builtin_amdgcn_s_barrier();
    SB();
    const int wqb = RFL(WQ_OFF + (j * 192 + v * 24) * KB);
#pragma unroll
    for (int i = 0; i < 16; ++i) wq[i] = ldw(rs, vo, wqb, i);
    SB();
    if (tid < 224) { *(LAS float*)(L + L_C1 + tid * 4) = k0; *(LAS float*)(L + L_C1 + 1024 + tid * 4) = k1; }
    else if (tid >= 256) *(LAS float*)(L + L_CQ + (tid - 256) * 4) = k0;
    else if (tid < 228) *(LAS unsigned*)(L + L_ONES + (tid - 224) * 4) = 0x3F803F80u;
    v4u SENT4 = {SENT, SENT, SENT, SENT};
    asm volatile("" : "+v"(SENT4));
#pragma unroll
    for (int hf = 0; hf < 2; ++hf) {
      if (v < 4) st_sc1(SENT4, rs, vo, KV1_OFF + ((4 * j + v) * 64 + 2 * c + hf) * 2048);
      else st_sc1(SENT4, rs, ((4 * j + 2 * ((v - 4) >> 1) + ((lane >> 4) & 1)) * 64) * 2048 + 1024 + (32 * hh + 16 * (v & 1) + (lane & 15)) * 16, KV1_OFF + (2 * c + hf) * 2048);
    }
    SB();
  }

  for (int lay = 0; lay < 2; ++lay) {
    const int KV_OFF = lay ? KV1_OFF : KV0_OFF, OT_OFF = lay ? OT1_OFF : OT0_OFF, X1T_OFF = lay ? X1T1_OFF : X1T0_OFF, HT_OFF = lay ? HT1_OFF : HT0_OFF;
    v4u wo[8], w1[32], w2[28], stB[4];
    {
      FRESH();
      const int xtb = RFL(XT1_OFF + c * TILE_B + v * 4096);
      const int wob = RFL(WO_OFF + (j * 64 + v * 8) * KB);
      const int kh = v & 1;
      f32x16 acc[2];
      auto xb_issue = [&]() {
        if (lay) {
#pragma unroll
          for (int i = 0; i < 4; ++i) stB[i] = ld_seq(rs, vo, xtb + 32768, i);
        } else {
#pragma unroll
          for (int i = 0; i < 4; ++i) stB[i] = __builtin_amdgcn_raw_buffer_load_b128(rx, tk * 1024 + hh * 16 + i * 32, (64 * c + 32) * 1024 + v * 128, 0);
        }
      };
      auto pass_k = [&](int hf) {
        v4u xs[3];
        const int x0 = L_TILE + hf * 32768 + kh * 16384 + vo;
#pragma unroll
        for (int s = 0; s < 2; ++s) xs[s] = LDS4(x0 + s * 1024);
        acc[hf] = Z;
        PRIO(1);
#pragma unroll
        for (int s = 0; s < 16; ++s) {
          if (s + 2 < 16) xs[(s + 2) % 3] = LDS4(x0 + (s + 2) * 1024);
          SB();
          acc[hf] = mfma32(wq[s], xs[s % 3], acc[hf]);
          if (hf == 0 && s == 8) xb_issue();
          SB();
        }
        KEEP(acc[hf]);
        PRIO(0);
      };
      auto pass_v = [&](int hf) {
        v4u xs[3];
        const int x0 = L_TILE + hf * 32768 + kh * 16384 + (vo ^ (kh * 256));
#pragma unroll
        for (int s = 0; s < 2; ++s) xs[s] = LDS4(x0 + s * 1024);
        acc[hf] = Z;
        PRIO(1);
#pragma unroll
        for (int s = 0; s < 16; ++s) {
          if (s + 2 < 16) xs[(s + 2) % 3] = LDS4(x0 + (s + 2) * 1024);
          SB();
          acc[hf] = mfma32(xs[s % 3], wq[s], acc[hf]);
          if (hf == 0 && s == 8) xb_issue();
          SB();
        }
        KEEP(acc[hf]);
        PRIO(0);
      };
      auto send = [&](int hf) {
#pragma unroll
        for (int u = 0; u < 2; ++u) *(LAS f32x4*)(L + (hf ? L_XK1 : L_XCH) + (v * 2 + u) * 1024 + vo) = q4(acc[hf], 2 + u);
      };
      auto epi_k = [&](int hf) {
        const f32x2 ab = final_stats<false>(L, lane, hf);
        f32x16 val;
#pragma unroll
        for (int u = 0; u < 2; ++u) {
          const f32x4 o = LDSF4((hf ? L_XK1 : L_XCH) + ((v ^ 1) * 2 + u) * 1024 + vo);
          const f32x4 csn = LDSF4(L_CQ + (64 + 16 * v + 8 * u + 4 * hh) * 4);
#pragma unroll
          for (int i = 0; i < 4; ++i) val[4 * u + i] = (acc[hf][4 * u + i] + o[i]) * ab.x + csn[i] * ab.y;
        }
        const v4u K = nosent2(pk8(val, 0));
        st_sc1(K, rs, vo, KV_OFF + ((4 * j + v) * 64 + 2 * c + hf) * 2048);
        *(LAS v4u*)(L + L_KV + ((8 + hf) * 4 + v) * 2048 + vo) = K;
      };
      auto epi_v = [&](int hf) {
        const int pp = (v - 4) >> 1;
        (void)final_stats<false>(L, lane, hf);
        const float csv = *(LAS float*)(L + L_CQ + (128 + 32 * pp + tk) * 4);
        f32x16 val;
#pragma unroll
        for (int u = 0; u < 2; ++u) {
          const f32x4 o = LDSF4((hf ? L_XK1 : L_XCH) + ((v ^ 1) * 2 + u) * 1024 + vo);
          const f32x4 t0 = LDSF4(L_FST + (hf * 32 + 16 * kh + 8 * u + 4 * hh) * 8), t1 = LDSF4(L_FST + (hf * 32 + 16 * kh + 8 * u + 4 * hh + 2) * 8);
          val[4 * u + 0] = (acc[hf][4 * u + 0] + o[0]) * t0[0] + csv * t0[1];
          val[4 * u + 1] = (acc[hf][4 * u + 1] + o[1]) * t0[2] + csv * t0[3];
          val[4 * u + 2] = (acc[hf][4 * u + 2] + o[2]) * t1[0] + csv * t1[1];
          val[4 * u + 3] = (acc[hf][4 * u + 3] + o[3]) * t1[2] + csv * t1[3];
        }
        const int hb = (lane >> 4) & 1, d = lane & 15;
        const v4u V = nosent2(pk8(val, 0));
        st_sc1(V, rs, ((4 * j + 2 * pp + hb) * 64) * 2048 + 1024 + (32 * hh + 16 * kh + d) * 16, KV_OFF + (2 * c + hf) * 2048);
        *(LAS v4u*)(L + L_KV + ((8 + hf) * 4 + 2 * pp + hb) * 2048 + 1024 + (32 * hh + 16 * kh + d) * 16) = V;
      };
      if (lay) verify<4>(stA, [&](int i) { return ld_seq(rs, vo, xtb, i); }, tmo, 0x10, lane);
      fill_half<true>(L, 0, v, lane, stA);
      __syncthreads();
      if (v < 4) pass_k(0); else pass_v(0);
      send(0);
      if (lay) verify<4>(stB, [&](int i) { return ld_seq(rs, vo, xtb + 32768, i); }, tmo, 0x11, lane);
      fill_half<true>(L, 1, v, lane, stB);
      __syncthreads();
      {
        const int wqb = RFL(WQ_OFF + (j * 192 + v * 24) * KB);
#pragma unroll
        for (int i = 16; i < 24; ++i) wq[i] = ldw(rs, vo, wqb, i);
        SB();
      }
      if (v < 4) { epi_k(0); pass_k(1); } else { pass_v(1); epi_v(0); }
      send(1);
      __syncthreads();
      if (v < 4) epi_k(1); else epi_v(1);
      {
        const int ho = (v >> 1) & 1;
        const int x0 = L_TILE + ho * 32768 + (v >> 1) * 8192 + vo, x1 = L_TILE + (1 - ho) * 32768 + (v >> 1) * 8192 + vo;
        v4u xs0[3], xs1[3];
#pragma unroll
        for (int s = 0; s < 2; ++s) { xs0[s] = LDS4(x0 + s * 1024); xs1[s] = LDS4(x1 + s * 1024); }
        acc[0] = Z; acc[1] = Z;
        PRIO(1);
#pragma unroll
        for (int s = 0; s < 8; ++s) {
          if (s + 2 < 8) { xs0[(s + 2) % 3] = LDS4(x0 + (s + 2) * 1024); xs1[(s + 2) % 3] = LDS4(x1 + (s + 2) * 1024); }
          SB();
          acc[0] = mfma32(wq[16 + s], xs0[s % 3], acc[0]); acc[1] = mfma32(wq[16 + s], xs1[s % 3], acc[1]);
          wo[s] = ldw(rs, vo, wob, s);
          SB();
        }
        KEEP(acc[0]); KEEP(acc[1]);
        PRIO(0);
#pragma unroll
        for (int u = 0; u < 2; ++u) {
          *(LAS f32x4*)(L + L_XCH + (((v ^ 4) * 3 + 0) * 2 + u) * 1024 + vo) = q4(acc[0], 2 + u);
          *(LAS f32x4*)(L + L_XCH + (((v ^ 2) * 3 + 1) * 2 + u) * 1024 + vo) = q4(acc[1], u);
          *(LAS f32x4*)(L + L_XCH + (((v ^ 6) * 3 + 2) * 2 + u) * 1024 + vo) = q4(acc[1], 2 + u);
        }
      }
      v4u kvn[8];
      auto kv_load = [&](int i) {
        const int kt = 2 * c - 8 + 4 * (v >> 2) + (i >> 1);
        return ld_sc1(rs, vo + (i & 1) * 1024, RFL(kt >= 0 ? KV_OFF + ((4 * j + (v & 3)) * 64 + kt) * 2048 : OOB));
      };
      auto kv_issue = [&]() {
#pragma unroll
        for (int i = 0; i < 8; ++i) kvn[i] = kv_load(i);
      };
      WAITV0();
      __syncthreads();
      STAMP(16 + lay * 24 + 6);
      kv_issue();
      {
        const int ho = (v >> 1) & 1, hl = 2 * (v & 1) + (v >> 2);
        const f32x2 ab = *(LAS f32x2*)(L + L_FST + (ho * 32 + tk) * 8);
        const float cexp = 0.25f * 1.4426950408889634f;
        f32x16 val;
#pragma unroll
        for (int u = 0; u < 2; ++u) {
          f32x4 o = q4(acc[0], u);
#pragma unroll
          for (int k = 0; k < 3; ++k) o += LDSF4(L_XCH + ((v * 3 + k) * 2 + u) * 1024 + vo);
          const f32x4 csn = LDSF4(L_CQ + (16 * hl + 8 * u + 4 * hh) * 4);
#pragma unroll
          for (int i = 0; i < 4; ++i) val[4 * u + i] = o[i] * (ab.x * cexp) + csn[i] * (ab.y * cexp);
        }
        *(LAS v4u*)(L + L_Q + (hl * 2 + ho) * 1024 + vo) = pk8(val, 0);
      }
      const int w1b = RFL(v < 7 ? W1_OFF + (j * 224 + v * 32) * KB : OOB);
#pragma unroll
      for (int i = 0; i < 4; ++i) w1[i] = ldw(rs, vo, w1b, i);
      SB();
      verify<8>(kvn, kv_load, tmo, 0x20, lane);
      STAMP(16 + lay * 24 + 7);
      SB();
#pragma unroll
      for (int i = 0; i < 4; ++i) {
        *(LAS v4u*)(L + L_KV + ((4 * (v >> 2) + i) * 4 + (v & 3)) * 2048 + vo) = kvn[2 * i];
        *(LAS v4u*)(L + L_KV + ((4 * (v >> 2) + i) * 4 + (v & 3)) * 2048 + 1024 + vo) = kvn[2 * i + 1];
      }
      __syncthreads();
    }
    STAMP(1 + lay * 8);
    {
      FRESH();
      const int hq = v & 3, r = v >> 2, tq = tk - 4 * hh;
      const int kb = L_KV + (r * 4 + hq) * 2048;
      const v4u Q = LDS4(L_Q + (hq * 2 + r) * 1024 + vo);
      v4u Kf[9];
#pragma unroll
      for (int i = 0; i < 9; ++i) Kf[i] = LDS4(kb + i * 8192 + vo);
      const bool ones_lane = (lane & 16) != 0;
      const unsigned vsel = ones_lane ? 0u : 1u, vbase = ones_lane ? (unsigned)L_ONES : (unsigned)(kb + 1024 + (32 * hh + (lane & 15)) * 16);
      auto vop = [&](int i, int s) -> v4u { return LDS4(vbase + vsel * (unsigned)(i * 8192 + s * 256)); };
      const int first_ok = 8 - r - 2 * c;
      v4u SENT4 = {SENT, SENT, SENT, SENT};
      asm volatile("" : "+v"(SENT4));
      const int tsl = (8 * j + 4 * (v & 1) + (v >> 1)) * 512 + tk * 16 + hh * 8;
#pragma unroll
      for (int hf = 0; hf < 2; ++hf) __builtin_amdgcn_raw_buffer_store_b64(v2u{SENT4.x, SENT4.y}, rs, tsl + X1T_OFF + c * TILE_B + hf * 32768, 0, AUX_SC1);
      const int hcb = RFL(v < 7 ? HT_OFF + c * HT_C + (28 * j + 4 * v) * 512 : OOB);
      const int xcb = RFL(lay == 0 ? XT1_OFF + c * TILE_B : OOB), ocb = RFL(lay == 0 ? OT1_OFF + c * TILE_B + (v >> 2) * 32768 + (2 * (4 * j + (v & 3))) * 512 : OOB);
      auto clean = [&](int i) {
        if (i < 4) st_sc1(SENT4, rs, vo, hcb + (i >> 1) * 114688 + 2 * (i & 1) * 512);
        else if (i < 6) __builtin_amdgcn_raw_buffer_store_b64(v2u{SENT4.x, SENT4.y}, rs, tsl + xcb + (i - 4) * 32768, 0, AUX_SC1);
        else st_sc1(SENT4, rs, vo, ocb);
      };
      SB();
      const int w1b = RFL(v < 7 ? W1_OFF + (j * 224 + v * 32) * KB : OOB);
      auto tmax = [&](const f32x16& x, float m) {
        const float a = max3f(x[0], x[1], x[2]), b2_ = max3f(x[3], x[4], x[5]), c2 = max3f(x[6], x[7], x[8]), d = max3f(x[9], x[10], x[11]), e = max3f(x[12], x[13], x[14]);
        return max3f(max3f(a, b2_, x[15]), max3f(c2, d, e), m);
      };
      f32x16 Sm, S[2];
      {
        f32x16 Sa = mfma32(Kf[0], Q, Z);
        const f32x16 Sb = mfma32(Kf[8], Q, Z);
        S[1] = mfma32(Kf[1], Q, Z);
        if (first_ok > 0) {
#pragma unroll
          for (int q = 0; q < 16; ++q) Sa[q] = -1e30f;
        }
#pragma unroll
        for (int q = 0; q < 16; ++q) { const int row = (q & 3) + 8 * (q >> 2); Sm[q] = row <= tq ? Sb[q] : Sa[q]; }
      }
      float mx = tmax(Sm, -1e30f);
      SB();
#pragma unroll
      for (int i = 1; i < 8; ++i) {
        if (i + 1 < 8) S[(i + 1) & 1] = mfma32(Kf[i + 1], Q, Z);
        if (i >= first_ok) mx = tmax(S[i & 1], mx);
        if (i < 6) w1[4 + (i - 1)] = ldw(rs, vo, w1b, 4 + (i - 1));
        SB();
      }
      { const f32x2 rr = both_halves(mx); mx = max3f(rr.x, rr.y, rr.y); }
      f32x16 Cm;
#pragma unroll
      for (int q = 0; q < 16; ++q) Cm[q] = -mx;
      S[1] = mfma32(Kf[1], Q, Cm);
      f32x16 O;
      {
        const v4u vn0 = vop(8, 0), vn1 = vop(8, 1), vo0 = vop(0, 0), vo1 = vop(0, 1);
        unsigned pp[8];
#pragma unroll
        for (int q = 0; q < 8; ++q) pp[q] = pk(__builtin_amdgcn_exp2f(Sm[2 * q] - mx), __builtin_amdgcn_exp2f(Sm[2 * q + 1] - mx));
        v4u pn[2], po[2];
#pragma unroll
        for (int q = 0; q < 8; ++q) {
          const int row0 = ((2 * q) & 3) + 8 * ((2 * q) >> 2);
          const unsigned mk = (row0 <= tq ? 0xFFFFu : 0u) | (row0 + 1 <= tq ? 0xFFFF0000u : 0u);
          pn[q >> 2][q & 3] = pp[q] & mk; po[q >> 2][q & 3] = pp[q] & ~mk;
        }
        O = mfma32(vn0, pn[0], Z); O = mfma32(vn1, pn[1], O);
        O = mfma32(vo0, po[0], O); O = mfma32(vo1, po[1], O);
        SB();
      }
#pragma unroll
      for (int i = 1; i < 8; ++i) {
        if (i + 1 < 8) S[(i + 1) & 1] = mfma32(Kf[i + 1], Q, Cm);
        const v4u va0 = vop(i, 0), va1 = vop(i, 1);
        if (i >= first_ok) {
          const f32x16& cur = S[i & 1];
          v4u pp[2];
#pragma unroll
          for (int q = 0; q < 8; ++q) pp[q >> 2][q & 3] = pk(__builtin_amdgcn_exp2f(cur[2 * q]), __builtin_amdgcn_exp2f(cur[2 * q + 1]));
          O = mfma32(va0, pp[0], O); O = mfma32(va1, pp[1], O);
        }
        w1[9 + (i - 1)] = ldw(rs, vo, w1b, 9 + (i - 1));
        clean(i - 1);
        SB();
      }
      KEEP(O);
      const float rl = __builtin_amdgcn_rcpf(O[8]);
      asm volatile("s_waitcnt vmcnt(19)" ::: "memory");
      st_sc1(nosent2(swap_T8(pk8(O, 0, rl))), rs, vo, OT_OFF + c * TILE_B + r * 32768 + (2 * (4 * j + hq)) * 512);
      if (v < 4) WAITV0();
    }
    STAMP(2 + lay * 8);
    {
      FRESH();
      const int otb = RFL(OT_OFF + c * TILE_B + v * 4096);
      const int w1b = RFL(v < 7 ? W1_OFF + (j * 224 + v * 32) * KB : OOB);
      auto oa_issue = [&]() {
#pragma unroll
        for (int i = 0; i < 4; ++i) stA[i] = ld_seq(rs, vo, otb, i);
      };
      auto ob_issue = [&]() {
#pragma unroll
        for (int i = 0; i < 4; ++i) stB[i] = ld_seq(rs, vo, otb + 32768, i);
      };
      f32x16 a;
      auto pass = [&](int hf) {
        const int x0 = L_TILE + hf * 32768 + (v >> 1) * 8192 + vo;
        v4u xs[8];
#pragma unroll
        for (int s = 0; s < 8; ++s) xs[s] = LDS4(x0 + s * 1024);
        a = Z;
#pragma unroll
        for (int s = 0; s < 8; ++s) a = mfma32(wo[s], xs[s], a);
        KEEP(a);
#pragma unroll
        for (int u = 1; u < 4; ++u) *(LAS f32x4*)(L + L_XCH + ((hf * 8 + (v ^ (2 * u))) * 3 + (u - 1)) * 1024 + vo) = q4(a, u);
      };
      auto epi = [&](int hf) {
        f32x4 o = q4(a, 0);
#pragma unroll
        for (int k = 0; k < 3; ++k) o += LDSF4(L_XCH + ((hf * 8 + v) * 3 + k) * 1024 + vo);
        xr[hf] = pk4(unpk4(xr[hf]) + o);
        __builtin_amdgcn_raw_buffer_store_b64(v2u{nosent(xr[hf].x), xr[hf].y}, rs, (8 * j + 4 * (v & 1) + (v >> 1)) * 512 + tk * 16 + hh * 8 + X1T_OFF + c * TILE_B + hf * 32768, 0, AUX_SC1);
      };
      oa_issue();
#pragma unroll
      for (int i = 16; i < 24; ++i) w1[i] = ldw(rs, vo, w1b, i);
      SB();
      verify<4>(stA, [&](int i) { return ld_seq(rs, vo, otb, i); }, tmo, 0x30, lane);
      STAMP(16 + lay * 24 + 8);
      __syncthreads();
      fill_half<false>(L, 0, v, lane, stA);
      ob_issue();
      __syncthreads();
      STAMP(16 + lay * 24 + 9);
      pass(0);
      STAMP(16 + lay * 24 + 21);
      verify<4>(stB, [&](int i) { return ld_seq(rs, vo, otb + 32768, i); }, tmo, 0x31, lane);
      fill_half<false>(L, 1, v, lane, stB);
      __syncthreads();
      epi(0);
      STAMP(16 + lay * 24 + 23);
      pass(1);
#pragma unroll
      for (int i = 24; i < 32; ++i) w1[i] = ldw(rs, vo, w1b, i);
      __syncthreads();
#pragma unroll
      for (int i = 0; i < 4; ++i) stA[i] = ld_seq(rs, vo, RFL(X1T_OFF + c * TILE_B + v * 4096), i);
      SB();
      epi(1);
    }
    STAMP(3 + lay * 8);
    v4u hA[14];
    {
      FRESH();
      const int x1b = RFL(X1T_OFF + c * TILE_B + v * 4096);
      const int w2b = RFL(W2_OFF + (j * 224 + v * 28) * KB);
      const int hb = RFL(v < 7 ? HT_OFF + c * HT_C + (28 * j + 4 * v) * 512 : OOB);
      auto xb_issue = [&]() {
#pragma unroll
        for (int i = 0; i < 4; ++i) stB[i] = ld_seq(rs, vo, x1b + 32768, i);
      };
      f32x16 a;
      auto pass = [&](int hf) {
        v4u xs[3];
        const int x0 = L_TILE + hf * 32768 + vo;
#pragma unroll
        for (int s = 0; s < 2; ++s) xs[s] = LDS4(x0 + s * 1024);
        a = Z;
        PRIO(1);
#pragma unroll
        for (int s = 0; s < 32; ++s) {
          if (s + 2 < 32) xs[(s + 2) % 3] = LDS4(x0 + (s + 2) * 1024);
          SB();
          a = mfma32(w1[s], xs[s % 3], a);
          if (hf == 0 && s == 8) xb_issue();
          if (hf == 0 && s >= 22) w2[s - 22] = ldw(rs, vo, w2b, s - 22);
          if (hf == 1 && s >= 2 && s < 20) w2[10 + (s - 2)] = ldw(rs, vo, w2b, 10 + (s - 2));
          SB();
        }
        KEEP(a);
        PRIO(0);
      };
      v4u ho[2];
      auto gelu = [&](int hf) {
        const f32x2 ab = final_stats<true>(L, lane, hf);
        const float c1 = -2.0f * 0.7978845608028654f * 1.4426950408889634f, c2 = c1 * 0.044715f;
#pragma unroll
        for (int u2 = 0; u2 < 2; ++u2) {
          f32x16 hv;
#pragma unroll
          for (int u = 2 * u2; u < 2 * u2 + 2; ++u) {
            const f32x4 csn = LDSF4(L_C1 + (32 * v + 8 * u + 4 * hh) * 4), bn = LDSF4(L_C1 + 1024 + (32 * v + 8 * u + 4 * hh) * 4);
#pragma unroll
            for (int i = 0; i < 4; ++i) {
              const float z = a[4 * u + i] * ab.x + (csn[i] * ab.y + bn[i]);
              const float e = __builtin_amdgcn_exp2f(z * (z * z * c2 + c1));
              hv[4 * u + i] = z * __builtin_amdgcn_rcpf(1.0f + e);
            }
          }
          ho[u2] = nosent2(swap_T8(pk8(hv, 8 * u2)));
          if (hf == 0) st_sc1(ho[u2], rs, vo, hb + 2 * u2 * 512);
        }
      };
      verify<4>(stA, [&](int i) { return ld_seq(rs, vo, x1b, i); }, tmo, 0x40, lane);
      STAMP(16 + lay * 24 + 10);
      fill_half<true>(L, 0, v, lane, stA);
      __syncthreads();
      STAMP(16 + lay * 24 + 11);
      pass(0);
      STAMP(16 + lay * 24 + 12);
      gelu(0);
      STAMP(16 + lay * 24 + 13);
      verify<4>(stB, [&](int i) { return ld_seq(rs, vo, x1b + 32768, i); }, tmo, 0x41, lane);
      fill_half<true>(L, 1, v, lane, stB);
      __syncthreads();
      STAMP(16 + lay * 24 + 14);
      pass(1);
      STAMP(16 + lay * 24 + 15);
#pragma unroll
      for (int s = 0; s < 14; ++s) hA[s] = ld_seq(rs, vo, RFL(HT_OFF + c * HT_C + 28 * v * 512), s);
      SB();
      gelu(1);
#pragma unroll
      for (int u2 = 0; u2 < 2; ++u2) st_sc1(ho[u2], rs, vo, hb + 114688 + 2 * u2 * 512);
    }
    STAMP(4 + lay * 8);
    {
      FRESH();
      const int htb = RFL(HT_OFF + c * HT_C + 28 * v * 512);
      const int wqb = RFL(lay == 0 ? WQ_OFF + (j * 192 + v * 24) * KB : OOB);
      const int xnb = RFL(lay == 0 ? XT1_OFF + c * TILE_B + v * 4096 : OOB);
#pragma unroll
      for (int hf = 0; hf < 2; ++hf) {
        verify<14>(hA, [&](int i) { return ld_seq(rs, vo, htb + hf * 114688, i); }, tmo, 0x50 + hf, lane);
        if (hf == 0) __syncthreads();
        STAMP(16 + lay * 24 + 17 + 2 * hf);
        f32x16 ac[2] = {Z, Z};
        PRIO(1);
#pragma unroll
        for (int s = 0; s < 14; ++s) {
          ac[0] = mfma32(w2[2 * s], hA[s], ac[0]);
          ac[1] = mfma32(w2[2 * s + 1], hA[s], ac[1]);
          if (hf == 1) wq[s] = ldw(rs, vo, wqb, s);
          else hA[s] = ld_seq(rs, vo, htb + 114688, s);
          SB();
        }
        KEEP(ac[0]); KEEP(ac[1]);
        PRIO(0);
        STAMP(16 + lay * 24 + 18 + 2 * hf);
        LAS unsigned char* R = L + (hf ? L_TILE : L_RB);
#pragma unroll
        for (int nt = 0; nt < 2; ++nt)
#pragma unroll
          for (int u = 0; u < 4; ++u) *(LAS f32x4*)(R + ((nt + 2 * u) * 8 + v) * 1024 + vo) = q4(ac[nt], u);
        if (hf == 1) { wq[14] = ldw(rs, vo, wqb, 14); wq[15] = ldw(rs, vo, wqb, 15); }
        __syncthreads();
        f32x4 s0 = {0, 0, 0, 0};
#pragma unroll
        for (int k = 0; k < 8; ++k) s0 += *(LAS f32x4*)(R + (v * 8 + k) * 1024 + vo);
        const f32x4 b0 = LDSF4(L_CQ + (192 + 32 * (v & 1) + 8 * (v >> 1) + 4 * hh) * 4);
        xr[hf] = pk4(unpk4(xr[hf]) + s0 + b0);
        if (hf == 1) {
#pragma unroll
          for (int i = 0; i < 4; ++i) stA[i] = ld_seq(rs, vo, xnb, i);
          SB();
        }
        if (lay == 0) {
          __builtin_amdgcn_raw_buffer_store_b64(v2u{nosent(xr[hf].x), xr[hf].y}, rs, (8 * j + 4 * (v & 1) + (v >> 1)) * 512 + tk * 16 + hh * 8 + XT1_OFF + c * TILE_B + hf * 32768, 0, AUX_SC1);
        } else {
          *(v2u*)(p.out + (size_t)(64 * c + 32 * hf + tk) * DM + 64 * j + 32 * (v & 1) + 8 * (v >> 1) + 4 * hh) = xr[hf];
        }
      }
      __syncthreads();
    }
    STAMP(5 + lay * 8);
  }
  STAMP_FLUSH();
}
}

extern "C" void kernel_launch(void* const* d_in, const int* in_sizes, int n_in, void* d_out, int out_size, void* d_ws, size_t ws_size,
                              hipStream_t stream) {
  using namespace fk;
  Params p;
  p.x = (const u16*)d_in[0]; p.wqkv = (const u16*)d_in[1]; p.wo = (const u16*)d_in[2];
  p.w1 = (const u16*)d_in[4]; p.b1 = (const u16*)d_in[5]; p.w2 = (const u16*)d_in[6]; p.b2 = (const u16*)d_in[7];
  p.out = (u16*)d_out; p.ws = (unsigned char*)d_ws;
  (void)hipFuncSetAttribute((const void*)k_main, hipFuncAttributeMaxDynamicSharedMemorySize, L_BYTES);
  k_prep<<<PREP_BLOCKS, 256, 0, stream>>>(p);
  k_main<<<256, 512, L_BYTES, stream>>>(p);
}
```

```cpp
#include <hip/hip_runtime.h>
#include <stdint.h>

namespace fk {
typedef unsigned short u16;
typedef unsigned v4u __attribute__((ext_vector_type(4)));
typedef unsigned v2u __attribute__((ext_vector_type(2)));
typedef float f32x4 __attribute__((ext_vector_type(4)));
typedef float f32x2 __attribute__((ext_vector_type(2)));
typedef float f32x16 __attribute__((ext_vector_type(16)));
typedef __bf16 bf8v __attribute__((ext_vector_type(8)));
typedef __bf16 bf2v __attribute__((ext_vector_type(2)));
#define LAS __attribute__((address_space(3)))
#define GAS __attribute__((address_space(1)))
typedef __amdgpu_buffer_rsrc_t rsrc_t;

constexpr int SEQ = 2048, DM = 512, FF = 1792;
constexpr int KB = 1024;
constexpr int WQ_OFF = 0;
constexpr int WO_OFF = WQ_OFF + 8 * 192 * KB;
constexpr int W1_OFF = WO_OFF + 8 * 64 * KB;
constexpr int W2_OFF = W1_OFF + 8 * 224 * KB;
constexpr int CS_OFF = W2_OFF + 8 * 224 * KB;
constexpr int CTRL_OFF = CS_OFF + 16 * KB;
constexpr int TILE_B = 64 * KB;
constexpr int KV_B = 32 * 64 * 2 * KB;
constexpr int HT_C = 2 * 224 * 512;
constexpr int KV0_OFF = CTRL_OFF + 16 * KB;
constexpr int OT0_OFF = KV0_OFF + KV_B;
constexpr int KV1_OFF = OT0_OFF + 32 * TILE_B;
constexpr int OT1_OFF = KV1_OFF + KV_B;
constexpr int X1T0_OFF = OT1_OFF + 32 * TILE_B;
constexpr int X1T1_OFF = X1T0_OFF + 32 * TILE_B;
constexpr int XT1_OFF = X1T1_OFF + 32 * TILE_B;
constexpr int HT0_OFF = XT1_OFF + 32 * TILE_B;
constexpr int HT1_OFF = HT0_OFF + 32 * HT_C;
constexpr int WS_END = HT1_OFF + 32 * HT_C;
constexpr unsigned SENT = 0xFFFFFFFFu;
constexpr int AUX_SC1 = 16;
constexpr unsigned SPIN_CAP = 1u << 14;

constexpr int L_TILE = 0;
constexpr int L_KV = 0;
constexpr int L_RB = 64 * KB;
constexpr int L_Q = 80 * KB;
constexpr int L_XK1 = 88 * KB;
constexpr int L_XCH = 104 * KB;
constexpr int L_PST = 152 * KB;
constexpr int L_FST = 156 * KB;
constexpr int L_C1 = 156 * KB + 512;
constexpr int L_CQ = 158 * KB + 512;
constexpr int L_ONES = 159 * KB + 512;
constexpr int L_BYTES = 159 * KB + 576;

__device__ __forceinline__ float bf2f(unsigned b) { return __builtin_bit_cast(float, b << 16); }
__device__ __forceinline__ float bflo(unsigned d) { return __builtin_bit_cast(float, d << 16); }
__device__ __forceinline__ float bfhi(unsigned d) { return __builtin_bit_cast(float, d & 0xFFFF0000u); }
__device__ __forceinline__ unsigned pk(float lo, float hi) {
  bf2v r = {(__bf16)lo, (__bf16)hi};
  return __builtin_bit_cast(unsigned, r);
}
__device__ __forceinline__ unsigned nosent(unsigned d) { return d < 0xFFFFFFFEu ? d : 0xFFFFFFFEu; }
__device__ __forceinline__ v2u pk4(f32x4 a) { return v2u{pk(a[0], a[1]), pk(a[2], a[3])}; }
__device__ __forceinline__ f32x4 unpk4(v2u v) { return f32x4{bflo(v.x), bfhi(v.x), bflo(v.y), bfhi(v.y)}; }
#define KEEP(x) asm volatile("" : "+v"(x))
#define WAITV0() asm volatile("s_waitcnt vmcnt(0)" ::: "memory")
#define PRIO(x) __builtin_amdgcn_s_setprio(x)
#define SB() __builtin_amdgcn_sched_barrier(0)

__device__ __forceinline__ v4u ldw(rsrc_t rs, int voff, int sbase, int i) {
  return __builtin_amdgcn_raw_buffer_load_b128(rs, voff + (i & 3) * 1024, sbase + (i >> 2) * 4096, 0);
}
__device__ __forceinline__ v4u ld_sc1(rsrc_t rs, int voff, int soff) {
  return __builtin_amdgcn_raw_buffer_load_b128(rs, voff, soff, AUX_SC1); }
__device__ __forceinline__ v4u ld_seq(rsrc_t rs, int voff, int sbase, int i) { return ld_sc1(rs, voff + (i & 3) * 1024, sbase + (i >> 2) * 4096); }
#define RFL(x) __builtin_amdgcn_readfirstlane(x)
__device__ __forceinline__ void st_sc1(v4u v, rsrc_t rs, int voff, int soff) {
  __builtin_amdgcn_raw_buffer_store_b128(v, rs, voff + soff, 0, AUX_SC1); }
__device__ __forceinline__ bool spin_fail(unsigned& spins, GAS unsigned* tmo, unsigned code, int lane) {
  ++spins;
  if (spins > SPIN_CAP) {
    if (lane == 0) __hip_atomic_store(tmo, code, __ATOMIC_RELAXED, __HIP_MEMORY_SCOPE_AGENT);
    return true;
  }
  if ((spins & 63u) == 0u && __hip_atomic_load(tmo, __ATOMIC_RELAXED, __HIP_MEMORY_SCOPE_AGENT) != 0u) return true;
  return false;
}

struct Params {
  const u16* x;
  const u16* wqkv;
  const u16* wo;
  const u16* w1;
  const u16* b1;
  const u16* w2;
  const u16* b2;
  u16* out;
  unsigned char* ws;
};

constexpr int NFRAG = 8 * 704;
constexpr int PB_TILE = NFRAG * 16 / 256;
constexpr int PB_CS = (1536 + 1792) / 64;
constexpr int CLEAN_B = KV_B + 32 * TILE_B;
constexpr int PB_CLEAN = CLEAN_B / (256 * 64);
constexpr int PREP_BLOCKS = PB_TILE + PB_CS + PB_CLEAN;

__global__ void __launch_bounds__(256) k_prep(Params p) {
  const int b = blockIdx.x, tid = threadIdx.x;
  if (b < PB_TILE) {
    const int F = b * 16 + (tid >> 4), t16 = tid & 15, cq = t16 & 7, h = t16 >> 3;
    const u16* W; int ldw_, k0, col;
    if (F < 8 * 192) {
      const int j = F / 192, v = (F % 192) / 24, i = F % 24;
      W = p.wqkv; ldw_ = 1536;
      if (i < 16) {
        k0 = 16 * (16 * (v & 1) + i);
        col = v < 4 ? 512 + (4 * j + 2 * (v >> 1)) * 16 + 4 * (cq ^ (4 * (v & 1))) : 1024 + (4 * j + 2 * ((v - 4) >> 1)) * 16 + 4 * cq;
      } else {
        k0 = 16 * (8 * (v >> 1) + i - 16);
        col = (4 * j + 2 * (v & 1)) * 16 + 4 * (cq ^ (4 * (v >> 2)));
      }
    } else if (F < 8 * 256) {
      const int f = F - 8 * 192, j = f >> 6, u = f & 63, v = u >> 3;
      W = p.wo; ldw_ = 512; k0 = 16 * (8 * (v >> 1) + (u & 7)); col = 64 * j + 32 * (v & 1) + 4 * (cq ^ (2 * (v >> 1)));
    } else if (F < 8 * 480) {
      const int f = F - 8 * 256, j = f / 224, u = f % 224;
      W = p.w1; ldw_ = 1792; k0 = 16 * (u & 31); col = 224 * j + 32 * (u >> 5) + 4 * cq;
    } else {
      const int f = F - 8 * 480, j = f / 224, u = f % 224, v = u / 28, q = u % 28;
      W = p.w2; ldw_ = 512; k0 = 224 * v + 16 * (q >> 1); col = 64 * j + 32 * (q & 1) + 4 * cq;
    }
    const u16* src = W + (size_t)(k0 + 8 * h) * ldw_ + col;
    v2u r[8];
#pragma unroll
    for (int e = 0; e < 8; ++e) r[e] = *(const v2u*)(src + (size_t)e * ldw_);
    v4u o[4];
#pragma unroll
    for (int e = 0; e < 4; ++e) {
      const unsigned a0 = r[2 * e].x, a1 = r[2 * e + 1].x, c0 = r[2 * e].y, c1 = r[2 * e + 1].y;
      o[0][e] = (a0 & 0xFFFFu) | (a1 << 16);
      o[1][e] = (a0 >> 16) | (a1 & 0xFFFF0000u);
      o[2][e] = (c0 & 0xFFFFu) | (c1 << 16);
      o[3][e] = (c0 >> 16) | (c1 & 0xFFFF0000u);
    }
    v4u* dst = (v4u*)(p.ws + (size_t)F * 1024 + (h * 32 + 4 * cq) * 16);
#pragma unroll
    for (int i = 0; i < 4; ++i) dst[i] = o[i];
  } else if (b < PB_TILE + PB_CS) {
    __shared__ float red[16][64];
    const int cb = b - PB_TILE;
    const u16* W; int ldw_, col;
    if (cb < 24) { W = p.wqkv; ldw_ = 1536; col = cb * 64; } else { W = p.w1; ldw_ = 1792; col = (cb - 24) * 64; }
    const int cq = tid & 15, rg = tid >> 4;
    float s0 = 0, s1 = 0, s2 = 0, s3 = 0;
    const u16* src = W + (size_t)(rg * 32) * ldw_ + col + 4 * cq;
#pragma unroll
    for (int e = 0; e < 32; ++e) {
      const v2u v = *(const v2u*)(src + (size_t)e * ldw_);
      s0 += bflo(v.x); s1 += bfhi(v.x); s2 += bflo(v.y); s3 += bfhi(v.y);
    }
    red[rg][4 * cq + 0] = s0; red[rg][4 * cq + 1] = s1; red[rg][4 * cq + 2] = s2; red[rg][4 * cq + 3] = s3;
    __syncthreads();
    if (tid < 64) {
      float s = 0;
#pragma unroll
      for (int r = 0; r < 16; ++r) s += red[r][tid];
      ((float*)(p.ws + CS_OFF))[cb * 64 + tid] = s;
    }
    if (cb == 0 && tid == 0) *(unsigned*)(p.ws + CTRL_OFF) = 0u;
  } else {
    const int cb = b - PB_TILE - PB_CS;
    v4u* dst = (v4u*)(p.ws + KV0_OFF + (size_t)cb * (256 * 64)) + tid;
    const v4u s = {SENT, SENT, SENT, SENT};
#pragma unroll
    for (int i = 0; i < 4; ++i) dst[i * 256] = s;
  }
}


__device__ __forceinline__ float max3f(float a, float b, float c) { float r; asm("v_max3_f32 %0, %1, %2, %3" : "=v"(r) : "v"(a), "v"(b), "v"(c)); return r; }
__device__ __forceinline__ unsigned max3u(unsigned a, unsigned b, unsigned c) { unsigned r; asm("v_max3_u32 %0, %1, %2, %3" : "=v"(r) : "v"(a), "v"(b), "v"(c)); return r; }
__device__ __forceinline__ f32x16 mfma32(v4u a, v4u b, f32x16 c) {
  return __builtin_amdgcn_mfma_f32_32x32x16_bf16(__builtin_bit_cast(bf8v, a), __builtin_bit_cast(bf8v, b), c, 0, 0, 0);
}
template <int N> __device__ __forceinline__ bool any_sent(const v4u (&d)[N]) {
  unsigned m0 = 0, m1 = 0;
#pragma unroll
  for (int i = 0; i < N; ++i) { if (i & 1) m1 = max3u(m1, d[i].x, d[i].z); else m0 = max3u(m0, d[i].x, d[i].z); }
  return __any(max3u(m0, m1, 0) == SENT);
}
template <int N, class F> __device__ __forceinline__ void verify(v4u (&d)[N], F load_i, GAS unsigned* tmo, unsigned code, int lane) {
  if (__builtin_expect(any_sent<N>(d), 0)) {
    unsigned spins = 0;
    do {
      __builtin_amdgcn_s_sleep(1);
      asm volatile("" ::: "memory");
#pragma unroll
      for (int i = 0; i < N; ++i)
        if (__any(max3u(d[i].x, d[i].z, 0u) == SENT)) d[i] = load_i(i);
    } while (any_sent<N>(d) && !spin_fail(spins, tmo, code, lane));
  }
}
__device__ __forceinline__ f32x2 both_halves(float v) {
  const unsigned a = __builtin_bit_cast(unsigned, v);
  auto r = __builtin_amdgcn_permlane32_swap(a, a, false, false);
  const unsigned r0 = r[0], r1 = r[1];
  return f32x2{__builtin_bit_cast(float, r0), __builtin_bit_cast(float, r1)};
}
__device__ __forceinline__ float dot2(unsigned a, unsigned b, float c) {
  return __builtin_amdgcn_fdot2_f32_bf16(__builtin_bit_cast(bf2v, a), __builtin_bit_cast(bf2v, b), c, false);
}
__device__ __forceinline__ v4u nosent2(v4u v) { return v4u{nosent(v.x), v.y, nosent(v.z), v.w}; }

__device__ __forceinline__ v4u pk8(const f32x16& a, int r0, float sc = 1.0f) {
  return v4u{pk(a[r0] * sc, a[r0 + 1] * sc), pk(a[r0 + 2] * sc, a[r0 + 3] * sc), pk(a[r0 + 4] * sc, a[r0 + 5] * sc), pk(a[r0 + 6] * sc, a[r0 + 7] * sc)};
}
__device__ __forceinline__ v4u swap_T8(v4u p) {
  auto r0 = __builtin_amdgcn_permlane32_swap(p[0], p[2], false, false);
  auto r1 = __builtin_amdgcn_permlane32_swap(p[1], p[3], false, false);
  return v4u{r0[0], r1[0], r0[1], r1[1]};
}
__device__ __forceinline__ f32x4 q4(const f32x16& a, int u) { return f32x4{a[4 * u], a[4 * u + 1], a[4 * u + 2], a[4 * u + 3]}; }
#define FRESH() int lane = lane_, c = c_, j = j_, v = v_; asm volatile("" : "+v"(lane), "+s"(c), "+s"(j), "+s"(v)); \
  const int vo = lane * 16, tk = lane & 31, hh = lane >> 5; (void)vo; (void)tk; (void)hh
#define LDS4(off) (*(const LAS v4u*)(L + (off)))
#define LDSF4(off) (*(const LAS f32x4*)(L + (off)))
constexpr int OOB = 0x7FF00000;

template <bool STATS>
__device__ __forceinline__ void fill_half(LAS unsigned char* L, int hf, int v, int lane, const v4u (&d)[4]) {
#pragma unroll
  for (int i = 0; i < 4; ++i) *(LAS v4u*)(L + L_TILE + hf * 32768 + v * 4096 + i * 1024 + lane * 16) = d[i];
  if (STATS) {
    float s0 = 0.f, s1 = 0.f, q0 = 0.f, q1 = 0.f;
#pragma unroll
    for (int i = 0; i < 4; ++i) {
      const v4u x = d[i];
      s0 = dot2(x[0], 0x3F803F80u, s0); q0 = dot2(x[0], x[0], q0);
      s1 = dot2(x[1], 0x3F803F80u, s1); q1 = dot2(x[1], x[1], q1);
      s0 = dot2(x[2], 0x3F803F80u, s0); q0 = dot2(x[2], x[2], q0);
      s1 = dot2(x[3], 0x3F803F80u, s1); q1 = dot2(x[3], x[3], q1);
    }
    const f32x2 ss = both_halves(s0 + s1), qq = both_halves(q0 + q1);
    if (lane < 32) *(LAS f32x2*)(L + L_PST + ((hf * 8 + v) * 32 + lane) * 8) = f32x2{ss.x + ss.y, qq.x + qq.y};
  }
}
template <bool DOUBLE_LN>
__device__ __forceinline__ f32x2 final_stats(LAS unsigned char* L, int lane, int hf) {
  const int t = lane & 31, hh = lane >> 5;
  f32x2 pv[4];
#pragma unroll
  for (int k = 0; k < 4; ++k) pv[k] = *(LAS f32x2*)(L + L_PST + ((hf * 8 + hh * 4 + k) * 32 + t) * 8);
  SB();
  const f32x2 s = (pv[0] + pv[1]) + (pv[2] + pv[3]);
  const f32x2 rs_ = both_halves(s.x), rq = both_halves(s.y);
  const float mean = (rs_.x + rs_.y) * (1.0f / DM);
  float var = (rq.x + rq.y) * (1.0f / DM) - mean * mean;
  var = var > 0.f ? var : 0.f;
  float r = __builtin_amdgcn_rsqf(var + 1e-5f);
  if (DOUBLE_LN) {
    const float v2 = var * r * r;
    r *= __builtin_amdgcn_rsqf(v2 + 1e-5f);
  }
  const f32x2 ab = {r, -mean * r};
  *(LAS f32x2*)(L + L_FST + (hf * 32 + t) * 8) = ab;
  return ab;
}

__global__ void __launch_bounds__(512, 2) k_main(Params p) {
  extern __shared__ __attribute__((aligned(16))) unsigned char smem[];
  LAS unsigned char* L = (LAS unsigned char*)smem;
  const int b = blockIdx.x, tid = threadIdx.x, lane_ = tid & 63, v_ = __builtin_amdgcn_readfirstlane(tid >> 6);
  int c_, j_;
  c_ = ((b & 7) >> 1) * 8 + (b >> 5); j_ = (b & 1) * 4 + ((b >> 3) & 3);
  const rsrc_t rs = __builtin_amdgcn_make_buffer_rsrc((void*)p.ws, 0, WS_END, 0x00020000);
  const rsrc_t rx = __builtin_amdgcn_make_buffer_rsrc((void*)p.x, 0, SEQ * DM * 2, 0x00020000);
  GAS unsigned* tmo = (GAS unsigned*)(p.ws + CTRL_OFF);
  v2u xr[2]; v4u wq[24], stA[4];
  f32x16 Z;
#pragma unroll
  for (int q = 0; q < 16; ++q) Z[q] = 0.f;
  {
    FRESH();
    const float* cs = (const float*)(p.ws + CS_OFF);
    float k0 = 0.f, k1 = 0.f;
    if (tid < 224) { k0 = cs[1536 + 224 * j + tid]; k1 = bf2f(p.b1[224 * j + tid]); }
    else if (tid >= 256) { const int t = tid - 256; k0 = t < 192 ? cs[(t >> 6) * 512 + 64 * j + (t & 63)] : bf2f(p.b2[64 * j + t - 192]); }
#pragma unroll
    for (int hf = 0; hf < 2; ++hf) xr[hf] = *(const v2u*)(p.x + (size_t)(64 * c + 32 * hf + tk) * DM + 64 * j + 32 * (v & 1) + 8 * (v >> 1) + 4 * hh);
#pragma unroll
    for (int i = 0; i < 4; ++i) stA[i] = __builtin_amdgcn_raw_buffer_load_b128(rx, tk * 1024 + hh * 16 + i * 32, (64 * c) * 1024 + v * 128, 0);
    SB();
    __builtin_amdgcn_s_barrier();
    SB();
    const int wqb = RFL(WQ_OFF + (j * 192 + v * 24) * KB);
#pragma unroll
    for (int i = 0; i < 16; ++i) wq[i] = ldw(rs, vo, wqb, i);
    SB();
    if (tid < 224) { *(LAS float*)(L + L_C1 + tid * 4) = k0; *(LAS float*)(L + L_C1 + 1024 + tid * 4) = k1; }
    else if (tid >= 256) *(LAS float*)(L + L_CQ + (tid - 256) * 4) = k0;
    else if (tid < 228) *(LAS unsigned*)(L + L_ONES + (tid - 224) * 4) = 0x3F803F80u;
    v4u SENT4 = {SENT, SENT, SENT, SENT};
    asm volatile("" : "+v"(SENT4));
#pragma unroll
    for (int hf = 0; hf < 2; ++hf) {
      if (v < 4) st_sc1(SENT4, rs, vo, KV1_OFF + ((4 * j + v) * 64 + 2 * c + hf) * 2048);
      else st_sc1(SENT4, rs, ((4 * j + 2 * ((v - 4) >> 1) + ((lane >> 4) & 1)) * 64) * 2048 + 1024 + (32 * hh + 16 * (v & 1) + (lane & 15)) * 16, KV1_OFF + (2 * c + hf) * 2048);
    }
    SB();
  }

  for (int lay = 0; lay < 2; ++lay) {
    const int KV_OFF = lay ? KV1_OFF : KV0_OFF, OT_OFF = lay ? OT1_OFF : OT0_OFF, X1T_OFF = lay ? X1T1_OFF : X1T0_OFF, HT_OFF = lay ? HT1_OFF : HT0_OFF;
    v4u wo[8], w1[32], w2[28], stB[4];
    {
      FRESH();
      const int xtb = RFL(XT1_OFF + c * TILE_B + v * 4096);
      const int wob = RFL(WO_OFF + (j * 64 + v * 8) * KB);
      const int kh = v & 1;
      f32x16 acc[2];
      auto xb_issue = [&]() {
        if (lay) {
#pragma unroll
          for (int i = 0; i < 4; ++i) stB[i] = ld_seq(rs, vo, xtb + 32768, i);
        } else {
#pragma unroll
          for (int i = 0; i < 4; ++i) stB[i] = __builtin_amdgcn_raw_buffer_load_b128(rx, tk * 1024 + hh * 16 + i * 32, (64 * c + 32) * 1024 + v * 128, 0);
        }
      };
      auto pass_k = [&](int hf) {
        v4u xs[3];
        const int x0 = L_TILE + hf * 32768 + kh * 16384 + vo;
#pragma unroll
        for (int s = 0; s < 2; ++s) xs[s] = LDS4(x0 + s * 1024);
        acc[hf] = Z;
        PRIO(1);
#pragma unroll
        for (int s = 0; s < 16; ++s) {
          if (s + 2 < 16) xs[(s + 2) % 3] = LDS4(x0 + (s + 2) * 1024);
          SB();
          acc[hf] = mfma32(wq[s], xs[s % 3], acc[hf]);
          if (hf == 0 && s == 8) xb_issue();
          SB();
        }
        KEEP(acc[hf]);
        PRIO(0);
      };
      auto pass_v = [&](int hf) {
        v4u xs[3];
        const int x0 = L_TILE + hf * 32768 + kh * 16384 + (vo ^ (kh * 256));
#pragma unroll
        for (int s = 0; s < 2; ++s) xs[s] = LDS4(x0 + s * 1024);
        acc[hf] = Z;
        PRIO(1);
#pragma unroll
        for (int s = 0; s < 16; ++s) {
          if (s + 2 < 16) xs[(s + 2) % 3] = LDS4(x0 + (s + 2) * 1024);
          SB();
          acc[hf] = mfma32(xs[s % 3], wq[s], acc[hf]);
          if (hf == 0 && s == 8) xb_issue();
          SB();
        }
        KEEP(acc[hf]);
        PRIO(0);
      };
      auto send = [&](int hf) {
#pragma unroll
        for (int u = 0; u < 2; ++u) *(LAS f32x4*)(L + (hf ? L_XK1 : L_XCH) + (v * 2 + u) * 1024 + vo) = q4(acc[hf], 2 + u);
      };
      auto epi_k = [&](int hf) {
        const f32x2 ab = final_stats<false>(L, lane, hf);
        f32x16 val;
#pragma unroll
        for (int u = 0; u < 2; ++u) {
          const f32x4 o = LDSF4((hf ? L_XK1 : L_XCH) + ((v ^ 1) * 2 + u) * 1024 + vo);
          const f32x4 csn = LDSF4(L_CQ + (64 + 16 * v + 8 * u + 4 * hh) * 4);
#pragma unroll
          for (int i = 0; i < 4; ++i) val[4 * u + i] = (acc[hf][4 * u + i] + o[i]) * ab.x + csn[i] * ab.y;
        }
        const v4u K = nosent2(pk8(val, 0));
        st_sc1(K, rs, vo, KV_OFF + ((4 * j + v) * 64 + 2 * c + hf) * 2048);
        *(LAS v4u*)(L + L_KV + ((8 + hf) * 4 + v) * 2048 + vo) = K;
      };
      auto epi_v = [&](int hf) {
        const int pp = (v - 4) >> 1;
        (void)final_stats<false>(L, lane, hf);
        const float csv = *(LAS float*)(L + L_CQ + (128 + 32 * pp + tk) * 4);
        f32x16 val;
#pragma unroll
        for (int u = 0; u < 2; ++u) {
          const f32x4 o = LDSF4((hf ? L_XK1 : L_XCH) + ((v ^ 1) * 2 + u) * 1024 + vo);
          const f32x4 t0 = LDSF4(L_FST + (hf * 32 + 16 * kh + 8 * u + 4 * hh) * 8), t1 = LDSF4(L_FST + (hf * 32 + 16 * kh + 8 * u + 4 * hh + 2) * 8);
          val[4 * u + 0] = (acc[hf][4 * u + 0] + o[0]) * t0[0] + csv * t0[1];
          val[4 * u + 1] = (acc[hf][4 * u + 1] + o[1]) * t0[2] + csv * t0[3];
          val[4 * u + 2] = (acc[hf][4 * u + 2] + o[2]) * t1[0] + csv * t1[1];
          val[4 * u + 3] = (acc[hf][4 * u + 3] + o[3]) * t1[2] + csv * t1[3];
        }
        const int hb = (lane >> 4) & 1, d = lane & 15;
        const v4u V = nosent2(pk8(val, 0));
        st_sc1(V, rs, ((4 * j + 2 * pp + hb) * 64) * 2048 + 1024 + (32 * hh + 16 * kh + d) * 16, KV_OFF + (2 * c + hf) * 2048);
        *(LAS v4u*)(L + L_KV + ((8 + hf) * 4 + 2 * pp + hb) * 2048 + 1024 + (32 * hh + 16 * kh + d) * 16) = V;
      };
      if (lay) verify<4>(stA, [&](int i) { return ld_seq(rs, vo, xtb, i); }, tmo, 0x10, lane);
      fill_half<true>(L, 0, v, lane, stA);
      __syncthreads();
      if (v < 4) pass_k(0); else pass_v(0);
      send(0);
      if (lay) verify<4>(stB, [&](int i) { return ld_seq(rs, vo, xtb + 32768, i); }, tmo, 0x11, lane);
      fill_half<true>(L, 1, v, lane, stB);
      __syncthreads();
      {
        const int wqb = RFL(WQ_OFF + (j * 192 + v * 24) * KB);
#pragma unroll
        for (int i = 16; i < 24; ++i) wq[i] = ldw(rs, vo, wqb, i);
        SB();
      }
      if (v < 4) { epi_k(0); pass_k(1); } else { pass_v(1); epi_v(0); }
      send(1);
      __syncthreads();
      if (v < 4) epi_k(1); else epi_v(1);
      {
        const int ho = (v >> 1) & 1;
        const int x0 = L_TILE + ho * 32768 + (v >> 1) * 8192 + vo, x1 = L_TILE + (1 - ho) * 32768 + (v >> 1) * 8192 + vo;
        v4u xs0[3], xs1[3];
#pragma unroll
        for (int s = 0; s < 2; ++s) { xs0[s] = LDS4(x0 + s * 1024); xs1[s] = LDS4(x1 + s * 1024); }
        acc[0] = Z; acc[1] = Z;
        PRIO(1);
#pragma unroll
        for (int s = 0; s < 8; ++s) {
          if (s + 2 < 8) { xs0[(s + 2) % 3] = LDS4(x0 + (s + 2) * 1024); xs1[(s + 2) % 3] = LDS4(x1 + (s + 2) * 1024); }
          SB();
          acc[0] = mfma32(wq[16 + s], xs0[s % 3], acc[0]); acc[1] = mfma32(wq[16 + s], xs1[s % 3], acc[1]);
          wo[s] = ldw(rs, vo, wob, s);
          SB();
        }
        KEEP(acc[0]); KEEP(acc[1]);
        PRIO(0);
#pragma unroll
        for (int u = 0; u < 2; ++u) {
          *(LAS f32x4*)(L + L_XCH + (((v ^ 4) * 3 + 0) * 2 + u) * 1024 + vo) = q4(acc[0], 2 + u);
          *(LAS f32x4*)(L + L_XCH + (((v ^ 2) * 3 + 1) * 2 + u) * 1024 + vo) = q4(acc[1], u);
          *(LAS f32x4*)(L + L_XCH + (((v ^ 6) * 3 + 2) * 2 + u) * 1024 + vo) = q4(acc[1], 2 + u);
        }
      }
      v4u kvn[8];
      auto kv_load = [&](int i) {
        const int kt = 2 * c - 8 + 4 * (v >> 2) + (i >> 1);
        return ld_sc1(rs, vo + (i & 1) * 1024, RFL(kt >= 0 ? KV_OFF + ((4 * j + (v & 3)) * 64 + kt) * 2048 : OOB));
      };
      auto kv_issue = [&]() {
#pragma unroll
        for (int i = 0; i < 8; ++i) kvn[i] = kv_load(i);
      };
      WAITV0();
      __syncthreads();
      kv_issue();
      {
        const int ho = (v >> 1) & 1, hl = 2 * (v & 1) + (v >> 2);
        const f32x2 ab = *(LAS f32x2*)(L + L_FST + (ho * 32 + tk) * 8);
        const float cexp = 0.25f * 1.4426950408889634f;
        f32x16 val;
#pragma unroll
        for (int u = 0; u < 2; ++u) {
          f32x4 o = q4(acc[0], u);
#pragma unroll
          for (int k = 0; k < 3; ++k) o += LDSF4(L_XCH + ((v * 3 + k) * 2 + u) * 1024 + vo);
          const f32x4 csn = LDSF4(L_CQ + (16 * hl + 8 * u + 4 * hh) * 4);
#pragma unroll
          for (int i = 0; i < 4; ++i) val[4 * u + i] = o[i] * (ab.x * cexp) + csn[i] * (ab.y * cexp);
        }
        *(LAS v4u*)(L + L_Q + (hl * 2 + ho) * 1024 + vo) = pk8(val, 0);
      }
      const int w1b = RFL(v < 7 ? W1_OFF + (j * 224 + v * 32) * KB : OOB);
#pragma unroll
      for (int i = 0; i < 4; ++i) w1[i] = ldw(rs, vo, w1b, i);
      SB();
      verify<8>(kvn, kv_load, tmo, 0x20, lane);
      SB();
#pragma unroll
      for (int i = 0; i < 4; ++i) {
        *(LAS v4u*)(L + L_KV + ((4 * (v >> 2) + i) * 4 + (v & 3)) * 2048 + vo) = kvn[2 * i];
        *(LAS v4u*)(L + L_KV + ((4 * (v >> 2) + i) * 4 + (v & 3)) * 2048 + 1024 + vo) = kvn[2 * i + 1];
      }
      __syncthreads();
    }
    {
      FRESH();
      const int hq = v & 3, r = v >> 2, tq = tk - 4 * hh;
      const int kb = L_KV + (r * 4 + hq) * 2048;
      const v4u Q = LDS4(L_Q + (hq * 2 + r) * 1024 + vo);
      v4u Kf[9];
#pragma unroll
      for (int i = 0; i < 9; ++i) Kf[i] = LDS4(kb + i * 8192 + vo);
      const bool ones_lane = (lane & 16) != 0;
      const unsigned vsel = ones_lane ? 0u : 1u, vbase = ones_lane ? (unsigned)L_ONES : (unsigned)(kb + 1024 + (32 * hh + (lane & 15)) * 16);
      auto vop = [&](int i, int s) -> v4u { return LDS4(vbase + vsel * (unsigned)(i * 8192 + s * 256)); };
      const int first_ok = 8 - r - 2 * c;
      v4u SENT4 = {SENT, SENT, SENT, SENT};
      asm volatile("" : "+v"(SENT4));
      const int tsl = (8 * j + 4 * (v & 1) + (v >> 1)) * 512 + tk * 16 + hh * 8;
#pragma unroll
      for (int hf = 0; hf < 2; ++hf) __builtin_amdgcn_raw_buffer_store_b64(v2u{SENT4.x, SENT4.y}, rs, tsl + X1T_OFF + c * TILE_B + hf * 32768, 0, AUX_SC1);
      const int hcb = RFL(v < 7 ? HT_OFF + c * HT_C + (28 * j + 4 * v) * 512 : OOB);
      const int xcb = RFL(lay == 0 ? XT1_OFF + c * TILE_B : OOB), ocb = RFL(lay == 0 ? OT1_OFF + c * TILE_B + (v >> 2) * 32768 + (2 * (4 * j + (v & 3))) * 512 : OOB);
      auto clean = [&](int i) {
        if (i < 4) st_sc1(SENT4, rs, vo, hcb + (i >> 1) * 114688 + 2 * (i & 1) * 512);
        else if (i < 6) __builtin_amdgcn_raw_buffer_store_b64(v2u{SENT4.x, SENT4.y}, rs, tsl + xcb + (i - 4) * 32768, 0, AUX_SC1);
        else st_sc1(SENT4, rs, vo, ocb);
      };
      SB();
      const int w1b = RFL(v < 7 ? W1_OFF + (j * 224 + v * 32) * KB : OOB);
      auto tmax = [&](const f32x16& x, float m) {
        const float a = max3f(x[0], x[1], x[2]), b2_ = max3f(x[3], x[4], x[5]), c2 = max3f(x[6], x[7], x[8]), d = max3f(x[9], x[10], x[11]), e = max3f(x[12], x[13], x[14]);
        return max3f(max3f(a, b2_, x[15]), max3f(c2, d, e), m);
      };
      f32x16 Sm, S[2];
      {
        f32x16 Sa = mfma32(Kf[0], Q, Z);
        const f32x16 Sb = mfma32(Kf[8], Q, Z);
        S[1] = mfma32(Kf[1], Q, Z);
        if (first_ok > 0) {
#pragma unroll
          for (int q = 0; q < 16; ++q) Sa[q] = -1e30f;
        }
#pragma unroll
        for (int q = 0; q < 16; ++q) { const int row = (q & 3) + 8 * (q >> 2); Sm[q] = row <= tq ? Sb[q] : Sa[q]; }
      }
      float mx = tmax(Sm, -1e30f);
      SB();
#pragma unroll
      for (int i = 1; i < 8; ++i) {
        if (i + 1 < 8) S[(i + 1) & 1] = mfma32(Kf[i + 1], Q, Z);
        if (i >= first_ok) mx = tmax(S[i & 1], mx);
        if (i < 6) w1[4 + (i - 1)] = ldw(rs, vo, w1b, 4 + (i - 1));
        SB();
      }
      { const f32x2 rr = both_halves(mx); mx = max3f(rr.x, rr.y, rr.y); }
      f32x16 Cm;
#pragma unroll
      for (int q = 0; q < 16; ++q) Cm[q] = -mx;
      S[1] = mfma32(Kf[1], Q, Cm);
      f32x16 O;
      {
        const v4u vn0 = vop(8, 0), vn1 = vop(8, 1), vo0 = vop(0, 0), vo1 = vop(0, 1);
        unsigned pp[8];
#pragma unroll
        for (int q = 0; q < 8; ++q) pp[q] = pk(__builtin_amdgcn_exp2f(Sm[2 * q] - mx), __builtin_amdgcn_exp2f(Sm[2 * q + 1] - mx));
        v4u pn[2], po[2];
#pragma unroll
        for (int q = 0; q < 8; ++q) {
          const int row0 = ((2 * q) & 3) + 8 * ((2 * q) >> 2);
          const unsigned mk = (row0 <= tq ? 0xFFFFu : 0u) | (row0 + 1 <= tq ? 0xFFFF0000u : 0u);
          pn[q >> 2][q & 3] = pp[q] & mk; po[q >> 2][q & 3] = pp[q] & ~mk;
        }
        O = mfma32(vn0, pn[0], Z); O = mfma32(vn1, pn[1], O);
        O = mfma32(vo0, po[0], O); O = mfma32(vo1, po[1], O);
        SB();
      }
#pragma unroll
      for (int i = 1; i < 8; ++i) {
        if (i + 1 < 8) S[(i + 1) & 1] = mfma32(Kf[i + 1], Q, Cm);
        const v4u va0 = vop(i, 0), va1 = vop(i, 1);
        if (i >= first_ok) {
          const f32x16& cur = S[i & 1];
          v4u pp[2];
#pragma unroll
          for (int q = 0; q < 8; ++q) pp[q >> 2][q & 3] = pk(__builtin_amdgcn_exp2f(cur[2 * q]), __builtin_amdgcn_exp2f(cur[2 * q + 1]));
          O = mfma32(va0, pp[0], O); O = mfma32(va1, pp[1], O);
        }
        w1[9 + (i - 1)] = ldw(rs, vo, w1b, 9 + (i - 1));
        clean(i - 1);
        SB();
      }
      KEEP(O);
      const float rl = __builtin_amdgcn_rcpf(O[8]);
      asm volatile("s_waitcnt vmcnt(19)" ::: "memory");
      st_sc1(nosent2(swap_T8(pk8(O, 0, rl))), rs, vo, OT_OFF + c * TILE_B + r * 32768 + (2 * (4 * j + hq)) * 512);
      if (v < 4) WAITV0();
    }
    {
      FRESH();
      const int otb = RFL(OT_OFF + c * TILE_B + v * 4096);
      const int w1b = RFL(v < 7 ? W1_OFF + (j * 224 + v * 32) * KB : OOB);
      auto oa_issue = [&]() {
#pragma unroll
        for (int i = 0; i < 4; ++i) stA[i] = ld_seq(rs, vo, otb, i);
      };
      auto ob_issue = [&]() {
#pragma unroll
        for (int i = 0; i < 4; ++i) stB[i] = ld_seq(rs, vo, otb + 32768, i);
      };
      f32x16 a;
      auto pass = [&](int hf) {
        const int x0 = L_TILE + hf * 32768 + (v >> 1) * 8192 + vo;
        v4u xs[8];
#pragma unroll
        for (int s = 0; s < 8; ++s) xs[s] = LDS4(x0 + s * 1024);
        a = Z;
#pragma unroll
        for (int s = 0; s < 8; ++s) a = mfma32(wo[s], xs[s], a);
        KEEP(a);
#pragma unroll
        for (int u = 1; u < 4; ++u) *(LAS f32x4*)(L + L_XCH + ((hf * 8 + (v ^ (2 * u))) * 3 + (u - 1)) * 1024 + vo) = q4(a, u);
      };
      auto epi = [&](int hf) {
        f32x4 o = q4(a, 0);
#pragma unroll
        for (int k = 0; k < 3; ++k) o += LDSF4(L_XCH + ((hf * 8 + v) * 3 + k) * 1024 + vo);
        xr[hf] = pk4(unpk4(xr[hf]) + o);
        __builtin_amdgcn_raw_buffer_store_b64(v2u{nosent(xr[hf].x), xr[hf].y}, rs, (8 * j + 4 * (v & 1) + (v >> 1)) * 512 + tk * 16 + hh * 8 + X1T_OFF + c * TILE_B + hf * 32768, 0, AUX_SC1);
      };
      oa_issue();
#pragma unroll
      for (int i = 16; i < 24; ++i) w1[i] = ldw(rs, vo, w1b, i);
      SB();
      verify<4>(stA, [&](int i) { return ld_seq(rs, vo, otb, i); }, tmo, 0x30, lane);
      __syncthreads();
      fill_half<false>(L, 0, v, lane, stA);
      ob_issue();
      __syncthreads();
      pass(0);
      verify<4>(stB, [&](int i) { return ld_seq(rs, vo, otb + 32768, i); }, tmo, 0x31, lane);
      fill_half<false>(L, 1, v, lane, stB);
      __syncthreads();
      epi(0);
      pass(1);
#pragma unroll
      for (int i = 24; i < 32; ++i) w1[i] = ldw(rs, vo, w1b, i);
      __syncthreads();
#pragma unroll
      for (int i = 0; i < 4; ++i) stA[i] = ld_seq(rs, vo, RFL(X1T_OFF + c * TILE_B + v * 4096), i);
      SB();
      epi(1);
    }
    v4u hA[14];
    {
      FRESH();
      const int x1b = RFL(X1T_OFF + c * TILE_B + v * 4096);
      const int w2b = RFL(W2_OFF + (j * 224 + v * 28) * KB);
      const int hb = RFL(v < 7 ? HT_OFF + c * HT_C + (28 * j + 4 * v) * 512 : OOB);
      auto xb_issue = [&]() {
#pragma unroll
        for (int i = 0; i < 4; ++i) stB[i] = ld_seq(rs, vo, x1b + 32768, i);
      };
      f32x16 a;
      auto pass = [&](int hf) {
        v4u xs[3];
        const int x0 = L_TILE + hf * 32768 + vo;
#pragma unroll
        for (int s = 0; s < 2; ++s) xs[s] = LDS4(x0 + s * 1024);
        a = Z;
        PRIO(1);
#pragma unroll
        for (int s = 0; s < 32; ++s) {
          if (s + 2 < 32) xs[(s + 2) % 3] = LDS4(x0 + (s + 2) * 1024);
          SB();
          a = mfma32(w1[s], xs[s % 3], a);
          if (hf == 0 && s == 8) xb_issue();
          if (hf == 0 && s >= 22) w2[s - 22] = ldw(rs, vo, w2b, s - 22);
          if (hf == 1 && s >= 2 && s < 20) w2[10 + (s - 2)] = ldw(rs, vo, w2b, 10 + (s - 2));
          SB();
        }
        KEEP(a);
        PRIO(0);
      };
      v4u ho[2];
      auto gelu = [&](int hf) {
        const f32x2 ab = final_stats<true>(L, lane, hf);
        const float c1 = -2.0f * 0.7978845608028654f * 1.4426950408889634f, c2 = c1 * 0.044715f;
#pragma unroll
        for (int u2 = 0; u2 < 2; ++u2) {
          f32x16 hv;
#pragma unroll
          for (int u = 2 * u2; u < 2 * u2 + 2; ++u) {
            const f32x4 csn = LDSF4(L_C1 + (32 * v + 8 * u + 4 * hh) * 4), bn = LDSF4(L_C1 + 1024 + (32 * v + 8 * u + 4 * hh) * 4);
#pragma unroll
            for (int i = 0; i < 4; ++i) {
              const float z = a[4 * u + i] * ab.x + (csn[i] * ab.y + bn[i]);
              const float e = __builtin_amdgcn_exp2f(z * (z * z * c2 + c1));
              hv[4 * u + i] = z * __builtin_amdgcn_rcpf(1.0f + e);
            }
          }
          ho[u2] = nosent2(swap_T8(pk8(hv, 8 * u2)));
          if (hf == 0) st_sc1(ho[u2], rs, vo, hb + 2 * u2 * 512);
        }
      };
      verify<4>(stA, [&](int i) { return ld_seq(rs, vo, x1b, i); }, tmo, 0x40, lane);
      fill_half<true>(L, 0, v, lane, stA);
      __syncthreads();
      pass(0);
      gelu(0);
      verify<4>(stB, [&](int i) { return ld_seq(rs, vo, x1b + 32768, i); }, tmo, 0x41, lane);
      fill_half<true>(L, 1, v, lane, stB);
      __syncthreads();
      pass(1);
#pragma unroll
      for (int s = 0; s < 14; ++s) hA[s] = ld_seq(rs, vo, RFL(HT_OFF + c * HT_C + 28 * v * 512), s);
      SB();
      gelu(1);
#pragma unroll
      for (int u2 = 0; u2 < 2; ++u2) st_sc1(ho[u2], rs, vo, hb + 114688 + 2 * u2 * 512);
    }
    {
      FRESH();
      const int htb = RFL(HT_OFF + c * HT_C + 28 * v * 512);
      const int wqb = RFL(lay == 0 ? WQ_OFF + (j * 192 + v * 24) * KB : OOB);
      const int xnb = RFL(lay == 0 ? XT1_OFF + c * TILE_B + v * 4096 : OOB);
#pragma unroll
      for (int hf = 0; hf < 2; ++hf) {
        verify<14>(hA, [&](int i) { return ld_seq(rs, vo, htb + hf * 114688, i); }, tmo, 0x50 + hf, lane);
        if (hf == 0) __syncthreads();
        f32x16 ac[2] = {Z, Z};
        PRIO(1);
#pragma unroll
        for (int s = 0; s < 14; ++s) {
          ac[0] = mfma32(w2[2 * s], hA[s], ac[0]);
          ac[1] = mfma32(w2[2 * s + 1], hA[s], ac[1]);
          if (hf == 1) wq[s] = ldw(rs, vo, wqb, s);
          else hA[s] = ld_seq(rs, vo, htb + 114688, s);
          SB();
        }
        KEEP(ac[0]); KEEP(ac[1]);
        PRIO(0);
        LAS unsigned char* R = L + (hf ? L_TILE : L_RB);
#pragma unroll
        for (int nt = 0; nt < 2; ++nt)
#pragma unroll
          for (int u = 0; u < 4; ++u) *(LAS f32x4*)(R + ((nt + 2 * u) * 8 + v) * 1024 + vo) = q4(ac[nt], u);
        if (hf == 1) { wq[14] = ldw(rs, vo, wqb, 14); wq[15] = ldw(rs, vo, wqb, 15); }
        __syncthreads();
        f32x4 s0 = {0, 0, 0, 0};
#pragma unroll
        for (int k = 0; k < 8; ++k) s0 += *(LAS f32x4*)(R + (v * 8 + k) * 1024 + vo);
        const f32x4 b0 = LDSF4(L_CQ + (192 + 32 * (v & 1) + 8 * (v >> 1) + 4 * hh) * 4);
        xr[hf] = pk4(unpk4(xr[hf]) + s0 + b0);
        if (hf == 1) {
#pragma unroll
          for (int i = 0; i < 4; ++i) stA[i] = ld_seq(rs, vo, xnb, i);
          SB();
        }
        if (lay == 0) {
          __builtin_amdgcn_raw_buffer_store_b64(v2u{nosent(xr[hf].x), xr[hf].y}, rs, (8 * j + 4 * (v & 1) + (v >> 1)) * 512 + tk * 16 + hh * 8 + XT1_OFF + c * TILE_B + hf * 32768, 0, AUX_SC1);
        } else {
          *(v2u*)(p.out + (size_t)(64 * c + 32 * hf + tk) * DM + 64 * j + 32 * (v & 1) + 8 * (v >> 1) + 4 * hh) = xr[hf];
        }
      }
      __syncthreads();
    }
  }
}
}

extern "C" void kernel_launch(void* const* d_in, const int* in_sizes, int n_in, void* d_out, int out_size, void* d_ws, size_t ws_size,
                              hipStream_t stream) {
  using namespace fk;
  Params p;
  p.x = (const u16*)d_in[0]; p.wqkv = (const u16*)d_in[1]; p.wo = (const u16*)d_in[2];
  p.w1 = (const u16*)d_in[4]; p.b1 = (const u16*)d_in[5]; p.w2 = (const u16*)d_in[6]; p.b2 = (const u16*)d_in[7];
  p.out = (u16*)d_out; p.ws = (unsigned char*)d_ws;
  (void)hipFuncSetAttribute((const void*)k_main, hipFuncAttributeMaxDynamicSharedMemorySize, L_BYTES);
  k_prep<<<PREP_BLOCKS, 256, 0, stream>>>(p);
  k_main<<<256, 512, L_BYTES, stream>>>(p);
}
```

```cpp
#include <hip/hip_runtime.h>
#include <stdint.h>

namespace fk {
typedef unsigned short u16;
typedef unsigned v4u __attribute__((ext_vector_type(4)));
typedef unsigned v2u __attribute__((ext_vector_type(2)));
typedef float f32x4 __attribute__((ext_vector_type(4)));
typedef float f32x2 __attribute__((ext_vector_type(2)));
typedef float f32x16 __attribute__((ext_vector_type(16)));
typedef __bf16 bf8v __attribute__((ext_vector_type(8)));
typedef __bf16 bf2v __attribute__((ext_vector_type(2)));
#define LAS __attribute__((address_space(3)))
#define GAS __attribute__((address_space(1)))
typedef __amdgpu_buffer_rsrc_t rsrc_t;

constexpr int SEQ = 2048, DM = 512, FF = 1792;
constexpr int KB = 1024;
constexpr int WQ_OFF = 0;
constexpr int WO_OFF = WQ_OFF + 8 * 192 * KB;
constexpr int W1_OFF = WO_OFF + 8 * 64 * KB;
constexpr int W2_OFF = W1_OFF + 8 * 224 * KB;
constexpr int CS_OFF = W2_OFF + 8 * 224 * KB;
constexpr int CTRL_OFF = CS_OFF + 16 * KB;
constexpr int TILE_B = 64 * KB;
constexpr int KV_B = 32 * 64 * 2 * KB;
constexpr int HT_C = 2 * 224 * 512;
constexpr int KV0_OFF = CTRL_OFF + 16 * KB;
constexpr int OT0_OFF = KV0_OFF + KV_B;
constexpr int KV1_OFF = OT0_OFF + 32 * TILE_B;
constexpr int OT1_OFF = KV1_OFF + KV_B;
constexpr int X1T0_OFF = OT1_OFF + 32 * TILE_B;
constexpr int X1T1_OFF = X1T0_OFF + 32 * TILE_B;
constexpr int XT1_OFF = X1T1_OFF + 32 * TILE_B;
constexpr int HT0_OFF = XT1_OFF + 32 * TILE_B;
constexpr int HT1_OFF = HT0_OFF + 32 * HT_C;
constexpr int STAMP_OFF = HT1_OFF + 32 * HT_C;
constexpr int WS_END = STAMP_OFF + 256 * 8 * 128 * 8;
constexpr unsigned SENT = 0xFFFFFFFFu;
constexpr int AUX_SC1 = 16;
constexpr unsigned SPIN_CAP = 1u << 14;

constexpr int L_TILE = 0;
constexpr int L_KV = 0;
constexpr int L_RB = 64 * KB;
constexpr int L_Q = 80 * KB;
constexpr int L_XK1 = 88 * KB;
constexpr int L_XCH = 104 * KB;
constexpr int L_PST = 152 * KB;
constexpr int L_FST = 156 * KB;
constexpr int L_C1 = 156 * KB + 512;
constexpr int L_CQ = 158 * KB + 512;
constexpr int L_ONES = 159 * KB + 512;
constexpr int L_BYTES = 159 * KB + 576;

__device__ __forceinline__ float bf2f(unsigned b) { return __builtin_bit_cast(float, b << 16); }
__device__ __forceinline__ float bflo(unsigned d) { return __builtin_bit_cast(float, d << 16); }
__device__ __forceinline__ float bfhi(unsigned d) { return __builtin_bit_cast(float, d & 0xFFFF0000u); }
__device__ __forceinline__ unsigned pk(float lo, float hi) {
  bf2v r = {(__bf16)lo, (__bf16)hi};
  return __builtin_bit_cast(unsigned, r);
}
__device__ __forceinline__ f32x4 mfma16(v4u a, v4u b, f32x4 c) {
  return __builtin_amdgcn_mfma_f32_16x16x32_bf16(__builtin_bit_cast(bf8v, a), __builtin_bit_cast(bf8v, b), c, 0, 0, 0);
}
__device__ __forceinline__ unsigned nosent(unsigned d) { return d < 0xFFFFFFFEu ? d : 0xFFFFFFFEu; }
__device__ __forceinline__ v4u nosent4(v4u v) { return v4u{nosent(v.x), nosent(v.y), nosent(v.z), nosent(v.w)}; }
__device__ __forceinline__ v4u pack_T(f32x4 a, f32x4 b) {
  unsigned x0 = pk(a[0], a[1]), x1 = pk(a[2], a[3]), y0 = pk(b[0], b[1]), y1 = pk(b[2], b[3]);
  auto r0 = __builtin_amdgcn_permlane16_swap(x0, y0, false, false);
  auto r1 = __builtin_amdgcn_permlane16_swap(x1, y1, false, false);
  return v4u{r0[0], r1[0], r0[1], r1[1]};
}
__device__ __forceinline__ v2u pk4(f32x4 a) { return v2u{pk(a[0], a[1]), pk(a[2], a[3])}; }
__device__ __forceinline__ f32x4 unpk4(v2u v) { return f32x4{bflo(v.x), bfhi(v.x), bflo(v.y), bfhi(v.y)}; }
__device__ __forceinline__ v4u swap_T(v2u x, v2u y) {
  auto r0 = __builtin_amdgcn_permlane16_swap(x.x, y.x, false, false);
  auto r1 = __builtin_amdgcn_permlane16_swap(x.y, y.y, false, false);
  return v4u{r0[0], r1[0], r0[1], r1[1]};
}
#define KEEP(x) asm volatile("" : "+v"(x))
#define WAITV0() asm volatile("s_waitcnt vmcnt(0)" ::: "memory")
#define PRIO(x) __builtin_amdgcn_s_setprio(x)
#define SB() __builtin_amdgcn_sched_barrier(0)

__device__ __forceinline__ v4u ldw(rsrc_t rs, int voff, int sbase, int i) {
  return __builtin_amdgcn_raw_buffer_load_b128(rs, voff + (i & 3) * 1024, sbase + (i >> 2) * 4096, 0);
}
__device__ __forceinline__ v4u ld_sc1(rsrc_t rs, int voff, int soff) {
  return __builtin_amdgcn_raw_buffer_load_b128(rs, voff, soff, AUX_SC1); }
__device__ __forceinline__ v4u ld_seq(rsrc_t rs, int voff, int sbase, int i) { return ld_sc1(rs, voff + (i & 3) * 1024, sbase + (i >> 2) * 4096); }
#define RFL(x) __builtin_amdgcn_readfirstlane(x)
__device__ __forceinline__ void st_sc1(v4u v, rsrc_t rs, int voff, int soff) {
  __builtin_amdgcn_raw_buffer_store_b128(v, rs, voff + soff, 0, AUX_SC1); }
__device__ __forceinline__ unsigned max4(unsigned m, v4u d) {
  m = __builtin_elementwise_max(__builtin_elementwise_max(m, d.x), d.y);
  return __builtin_elementwise_max(__builtin_elementwise_max(m, d.z), d.w);
}
__device__ __forceinline__ bool spin_fail(unsigned& spins, GAS unsigned* tmo, unsigned code, int lane) {
  ++spins;
  if (spins > SPIN_CAP) {
    if (lane == 0) __hip_atomic_store(tmo, code, __ATOMIC_RELAXED, __HIP_MEMORY_SCOPE_AGENT);
    return true;
  }
  if ((spins & 63u) == 0u && __hip_atomic_load(tmo, __ATOMIC_RELAXED, __HIP_MEMORY_SCOPE_AGENT) != 0u) return true;
  return false;
}

struct Params {
  const u16* x;
  const u16* wqkv;
  const u16* wo;
  const u16* w1;
  const u16* b1;
  const u16* w2;
  const u16* b2;
  u16* out;
  unsigned char* ws;
};

constexpr int NFRAG = 8 * 704;
constexpr int PB_TILE = NFRAG * 16 / 256;
constexpr int PB_CS = (1536 + 1792) / 64;
constexpr int CLEAN_B = KV_B + 32 * TILE_B;
constexpr int PB_CLEAN = CLEAN_B / (256 * 64);
constexpr int PREP_BLOCKS = PB_TILE + PB_CS + PB_CLEAN;

__global__ void __launch_bounds__(256) k_prep(Params p) {
  const int b = blockIdx.x, tid = threadIdx.x;
  if (b < PB_TILE) {
    const int F = b * 16 + (tid >> 4), t16 = tid & 15, cq = t16 & 7, h = t16 >> 3;
    const u16* W; int ldw_, k0, col;
    if (F < 8 * 192) {
      const int j = F / 192, v = (F % 192) / 24, i = F % 24;
      W = p.wqkv; ldw_ = 1536;
      if (i < 16) {
        k0 = 16 * (16 * (v & 1) + i);
        col = v < 4 ? 512 + (4 * j + 2 * (v >> 1)) * 16 + 4 * (cq ^ (4 * (v & 1))) : 1024 + (4 * j + 2 * ((v - 4) >> 1)) * 16 + 4 * cq;
      } else {
        k0 = 16 * (8 * (v >> 1) + i - 16);
        col = (4 * j + 2 * (v & 1)) * 16 + 4 * (cq ^ (4 * (v >> 2)));
      }
    } else if (F < 8 * 256) {
      const int f = F - 8 * 192, j = f >> 6, u = f & 63, v = u >> 3;
      W = p.wo; ldw_ = 512; k0 = 16 * (8 * (v >> 1) + (u & 7)); col = 64 * j + 32 * (v & 1) + 4 * (cq ^ (2 * (v >> 1)));
    } else if (F < 8 * 480) {
      const int f = F - 8 * 256, j = f / 224, u = f % 224;
      W = p.w1; ldw_ = 1792; k0 = 16 * (u & 31); col = 224 * j + 32 * (u >> 5) + 4 * cq;
    } else {
      const int f = F - 8 * 480, j = f / 224, u = f % 224, v = u / 28, q = u % 28;
      W = p.w2; ldw_ = 512; k0 = 224 * v + 16 * (q >> 1); col = 64 * j + 32 * (q & 1) + 4 * cq;
    }
    const u16* src = W + (size_t)(k0 + 8 * h) * ldw_ + col;
    v2u r[8];
#pragma unroll
    for (int e = 0; e < 8; ++e) r[e] = *(const v2u*)(src + (size_t)e * ldw_);
    v4u o[4];
#pragma unroll
    for (int e = 0; e < 4; ++e) {
      const unsigned a0 = r[2 * e].x, a1 = r[2 * e + 1].x, c0 = r[2 * e].y, c1 = r[2 * e + 1].y;
      o[0][e] = (a0 & 0xFFFFu) | (a1 << 16);
      o[1][e] = (a0 >> 16) | (a1 & 0xFFFF0000u);
      o[2][e] = (c0 & 0xFFFFu) | (c1 << 16);
      o[3][e] = (c0 >> 16) | (c1 & 0xFFFF0000u);
    }
    v4u* dst = (v4u*)(p.ws + (size_t)F * 1024 + (h * 32 + 4 * cq) * 16);
#pragma unroll
    for (int i = 0; i < 4; ++i) dst[i] = o[i];
  } else if (b < PB_TILE + PB_CS) {
    __shared__ float red[16][64];
    const int cb = b - PB_TILE;
    const u16* W; int ldw_, col;
    if (cb < 24) { W = p.wqkv; ldw_ = 1536; col = cb * 64; } else { W = p.w1; ldw_ = 1792; col = (cb - 24) * 64; }
    const int cq = tid & 15, rg = tid >> 4;
    float s0 = 0, s1 = 0, s2 = 0, s3 = 0;
    const u16* src = W + (size_t)(rg * 32) * ldw_ + col + 4 * cq;
#pragma unroll
    for (int e = 0; e < 32; ++e) {
      const v2u v = *(const v2u*)(src + (size_t)e * ldw_);
      s0 += bflo(v.x); s1 += bfhi(v.x); s2 += bflo(v.y); s3 += bfhi(v.y);
    }
    red[rg][4 * cq + 0] = s0; red[rg][4 * cq + 1] = s1; red[rg][4 * cq + 2] = s2; red[rg][4 * cq + 3] = s3;
    __syncthreads();
    if (tid < 64) {
      float s = 0;
#pragma unroll
      for (int r = 0; r < 16; ++r) s += red[r][tid];
      ((float*)(p.ws + CS_OFF))[cb * 64 + tid] = s;
    }
    if (cb == 0 && tid == 0) *(unsigned*)(p.ws + CTRL_OFF) = 0u;
  } else {
    const int cb = b - PB_TILE - PB_CS;
    v4u* dst = (v4u*)(p.ws + KV0_OFF + (size_t)cb * (256 * 64)) + tid;
    const v4u s = {SENT, SENT, SENT, SENT};
#pragma unroll
    for (int i = 0; i < 4; ++i) dst[i * 256] = s;
  }
}

#define STAMP(i) do {} while (0)
#define STAMP_FLUSH() do {} while (0)

__device__ __forceinline__ float max3f(float a, float b, float c) { float r; asm("v_max3_f32 %0, %1, %2, %3" : "=v"(r) : "v"(a), "v"(b), "v"(c)); return r; }
__device__ __forceinline__ unsigned max3u(unsigned a, unsigned b, unsigned c) { unsigned r; asm("v_max3_u32 %0, %1, %2, %3" : "=v"(r) : "v"(a), "v"(b), "v"(c)); return r; }
__device__ __forceinline__ f32x16 mfma32(v4u a, v4u b, f32x16 c) {
  return __builtin_amdgcn_mfma_f32_32x32x16_bf16(__builtin_bit_cast(bf8v, a), __builtin_bit_cast(bf8v, b), c, 0, 0, 0);
}
template <int N> __device__ __forceinline__ bool any_sent(const v4u (&d)[N]) {
  unsigned m0 = 0, m1 = 0;
#pragma unroll
  for (int i = 0; i < N; ++i) { if (i & 1) m1 = max3u(m1, d[i].x, d[i].z); else m0 = max3u(m0, d[i].x, d[i].z); }
  return __any(max3u(m0, m1, 0) == SENT);
}
template <int N> __device__ __forceinline__ void issue(v4u (&d)[N], rsrc_t rs, int voff, int soff, int stride) {
#pragma unroll
  for (int i = 0; i < N; ++i) d[i] = ld_sc1(rs, voff, soff + i * stride);
}
template <int N, class F> __device__ __forceinline__ void verify(v4u (&d)[N], F load_i, GAS unsigned* tmo, unsigned code, int lane) {
  if (__builtin_expect(any_sent<N>(d), 0)) {
    unsigned spins = 0;
    do {
      __builtin_amdgcn_s_sleep(1);
      asm volatile("" ::: "memory");
#pragma unroll
      for (int i = 0; i < N; ++i)
        if (__any(max3u(d[i].x, d[i].z, 0u) == SENT)) d[i] = load_i(i);
    } while (any_sent<N>(d) && !spin_fail(spins, tmo, code, lane));
  }
}
__device__ __forceinline__ f32x2 both_halves(float v) {
  const unsigned a = __builtin_bit_cast(unsigned, v);
  auto r = __builtin_amdgcn_permlane32_swap(a, a, false, false);
  const unsigned r0 = r[0], r1 = r[1];
  return f32x2{__builtin_bit_cast(float, r0), __builtin_bit_cast(float, r1)};
}
__device__ __forceinline__ float dot2(unsigned a, unsigned b, float c) {
  return __builtin_amdgcn_fdot2_f32_bf16(__builtin_bit_cast(bf2v, a), __builtin_bit_cast(bf2v, b), c, false);
}
__device__ __forceinline__ v4u nosent2(v4u v) { return v4u{nosent(v.x), v.y, nosent(v.z), v.w}; }
__device__ __forceinline__ f32x4 round4(f32x4 s) {
  const unsigned u0 = pk(s[0], s[1]), u1 = pk(s[2], s[3]);
  return f32x4{bflo(u0), bfhi(u0), bflo(u1), bfhi(u1)};
}

__device__ __forceinline__ v4u pk8(const f32x16& a, int r0, float sc = 1.0f) {
  return v4u{pk(a[r0] * sc, a[r0 + 1] * sc), pk(a[r0 + 2] * sc, a[r0 + 3] * sc), pk(a[r0 + 4] * sc, a[r0 + 5] * sc), pk(a[r0 + 6] * sc, a[r0 + 7] * sc)};
}
__device__ __forceinline__ v4u swap_T8(v4u p) {
  auto r0 = __builtin_amdgcn_permlane32_swap(p[0], p[2], false, false);
  auto r1 = __builtin_amdgcn_permlane32_swap(p[1], p[3], false, false);
  return v4u{r0[0], r1[0], r0[1], r1[1]};
}
__device__ __forceinline__ f32x4 q4(const f32x16& a, int u) { return f32x4{a[4 * u], a[4 * u + 1], a[4 * u + 2], a[4 * u + 3]}; }
#define FRESH() int lane = lane_, c = c_, j = j_, v = v_; asm volatile("" : "+v"(lane), "+s"(c), "+s"(j), "+s"(v)); \
  const int vo = lane * 16, tk = lane & 31, hh = lane >> 5; (void)vo; (void)tk; (void)hh
#define LDS4(off) (*(const LAS v4u*)(L + (off)))
#define LDSF4(off) (*(const LAS f32x4*)(L + (off)))
constexpr int OOB = 0x7FF00000;

template <bool STATS>
__device__ __forceinline__ void fill_half(LAS unsigned char* L, int hf, int v, int lane, const v4u (&d)[4]) {
#pragma unroll
  for (int i = 0; i < 4; ++i) *(LAS v4u*)(L + L_TILE + hf * 32768 + v * 4096 + i * 1024 + lane * 16) = d[i];
  if (STATS) {
    float s0 = 0.f, s1 = 0.f, q0 = 0.f, q1 = 0.f;
#pragma unroll
    for (int i = 0; i < 4; ++i) {
      const v4u x = d[i];
      s0 = dot2(x[0], 0x3F803F80u, s0); q0 = dot2(x[0], x[0], q0);
      s1 = dot2(x[1], 0x3F803F80u, s1); q1 = dot2(x[1], x[1], q1);
      s0 = dot2(x[2], 0x3F803F80u, s0); q0 = dot2(x[2], x[2], q0);
      s1 = dot2(x[3], 0x3F803F80u, s1); q1 = dot2(x[3], x[3], q1);
    }
    const f32x2 ss = both_halves(s0 + s1), qq = both_halves(q0 + q1);
    if (lane < 32) *(LAS f32x2*)(L + L_PST + ((hf * 8 + v) * 32 + lane) * 8) = f32x2{ss.x + ss.y, qq.x + qq.y};
  }
}
template <bool DOUBLE_LN>
__device__ __forceinline__ f32x2 final_stats(LAS unsigned char* L, int lane, int hf) {
  const int t = lane & 31, hh = lane >> 5;
  f32x2 pv[4];
#pragma unroll
  for (int k = 0; k < 4; ++k) pv[k] = *(LAS f32x2*)(L + L_PST + ((hf * 8 + hh * 4 + k) * 32 + t) * 8);
  SB();
  const f32x2 s = (pv[0] + pv[1]) + (pv[2] + pv[3]);
  const f32x2 rs_ = both_halves(s.x), rq = both_halves(s.y);
  const float mean = (rs_.x + rs_.y) * (1.0f / DM);
  float var = (rq.x + rq.y) * (1.0f / DM) - mean * mean;
  var = var > 0.f ? var : 0.f;
  float r = __builtin_amdgcn_rsqf(var + 1e-5f);
  if (DOUBLE_LN) {
    const float v2 = var * r * r;
    r *= __builtin_amdgcn_rsqf(v2 + 1e-5f);
  }
  const f32x2 ab = {r, -mean * r};
  *(LAS f32x2*)(L + L_FST + (hf * 32 + t) * 8) = ab;
  return ab;
}

__global__ void __launch_bounds__(512, 2) k_main(Params p) {
  extern __shared__ __attribute__((aligned(16))) unsigned char smem[];
  LAS unsigned char* L = (LAS unsigned char*)smem;
  const int b = blockIdx.x, tid = threadIdx.x, lane_ = tid & 63, v_ = __builtin_amdgcn_readfirstlane(tid >> 6);
#define MAPPING 1
  int c_, j_;
  if (MAPPING == 0) { c_ = (b & 7) * 4 + (b >> 6); j_ = (b >> 3) & 7; }
  else { c_ = ((b & 7) >> 1) * 8 + (b >> 5); j_ = (b & 1) * 4 + ((b >> 3) & 3); }
  const rsrc_t rs = __builtin_amdgcn_make_buffer_rsrc((void*)p.ws, 0, WS_END, 0x00020000);
  const rsrc_t rx = __builtin_amdgcn_make_buffer_rsrc((void*)p.x, 0, SEQ * DM * 2, 0x00020000);
  GAS unsigned* tmo = (GAS unsigned*)(p.ws + CTRL_OFF);
  STAMP(0);
  v2u xr[2]; v4u wq[24], stA[4];
  f32x16 Z;
#pragma unroll
  for (int q = 0; q < 16; ++q) Z[q] = 0.f;
  {
    FRESH();
    const float* cs = (const float*)(p.ws + CS_OFF);
    float k0 = 0.f, k1 = 0.f;
    if (tid < 224) { k0 = cs[1536 + 224 * j + tid]; k1 = bf2f(p.b1[224 * j + tid]); }
    else if (tid >= 256) { const int t = tid - 256; k0 = t < 192 ? cs[(t >> 6) * 512 + 64 * j + (t & 63)] : bf2f(p.b2[64 * j + t - 192]); }
#pragma unroll
    for (int hf = 0; hf < 2; ++hf) xr[hf] = *(const v2u*)(p.x + (size_t)(64 * c + 32 * hf + tk) * DM + 64 * j + 32 * (v & 1) + 8 * (v >> 1) + 4 * hh);
#pragma unroll
    for (int i = 0; i < 4; ++i) stA[i] = __builtin_amdgcn_raw_buffer_load_b128(rx, tk * 1024 + hh * 16 + i * 32, (64 * c) * 1024 + v * 128, 0);
    SB();
    __builtin_amdgcn_s_barrier();
    SB();
    const int wqb = RFL(WQ_OFF + (j * 192 + v * 24) * KB);
#pragma unroll
    for (int i = 0; i < 16; ++i) wq[i] = ldw(rs, vo, wqb, i);
    SB();
    if (tid < 224) { *(LAS float*)(L + L_C1 + tid * 4) = k0; *(LAS float*)(L + L_C1 + 1024 + tid * 4) = k1; }
    else if (tid >= 256) *(LAS float*)(L + L_CQ + (tid - 256) * 4) = k0;
    else if (tid < 228) *(LAS unsigned*)(L + L_ONES + (tid - 224) * 4) = 0x3F803F80u;
    v4u SENT4 = {SENT, SENT, SENT, SENT};
    asm volatile("" : "+v"(SENT4));
#pragma unroll
    for (int hf = 0; hf < 2; ++hf) {
      if (v < 4) st_sc1(SENT4, rs, vo, KV1_OFF + ((4 * j + v) * 64 + 2 * c + hf) * 2048);
      else st_sc1(SENT4, rs, ((4 * j + 2 * ((v - 4) >> 1) + ((lane >> 4) & 1)) * 64) * 2048 + 1024 + (32 * hh + 16 * (v & 1) + (lane & 15)) * 16, KV1_OFF + (2 * c + hf) * 2048);
    }
    SB();
  }

  for (int lay = 0; lay < 2; ++lay) {
    const int KV_OFF = lay ? KV1_OFF : KV0_OFF, OT_OFF = lay ? OT1_OFF : OT0_OFF, X1T_OFF = lay ? X1T1_OFF : X1T0_OFF, HT_OFF = lay ? HT1_OFF : HT0_OFF;
    v4u wo[8], w1[32], w2[28], stB[4];
    {
      FRESH();
      const int xtb = RFL(XT1_OFF + c * TILE_B + v * 4096);
      const int wob = RFL(WO_OFF + (j * 64 + v * 8) * KB);
      const int kh = v & 1;
      f32x16 acc[2];
      auto xb_issue = [&]() {
        if (lay) {
#pragma unroll
          for (int i = 0; i < 4; ++i) stB[i] = ld_seq(rs, vo, xtb + 32768, i);
        } else {
#pragma unroll
          for (int i = 0; i < 4; ++i) stB[i] = __builtin_amdgcn_raw_buffer_load_b128(rx, tk * 1024 + hh * 16 + i * 32, (64 * c + 32) * 1024 + v * 128, 0);
        }
      };
      auto pass_k = [&](int hf) {
        v4u xs[3];
        const int x0 = L_TILE + hf * 32768 + kh * 16384 + vo;
#pragma unroll
        for (int s = 0; s < 2; ++s) xs[s] = LDS4(x0 + s * 1024);
        acc[hf] = Z;
        PRIO(1);
#pragma unroll
        for (int s = 0; s < 16; ++s) {
          if (s + 2 < 16) xs[(s + 2) % 3] = LDS4(x0 + (s + 2) * 1024);
          SB();
          acc[hf] = mfma32(wq[s], xs[s % 3], acc[hf]);
          if (hf == 0 && s == 8) xb_issue();
          SB();
        }
        KEEP(acc[hf]);
        PRIO(0);
      };
      auto pass_v = [&](int hf) {
        v4u xs[3];
        const int x0 = L_TILE + hf * 32768 + kh * 16384 + (vo ^ (kh * 256));
#pragma unroll
        for (int s = 0; s < 2; ++s) xs[s] = LDS4(x0 + s * 1024);
        acc[hf] = Z;
        PRIO(1);
#pragma unroll
        for (int s = 0; s < 16; ++s) {
          if (s + 2 < 16) xs[(s + 2) % 3] = LDS4(x0 + (s + 2) * 1024);
          SB();
          acc[hf] = mfma32(xs[s % 3], wq[s], acc[hf]);
          if (hf == 0 && s == 8) xb_issue();
          SB();
        }
        KEEP(acc[hf]);
        PRIO(0);
      };
      auto send = [&](int hf) {
#pragma unroll
        for (int u = 0; u < 2; ++u) *(LAS f32x4*)(L + (hf ? L_XK1 : L_XCH) + (v * 2 + u) * 1024 + vo) = q4(acc[hf], 2 + u);
      };
      auto epi_k = [&](int hf) {
        const f32x2 ab = final_stats<false>(L, lane, hf);
        f32x16 val;
#pragma unroll
        for (int u = 0; u < 2; ++u) {
          const f32x4 o = LDSF4((hf ? L_XK1 : L_XCH) + ((v ^ 1) * 2 + u) * 1024 + vo);
          const f32x4 csn = LDSF4(L_CQ + (64 + 16 * v + 8 * u + 4 * hh) * 4);
#pragma unroll
          for (int i = 0; i < 4; ++i) val[4 * u + i] = (acc[hf][4 * u + i] + o[i]) * ab.x + csn[i] * ab.y;
        }
        const v4u K = nosent2(pk8(val, 0));
        st_sc1(K, rs, vo, KV_OFF + ((4 * j + v) * 64 + 2 * c + hf) * 2048);
        *(LAS v4u*)(L + L_KV + ((8 + hf) * 4 + v) * 2048 + vo) = K;
      };
      auto epi_v = [&](int hf) {
        const int pp = (v - 4) >> 1;
        (void)final_stats<false>(L, lane, hf);
        const float csv = *(LAS float*)(L + L_CQ + (128 + 32 * pp + tk) * 4);
        f32x16 val;
#pragma unroll
        for (int u = 0; u < 2; ++u) {
          const f32x4 o = LDSF4((hf ? L_XK1 : L_XCH) + ((v ^ 1) * 2 + u) * 1024 + vo);
          const f32x4 t0 = LDSF4(L_FST + (hf * 32 + 16 * kh + 8 * u + 4 * hh) * 8), t1 = LDSF4(L_FST + (hf * 32 + 16 * kh + 8 * u + 4 * hh + 2) * 8);
          val[4 * u + 0] = (acc[hf][4 * u + 0] + o[0]) * t0[0] + csv * t0[1];
          val[4 * u + 1] = (acc[hf][4 * u + 1] + o[1]) * t0[2] + csv * t0[3];
          val[4 * u + 2] = (acc[hf][4 * u + 2] + o[2]) * t1[0] + csv * t1[1];
          val[4 * u + 3] = (acc[hf][4 * u + 3] + o[3]) * t1[2] + csv * t1[3];
        }
        const int hb = (lane >> 4) & 1, d = lane & 15;
        const v4u V = nosent2(pk8(val, 0));
        st_sc1(V, rs, ((4 * j + 2 * pp + hb) * 64) * 2048 + 1024 + (32 * hh + 16 * kh + d) * 16, KV_OFF + (2 * c + hf) * 2048);
        *(LAS v4u*)(L + L_KV + ((8 + hf) * 4 + 2 * pp + hb) * 2048 + 1024 + (32 * hh + 16 * kh + d) * 16) = V;
      };
      if (lay) verify<4>(stA, [&](int i) { return ld_seq(rs, vo, xtb, i); }, tmo, 0x10, lane);
      fill_half<true>(L, 0, v, lane, stA);
      __syncthreads();
      if (v < 4) pass_k(0); else pass_v(0);
      send(0);
      if (lay) verify<4>(stB, [&](int i) { return ld_seq(rs, vo, xtb + 32768, i); }, tmo, 0x11, lane);
      fill_half<true>(L, 1, v, lane, stB);
      __syncthreads();
      {
        const int wqb = RFL(WQ_OFF + (j * 192 + v * 24) * KB);
#pragma unroll
        for (int i = 16; i < 24; ++i) wq[i] = ldw(rs, vo, wqb, i);
        SB();
      }
      if (v < 4) { epi_k(0); pass_k(1); } else { pass_v(1); epi_v(0); }
      send(1);
      __syncthreads();
      if (v < 4) epi_k(1); else epi_v(1);
      {
        const int ho = (v >> 1) & 1;
        const int x0 = L_TILE + ho * 32768 + (v >> 1) * 8192 + vo, x1 = L_TILE + (1 - ho) * 32768 + (v >> 1) * 8192 + vo;
        v4u xs0[3], xs1[3];
#pragma unroll
        for (int s = 0; s < 2; ++s) { xs0[s] = LDS4(x0 + s * 1024); xs1[s] = LDS4(x1 + s * 1024); }
        acc[0] = Z; acc[1] = Z;
        PRIO(1);
#pragma unroll
        for (int s = 0; s < 8; ++s) {
          if (s + 2 < 8) { xs0[(s + 2) % 3] = LDS4(x0 + (s + 2) * 1024); xs1[(s + 2) % 3] = LDS4(x1 + (s + 2) * 1024); }
          SB();
          acc[0] = mfma32(wq[16 + s], xs0[s % 3], acc[0]); acc[1] = mfma32(wq[16 + s], xs1[s % 3], acc[1]);
          wo[s] = ldw(rs, vo, wob, s);
          SB();
        }
        KEEP(acc[0]); KEEP(acc[1]);
        PRIO(0);
#pragma unroll
        for (int u = 0; u < 2; ++u) {
          *(LAS f32x4*)(L + L_XCH + (((v ^ 4) * 3 + 0) * 2 + u) * 1024 + vo) = q4(acc[0], 2 + u);
          *(LAS f32x4*)(L + L_XCH + (((v ^ 2) * 3 + 1) * 2 + u) * 1024 + vo) = q4(acc[1], u);
          *(LAS f32x4*)(L + L_XCH + (((v ^ 6) * 3 + 2) * 2 + u) * 1024 + vo) = q4(acc[1], 2 + u);
        }
      }
      v4u kvn[8];
      auto kv_load = [&](int i) {
        const int kt = 2 * c - 8 + 4 * (v >> 2) + (i >> 1);
        return ld_sc1(rs, vo + (i & 1) * 1024, RFL(kt >= 0 ? KV_OFF + ((4 * j + (v & 3)) * 64 + kt) * 2048 : OOB));
      };
      auto kv_issue = [&]() {
#pragma unroll
        for (int i = 0; i < 8; ++i) kvn[i] = kv_load(i);
      };
      WAITV0();
      __syncthreads();
      STAMP(16 + lay * 24 + 6);
      kv_issue();
      {
        const int ho = (v >> 1) & 1, hl = 2 * (v & 1) + (v >> 2);
        const f32x2 ab = *(LAS f32x2*)(L + L_FST + (ho * 32 + tk) * 8);
        const float cexp = 0.25f * 1.4426950408889634f;
        f32x16 val;
#pragma unroll
        for (int u = 0; u < 2; ++u) {
          f32x4 o = q4(acc[0], u);
#pragma unroll
          for (int k = 0; k < 3; ++k) o += LDSF4(L_XCH + ((v * 3 + k) * 2 + u) * 1024 + vo);
          const f32x4 csn = LDSF4(L_CQ + (16 * hl + 8 * u + 4 * hh) * 4);
#pragma unroll
          for (int i = 0; i < 4; ++i) val[4 * u + i] = o[i] * (ab.x * cexp) + csn[i] * (ab.y * cexp);
        }
        *(LAS v4u*)(L + L_Q + (hl * 2 + ho) * 1024 + vo) = pk8(val, 0);
      }
      const int w1b = RFL(v < 7 ? W1_OFF + (j * 224 + v * 32) * KB : OOB);
#pragma unroll
      for (int i = 0; i < 4; ++i) w1[i] = ldw(rs, vo, w1b, i);
      SB();
      verify<8>(kvn, kv_load, tmo, 0x20, lane);
      STAMP(16 + lay * 24 + 7);
      SB();
#pragma unroll
      for (int i = 0; i < 4; ++i) {
        *(LAS v4u*)(L + L_KV + ((4 * (v >> 2) + i) * 4 + (v & 3)) * 2048 + vo) = kvn[2 * i];
        *(LAS v4u*)(L + L_KV + ((4 * (v >> 2) + i) * 4 + (v & 3)) * 2048 + 1024 + vo) = kvn[2 * i + 1];
      }
      __syncthreads();
    }
    STAMP(1 + lay * 8);
    {
      FRESH();
      const int hq = v & 3, r = v >> 2, tq = tk - 4 * hh;
      const int kb = L_KV + (r * 4 + hq) * 2048;
      const v4u Q = LDS4(L_Q + (hq * 2 + r) * 1024 + vo);
      v4u Kf[9];
#pragma unroll
      for (int i = 0; i < 9; ++i) Kf[i] = LDS4(kb + i * 8192 + vo);
      const bool ones_lane = (lane & 16) != 0;
      const unsigned vsel = ones_lane ? 0u : 1u, vbase = ones_lane ? (unsigned)L_ONES : (unsigned)(kb + 1024 + (32 * hh + (lane & 15)) * 16);
      auto vop = [&](int i, int s) -> v4u { return LDS4(vbase + vsel * (unsigned)(i * 8192 + s * 256)); };
      const int first_ok = 8 - r - 2 * c;
      v4u SENT4 = {SENT, SENT, SENT, SENT};
      asm volatile("" : "+v"(SENT4));
      const int tsl = (8 * j + 4 * (v & 1) + (v >> 1)) * 512 + tk * 16 + hh * 8;
#pragma unroll
      for (int hf = 0; hf < 2; ++hf) __builtin_amdgcn_raw_buffer_store_b64(v2u{SENT4.x, SENT4.y}, rs, tsl + X1T_OFF + c * TILE_B + hf * 32768, 0, AUX_SC1);
      const int hcb = RFL(v < 7 ? HT_OFF + c * HT_C + (28 * j + 4 * v) * 512 : OOB);
      const int xcb = RFL(lay == 0 ? XT1_OFF + c * TILE_B : OOB), ocb = RFL(lay == 0 ? OT1_OFF + c * TILE_B + (v >> 2) * 32768 + (2 * (4 * j + (v & 3))) * 512 : OOB);
      auto clean = [&](int i) {
        if (i < 4) st_sc1(SENT4, rs, vo, hcb + (i >> 1) * 114688 + 2 * (i & 1) * 512);
        else if (i < 6) __builtin_amdgcn_raw_buffer_store_b64(v2u{SENT4.x, SENT4.y}, rs, tsl + xcb + (i - 4) * 32768, 0, AUX_SC1);
        else st_sc1(SENT4, rs, vo, ocb);
      };
      SB();
      const int w1b = RFL(v < 7 ? W1_OFF + (j * 224 + v * 32) * KB : OOB);
      auto tmax = [&](const f32x16& x, float m) {
        const float a = max3f(x[0], x[1], x[2]), b2_ = max3f(x[3], x[4], x[5]), c2 = max3f(x[6], x[7], x[8]), d = max3f(x[9], x[10], x[11]), e = max3f(x[12], x[13], x[14]);
        return max3f(max3f(a, b2_, x[15]), max3f(c2, d, e), m);
      };
      f32x16 Sm, S[2];
      {
        f32x16 Sa = mfma32(Kf[0], Q, Z);
        const f32x16 Sb = mfma32(Kf[8], Q, Z);
        S[1] = mfma32(Kf[1], Q, Z);
        if (first_ok > 0) {
#pragma unroll
          for (int q = 0; q < 16; ++q) Sa[q] = -1e30f;
        }
#pragma unroll
        for (int q = 0; q < 16; ++q) { const int row = (q & 3) + 8 * (q >> 2); Sm[q] = row <= tq ? Sb[q] : Sa[q]; }
      }
      float mx = tmax(Sm, -1e30f);
      SB();
#pragma unroll
      for (int i = 1; i < 8; ++i) {
        if (i + 1 < 8) S[(i + 1) & 1] = mfma32(Kf[i + 1], Q, Z);
        if (i >= first_ok) mx = tmax(S[i & 1], mx);
        if (i < 6) w1[4 + (i - 1)] = ldw(rs, vo, w1b, 4 + (i - 1));
        if (i == 1) clean(0);
        if (i == 3) clean(4);
        if (i == 5) clean(5);
        SB();
      }
      { const f32x2 rr = both_halves(mx); mx = max3f(rr.x, rr.y, rr.y); }
      f32x16 Cm;
#pragma unroll
      for (int q = 0; q < 16; ++q) Cm[q] = -mx;
      S[1] = mfma32(Kf[1], Q, Cm);
      f32x16 O;
      {
        const v4u vn0 = vop(8, 0), vn1 = vop(8, 1), vo0 = vop(0, 0), vo1 = vop(0, 1);
        unsigned pp[8];
#pragma unroll
        for (int q = 0; q < 8; ++q) pp[q] = pk(__builtin_amdgcn_exp2f(Sm[2 * q] - mx), __builtin_amdgcn_exp2f(Sm[2 * q + 1] - mx));
        v4u pn[2], po[2];
#pragma unroll
        for (int q = 0; q < 8; ++q) {
          const int row0 = ((2 * q) & 3) + 8 * ((2 * q) >> 2);
          const unsigned mk = (row0 <= tq ? 0xFFFFu : 0u) | (row0 + 1 <= tq ? 0xFFFF0000u : 0u);
          pn[q >> 2][q & 3] = pp[q] & mk; po[q >> 2][q & 3] = pp[q] & ~mk;
        }
        O = mfma32(vn0, pn[0], Z); O = mfma32(vn1, pn[1], O);
        O = mfma32(vo0, po[0], O); O = mfma32(vo1, po[1], O);
        SB();
      }
#pragma unroll
      for (int i = 1; i < 8; ++i) {
        if (i + 1 < 8) S[(i + 1) & 1] = mfma32(Kf[i + 1], Q, Cm);
        const v4u va0 = vop(i, 0), va1 = vop(i, 1);
        if (i >= first_ok) {
          const f32x16& cur = S[i & 1];
          v4u pp[2];
#pragma unroll
          for (int q = 0; q < 8; ++q) pp[q >> 2][q & 3] = pk(__builtin_amdgcn_exp2f(cur[2 * q]), __builtin_amdgcn_exp2f(cur[2 * q + 1]));
          O = mfma32(va0, pp[0], O); O = mfma32(va1, pp[1], O);
        }
        w1[9 + (i - 1)] = ldw(rs, vo, w1b, 9 + (i - 1));
        if (i == 1) clean(1);
        if (i == 3) clean(2);
        if (i == 5) clean(3);
        if (i == 7) clean(6);
        SB();
      }
      KEEP(O);
      const float rl = __builtin_amdgcn_rcpf(O[8]);
      asm volatile("s_waitcnt vmcnt(19)" ::: "memory");
      st_sc1(nosent2(swap_T8(pk8(O, 0, rl))), rs, vo, OT_OFF + c * TILE_B + r * 32768 + (2 * (4 * j + hq)) * 512);
      if (v < 4) WAITV0();
    }
    STAMP(2 + lay * 8);
    {
      FRESH();
      const int otb = RFL(OT_OFF + c * TILE_B + v * 4096);
      const int w1b = RFL(v < 7 ? W1_OFF + (j * 224 + v * 32) * KB : OOB);
      auto oa_issue = [&]() {
#pragma unroll
        for (int i = 0; i < 4; ++i) stA[i] = ld_seq(rs, vo, otb, i);
      };
      auto ob_issue = [&]() {
#pragma unroll
        for (int i = 0; i < 4; ++i) stB[i] = ld_seq(rs, vo, otb + 32768, i);
      };
      f32x16 a;
      auto pass = [&](int hf) {
        const int x0 = L_TILE + hf * 32768 + (v >> 1) * 8192 + vo;
        v4u xs[8];
#pragma unroll
        for (int s = 0; s < 8; ++s) xs[s] = LDS4(x0 + s * 1024);
        a = Z;
#pragma unroll
        for (int s = 0; s < 8; ++s) a = mfma32(wo[s], xs[s], a);
        KEEP(a);
#pragma unroll
        for (int u = 1; u < 4; ++u) *(LAS f32x4*)(L + L_XCH + ((hf * 8 + (v ^ (2 * u))) * 3 + (u - 1)) * 1024 + vo) = q4(a, u);
      };
      auto epi = [&](int hf) {
        f32x4 o = q4(a, 0);
#pragma unroll
        for (int k = 0; k < 3; ++k) o += LDSF4(L_XCH + ((hf * 8 + v) * 3 + k) * 1024 + vo);
        xr[hf] = pk4(unpk4(xr[hf]) + o);
        __builtin_amdgcn_raw_buffer_store_b64(v2u{nosent(xr[hf].x), xr[hf].y}, rs, (8 * j + 4 * (v & 1) + (v >> 1)) * 512 + tk * 16 + hh * 8 + X1T_OFF + c * TILE_B + hf * 32768, 0, AUX_SC1);
      };
      oa_issue();
#pragma unroll
      for (int i = 16; i < 24; ++i) w1[i] = ldw(rs, vo, w1b, i);
      SB();
      verify<4>(stA, [&](int i) { return ld_seq(rs, vo, otb, i); }, tmo, 0x30, lane);
      STAMP(16 + lay * 24 + 8);
      __syncthreads();
      fill_half<false>(L, 0, v, lane, stA);
      ob_issue();
      __syncthreads();
      STAMP(16 + lay * 24 + 9);
      pass(0);
      STAMP(16 + lay * 24 + 21);
      verify<4>(stB, [&](int i) { return ld_seq(rs, vo, otb + 32768, i); }, tmo, 0x31, lane);
      fill_half<false>(L, 1, v, lane, stB);
      __syncthreads();
      epi(0);
      STAMP(16 + lay * 24 + 23);
      pass(1);
#pragma unroll
      for (int i = 24; i < 32; ++i) w1[i] = ldw(rs, vo, w1b, i);
      __syncthreads();
#pragma unroll
      for (int i = 0; i < 4; ++i) stA[i] = ld_seq(rs, vo, RFL(X1T_OFF + c * TILE_B + v * 4096), i);
      SB();
      epi(1);
    }
    STAMP(3 + lay * 8);
    v4u hA[14];
    {
      FRESH();
      const int x1b = RFL(X1T_OFF + c * TILE_B + v * 4096);
      const int w2b = RFL(W2_OFF + (j * 224 + v * 28) * KB);
      const int hb = RFL(v < 7 ? HT_OFF + c * HT_C + (28 * j + 4 * v) * 512 : OOB);
      auto xb_issue = [&]() {
#pragma unroll
        for (int i = 0; i < 4; ++i) stB[i] = ld_seq(rs, vo, x1b + 32768, i);
      };
      f32x16 a;
      auto pass = [&](int hf) {
        v4u xs[3];
        const int x0 = L_TILE + hf * 32768 + vo;
#pragma unroll
        for (int s = 0; s < 2; ++s) xs[s] = LDS4(x0 + s * 1024);
        a = Z;
        PRIO(1);
#pragma unroll
        for (int s = 0; s < 32; ++s) {
          if (s + 2 < 32) xs[(s + 2) % 3] = LDS4(x0 + (s + 2) * 1024);
          SB();
          a = mfma32(w1[s], xs[s % 3], a);
          if (hf == 0 && s == 8) xb_issue();
          if (hf == 0 && s >= 22) w2[s - 22] = ldw(rs, vo, w2b, s - 22);
          if (hf == 1 && s >= 2 && s < 20) w2[10 + (s - 2)] = ldw(rs, vo, w2b, 10 + (s - 2));
          SB();
        }
        KEEP(a);
        PRIO(0);
      };
      v4u ho[2];
      auto gelu = [&](int hf) {
        const f32x2 ab = final_stats<true>(L, lane, hf);
        const float c1 = -2.0f * 0.7978845608028654f * 1.4426950408889634f, c2 = c1 * 0.044715f;
#pragma unroll
        for (int u2 = 0; u2 < 2; ++u2) {
          f32x16 hv;
#pragma unroll
          for (int u = 2 * u2; u < 2 * u2 + 2; ++u) {
            const f32x4 csn = LDSF4(L_C1 + (32 * v + 8 * u + 4 * hh) * 4), bn = LDSF4(L_C1 + 1024 + (32 * v + 8 * u + 4 * hh) * 4);
#pragma unroll
            for (int i = 0; i < 4; ++i) {
              const float z = a[4 * u + i] * ab.x + (csn[i] * ab.y + bn[i]);
              const float e = __builtin_amdgcn_exp2f(z * (z * z * c2 + c1));
              hv[4 * u + i] = z * __builtin_amdgcn_rcpf(1.0f + e);
            }
          }
          ho[u2] = nosent2(swap_T8(pk8(hv, 8 * u2)));
          if (hf == 0) st_sc1(ho[u2], rs, vo, hb + 2 * u2 * 512);
        }
      };
      verify<4>(stA, [&](int i) { return ld_seq(rs, vo, x1b, i); }, tmo, 0x40, lane);
      STAMP(16 + lay * 24 + 10);
      fill_half<true>(L, 0, v, lane, stA);
      __syncthreads();
      STAMP(16 + lay * 24 + 11);
      pass(0);
      STAMP(16 + lay * 24 + 12);
      gelu(0);
      STAMP(16 + lay * 24 + 13);
      verify<4>(stB, [&](int i) { return ld_seq(rs, vo, x1b + 32768, i); }, tmo, 0x41, lane);
      fill_half<true>(L, 1, v, lane, stB);
      __syncthreads();
      STAMP(16 + lay * 24 + 14);
      pass(1);
      STAMP(16 + lay * 24 + 15);
#pragma unroll
      for (int s = 0; s < 14; ++s) hA[s] = ld_seq(rs, vo, RFL(HT_OFF + c * HT_C + 28 * v * 512), s);
      SB();
      gelu(1);
#pragma unroll
      for (int u2 = 0; u2 < 2; ++u2) st_sc1(ho[u2], rs, vo, hb + 114688 + 2 * u2 * 512);
    }
    STAMP(4 + lay * 8);
    {
      FRESH();
      const int htb = RFL(HT_OFF + c * HT_C + 28 * v * 512);
      const int wqb = RFL(lay == 0 ? WQ_OFF + (j * 192 + v * 24) * KB : OOB);
      const int xnb = RFL(lay == 0 ? XT1_OFF + c * TILE_B + v * 4096 : OOB);
#pragma unroll
      for (int hf = 0; hf < 2; ++hf) {
        verify<14>(hA, [&](int i) { return ld_seq(rs, vo, htb + hf * 114688, i); }, tmo, 0x50 + hf, lane);
        if (hf == 0) __syncthreads();
        STAMP(16 + lay * 24 + 17 + 2 * hf);
        f32x16 ac[2] = {Z, Z};
        PRIO(1);
#pragma unroll
        for (int s = 0; s < 14; ++s) {
          ac[0] = mfma32(w2[2 * s], hA[s], ac[0]);
          ac[1] = mfma32(w2[2 * s + 1], hA[s], ac[1]);
          if (hf == 1) wq[s] = ldw(rs, vo, wqb, s);
          else hA[s] = ld_seq(rs, vo, htb + 114688, s);
          SB();
        }
        KEEP(ac[0]); KEEP(ac[1]);
        PRIO(0);
        STAMP(16 + lay * 24 + 18 + 2 * hf);
        LAS unsigned char* R = L + (hf ? L_TILE : L_RB);
#pragma unroll
        for (int nt = 0; nt < 2; ++nt)
#pragma unroll
          for (int u = 0; u < 4; ++u) *(LAS f32x4*)(R + ((nt + 2 * u) * 8 + v) * 1024 + vo) = q4(ac[nt], u);
        if (hf == 1) { wq[14] = ldw(rs, vo, wqb, 14); wq[15] = ldw(rs, vo, wqb, 15); }
        __syncthreads();
        f32x4 s0 = {0, 0, 0, 0};
#pragma unroll
        for (int k = 0; k < 8; ++k) s0 += *(LAS f32x4*)(R + (v * 8 + k) * 1024 + vo);
        const f32x4 b0 = LDSF4(L_CQ + (192 + 32 * (v & 1) + 8 * (v >> 1) + 4 * hh) * 4);
        xr[hf] = pk4(unpk4(xr[hf]) + s0 + b0);
        if (hf == 1) {
#pragma unroll
          for (int i = 0; i < 4; ++i) stA[i] = ld_seq(rs, vo, xnb, i);
          SB();
        }
        if (lay == 0) {
          __builtin_amdgcn_raw_buffer_store_b64(v2u{nosent(xr[hf].x), xr[hf].y}, rs, (8 * j + 4 * (v & 1) + (v >> 1)) * 512 + tk * 16 + hh * 8 + XT1_OFF + c * TILE_B + hf * 32768, 0, AUX_SC1);
        } else {
          *(v2u*)(p.out + (size_t)(64 * c + 32 * hf + tk) * DM + 64 * j + 32 * (v & 1) + 8 * (v >> 1) + 4 * hh) = xr[hf];
        }
      }
      __syncthreads();
    }
    STAMP(5 + lay * 8);
  }
  STAMP_FLUSH();
}
}

extern "C" void kernel_launch(void* const* d_in, const int* in_sizes, int n_in, void* d_out, int out_size, void* d_ws, size_t ws_size,
                              hipStream_t stream) {
  using namespace fk;
  Params p;
  p.x = (const u16*)d_in[0]; p.wqkv = (const u16*)d_in[1]; p.wo = (const u16*)d_in[2];
  p.w1 = (const u16*)d_in[4]; p.b1 = (const u16*)d_in[5]; p.w2 = (const u16*)d_in[6]; p.b2 = (const u16*)d_in[7];
  p.out = (u16*)d_out; p.ws = (unsigned char*)d_ws;
  (void)hipFuncSetAttribute((const void*)k_main, hipFuncAttributeMaxDynamicSharedMemorySize, L_BYTES);
  k_prep<<<PREP_BLOCKS, 256, 0, stream>>>(p);
  k_main<<<256, 512, L_BYTES, stream>>>(p);
}
```

```cpp
#include <hip/hip_runtime.h>
#include <stdint.h>

namespace fk {
typedef unsigned short u16;
typedef unsigned v4u __attribute__((ext_vector_type(4)));
typedef unsigned v2u __attribute__((ext_vector_type(2)));
typedef float f32x4 __attribute__((ext_vector_type(4)));
typedef float f32x2 __attribute__((ext_vector_type(2)));
typedef float f32x16 __attribute__((ext_vector_type(16)));
typedef __bf16 bf8v __attribute__((ext_vector_type(8)));
typedef __bf16 bf2v __attribute__((ext_vector_type(2)));
#define LAS __attribute__((address_space(3)))
#define GAS __attribute__((address_space(1)))
typedef __amdgpu_buffer_rsrc_t rsrc_t;

constexpr int SEQ = 2048, DM = 512, FF = 1792;
constexpr int KB = 1024;
constexpr int WQ_OFF = 0;
constexpr int WO_OFF = WQ_OFF + 8 * 192 * KB;
constexpr int W1_OFF = WO_OFF + 8 * 64 * KB;
constexpr int W2_OFF = W1_OFF + 8 * 224 * KB;
constexpr int CS_OFF = W2_OFF + 8 * 224 * KB;
constexpr int CTRL_OFF = CS_OFF + 16 * KB;
constexpr int TILE_B = 64 * KB;
constexpr int KV_B = 32 * 64 * 2 * KB;
constexpr int HT_C = 2 * 224 * 512;
constexpr int KV0_OFF = CTRL_OFF + 16 * KB;
constexpr int OT0_OFF = KV0_OFF + KV_B;
constexpr int KV1_OFF = OT0_OFF + 32 * TILE_B;
constexpr int OT1_OFF = KV1_OFF + KV_B;
constexpr int X1T0_OFF = OT1_OFF + 32 * TILE_B;
constexpr int X1T1_OFF = X1T0_OFF + 32 * TILE_B;
constexpr int XT1_OFF = X1T1_OFF + 32 * TILE_B;
constexpr int HT0_OFF = XT1_OFF + 32 * TILE_B;
constexpr int HT1_OFF = HT0_OFF + 32 * HT_C;
constexpr int WS_END = HT1_OFF + 32 * HT_C;
constexpr unsigned SENT = 0xFFFFFFFFu;
constexpr int AUX_SC1 = 16;
constexpr unsigned SPIN_CAP = 1u << 14;

constexpr int L_TILE = 0;
constexpr int L_KV = 0;
constexpr int L_RB = 64 * KB;
constexpr int L_Q = 80 * KB;
constexpr int L_XK1 = 88 * KB;
constexpr int L_XCH = 104 * KB;
constexpr int L_PST = 152 * KB;
constexpr int L_FST = 156 * KB;
constexpr int L_C1 = 156 * KB + 512;
constexpr int L_CQ = 158 * KB + 512;
constexpr int L_ONES = 159 * KB + 512;
constexpr int L_BYTES = 159 * KB + 576;

__device__ __forceinline__ float bf2f(unsigned b) { return __builtin_bit_cast(float, b << 16); }
__device__ __forceinline__ float bflo(unsigned d) { return __builtin_bit_cast(float, d << 16); }
__device__ __forceinline__ float bfhi(unsigned d) { return __builtin_bit_cast(float, d & 0xFFFF0000u); }
__device__ __forceinline__ unsigned pk(float lo, float hi) {
  bf2v r = {(__bf16)lo, (__bf16)hi};
  return __builtin_bit_cast(unsigned, r);
}
__device__ __forceinline__ unsigned nosent(unsigned d) { return d < 0xFFFFFFFEu ? d : 0xFFFFFFFEu; }
__device__ __forceinline__ v2u pk4(f32x4 a) { return v2u{pk(a[0], a[1]), pk(a[2], a[3])}; }
__device__ __forceinline__ f32x4 unpk4(v2u v) { return f32x4{bflo(v.x), bfhi(v.x), bflo(v.y), bfhi(v.y)}; }
#define KEEP(x) asm volatile("" : "+v"(x))
#define WAITV0() asm volatile("s_waitcnt vmcnt(0)" ::: "memory")
#define PRIO(x) __builtin_amdgcn_s_setprio(x)
#define SB() __builtin_amdgcn_sched_barrier(0)

__device__ __forceinline__ v4u ldw(rsrc_t rs, int voff, int sbase, int i) {
  return __builtin_amdgcn_raw_buffer_load_b128(rs, voff + (i & 3) * 1024, sbase + (i >> 2) * 4096, 0);
}
__device__ __forceinline__ v4u ld_sc1(rsrc_t rs, int voff, int soff) {
  return __builtin_amdgcn_raw_buffer_load_b128(rs, voff, soff, AUX_SC1); }
__device__ __forceinline__ v4u ld_seq(rsrc_t rs, int voff, int sbase, int i) { return ld_sc1(rs, voff + (i & 3) * 1024, sbase + (i >> 2) * 4096); }
#define RFL(x) __builtin_amdgcn_readfirstlane(x)
__device__ __forceinline__ void st_sc1(v4u v, rsrc_t rs, int voff, int soff) {
  __builtin_amdgcn_raw_buffer_store_b128(v, rs, voff + soff, 0, AUX_SC1); }
__device__ __forceinline__ bool spin_fail(unsigned& spins, GAS unsigned* tmo, unsigned code, int lane) {
  ++spins;
  if (spins > SPIN_CAP) {
    if (lane == 0) __hip_atomic_store(tmo, code, __ATOMIC_RELAXED, __HIP_MEMORY_SCOPE_AGENT);
    return true;
  }
  if ((spins & 63u) == 0u && __hip_atomic_load(tmo, __ATOMIC_RELAXED, __HIP_MEMORY_SCOPE_AGENT) != 0u) return true;
  return false;
}

struct Params {
  const u16* x;
  const u16* wqkv;
  const u16* wo;
  const u16* w1;
  const u16* b1;
  const u16* w2;
  const u16* b2;
  u16* out;
  unsigned char* ws;
};

constexpr int NFRAG = 8 * 704;
constexpr int PB_TILE = NFRAG * 16 / 256;
constexpr int PB_CS = (1536 + 1792) / 64;
constexpr int CLEAN_B = KV_B + 32 * TILE_B;
constexpr int PB_CLEAN = CLEAN_B / (256 * 64);
constexpr int PREP_BLOCKS = PB_TILE + PB_CS + PB_CLEAN;

__global__ void __launch_bounds__(256) k_prep(Params p) {
  const int b = blockIdx.x, tid = threadIdx.x;
  if (b < 48) {
    unsigned lo, hi;
    asm volatile("s_getpc_b64 s[20:21]\n\ts_add_u32 s20, s20, _ZN2fk6k_mainENS_6ParamsE@rel32@lo+4\n\ts_addc_u32 s21, s21, _ZN2fk6k_mainENS_6ParamsE@rel32@hi+12\n\ts_mov_b32 %0, s20\n\ts_mov_b32 %1, s21"
                 : "=s"(lo), "=s"(hi) :: "s20", "s21", "scc");
    const unsigned long long pc = ((unsigned long long)hi << 32) | lo;
    const v4u t = *(const GAS v4u*)(pc + (b >> 3) * 4096 + tid * 16);
    asm volatile("" :: "v"(t));
  }
  if (b < PB_TILE) {
    const int F = b * 16 + (tid >> 4), t16 = tid & 15, cq = t16 & 7, h = t16 >> 3;
    const u16* W; int ldw_, k0, col;
    if (F < 8 * 192) {
      const int j = F / 192, v = (F % 192) / 24, i = F % 24;
      W = p.wqkv; ldw_ = 1536;
      if (i < 16) {
        k0 = 16 * (16 * (v & 1) + i);
        col = v < 4 ? 512 + (4 * j + 2 * (v >> 1)) * 16 + 4 * (cq ^ (4 * (v & 1))) : 1024 + (4 * j + 2 * ((v - 4) >> 1)) * 16 + 4 * cq;
      } else {
        k0 = 16 * (8 * (v >> 1) + i - 16);
        col = (4 * j + 2 * (v & 1)) * 16 + 4 * (cq ^ (4 * (v >> 2)));
      }
    } else if (F < 8 * 256) {
      const int f = F - 8 * 192, j = f >> 6, u = f & 63, v = u >> 3;
      W = p.wo; ldw_ = 512; k0 = 16 * (8 * (v >> 1) + (u & 7)); col = 64 * j + 32 * (v & 1) + 4 * (cq ^ (2 * (v >> 1)));
    } else if (F < 8 * 480) {
      const int f = F - 8 * 256, j = f / 224, u = f % 224;
      W = p.w1; ldw_ = 1792; k0 = 16 * (u & 31); col = 224 * j + 32 * (u >> 5) + 4 * cq;
    } else {
      const int f = F - 8 * 480, j = f / 224, u = f % 224, v = u / 28, q = u % 28;
      W = p.w2; ldw_ = 512; k0 = 224 * v + 16 * (q >> 1); col = 64 * j + 32 * (q & 1) + 4 * cq;
    }
    const u16* src = W + (size_t)(k0 + 8 * h) * ldw_ + col;
    v2u r[8];
#pragma unroll
    for (int e = 0; e < 8; ++e) r[e] = *(const v2u*)(src + (size_t)e * ldw_);
    v4u o[4];
#pragma unroll
    for (int e = 0; e < 4; ++e) {
      const unsigned a0 = r[2 * e].x, a1 = r[2 * e + 1].x, c0 = r[2 * e].y, c1 = r[2 * e + 1].y;
      o[0][e] = (a0 & 0xFFFFu) | (a1 << 16);
      o[1][e] = (a0 >> 16) | (a1 & 0xFFFF0000u);
      o[2][e] = (c0 & 0xFFFFu) | (c1 << 16);
      o[3][e] = (c0 >> 16) | (c1 & 0xFFFF0000u);
    }
    v4u* dst = (v4u*)(p.ws + (size_t)F * 1024 + (h * 32 + 4 * cq) * 16);
#pragma unroll
    for (int i = 0; i < 4; ++i) dst[i] = o[i];
  } else if (b < PB_TILE + PB_CS) {
    __shared__ float red[16][64];
    const int cb = b - PB_TILE;
    const u16* W; int ldw_, col;
    if (cb < 24) { W = p.wqkv; ldw_ = 1536; col = cb * 64; } else { W = p.w1; ldw_ = 1792; col = (cb - 24) * 64; }
    const int cq = tid & 15, rg = tid >> 4;
    float s0 = 0, s1 = 0, s2 = 0, s3 = 0;
    const u16* src = W + (size_t)(rg * 32) * ldw_ + col + 4 * cq;
#pragma unroll
    for (int e = 0; e < 32; ++e) {
      const v2u v = *(const v2u*)(src + (size_t)e * ldw_);
      s0 += bflo(v.x); s1 += bfhi(v.x); s2 += bflo(v.y); s3 += bfhi(v.y);
    }
    red[rg][4 * cq + 0] = s0; red[rg][4 * cq + 1] = s1; red[rg][4 * cq + 2] = s2; red[rg][4 * cq + 3] = s3;
    __syncthreads();
    if (tid < 64) {
      float s = 0;
#pragma unroll
      for (int r = 0; r < 16; ++r) s += red[r][tid];
      ((float*)(p.ws + CS_OFF))[cb * 64 + tid] = s;
    }
    if (cb == 0 && tid == 0) *(unsigned*)(p.ws + CTRL_OFF) = 0u;
  } else {
    const int cb = b - PB_TILE - PB_CS;
    v4u* dst = (v4u*)(p.ws + KV0_OFF + (size_t)cb * (256 * 64)) + tid;
    const v4u s = {SENT, SENT, SENT, SENT};
#pragma unroll
    for (int i = 0; i < 4; ++i) dst[i * 256] = s;
  }
}


__device__ __forceinline__ float max3f(float a, float b, float c) { float r; asm("v_max3_f32 %0, %1, %2, %3" : "=v"(r) : "v"(a), "v"(b), "v"(c)); return r; }
__device__ __forceinline__ unsigned max3u(unsigned a, unsigned b, unsigned c) { unsigned r; asm("v_max3_u32 %0, %1, %2, %3" : "=v"(r) : "v"(a), "v"(b), "v"(c)); return r; }
__device__ __forceinline__ f32x16 mfma32(v4u a, v4u b, f32x16 c) {
  return __builtin_amdgcn_mfma_f32_32x32x16_bf16(__builtin_bit_cast(bf8v, a), __builtin_bit_cast(bf8v, b), c, 0, 0, 0);
}
template <int N> __device__ __forceinline__ bool any_sent(const v4u (&d)[N]) {
  unsigned m0 = 0, m1 = 0;
#pragma unroll
  for (int i = 0; i < N; ++i) { if (i & 1) m1 = max3u(m1, d[i].x, d[i].z); else m0 = max3u(m0, d[i].x, d[i].z); }
  return __any(max3u(m0, m1, 0) == SENT);
}
template <int N, class F> __device__ __forceinline__ void verify(v4u (&d)[N], F load_i, GAS unsigned* tmo, unsigned code, int lane) {
  if (__builtin_expect(any_sent<N>(d), 0)) {
    unsigned spins = 0;
    do {
      __builtin_amdgcn_s_sleep(1);
      asm volatile("" ::: "memory");
#pragma unroll
      for (int i = 0; i < N; ++i)
        if (__any(max3u(d[i].x, d[i].z, 0u) == SENT)) d[i] = load_i(i);
    } while (any_sent<N>(d) && !spin_fail(spins, tmo, code, lane));
  }
}
__device__ __forceinline__ f32x2 both_halves(float v) {
  const unsigned a = __builtin_bit_cast(unsigned, v);
  auto r = __builtin_amdgcn_permlane32_swap(a, a, false, false);
  const unsigned r0 = r[0], r1 = r[1];
  return f32x2{__builtin_bit_cast(float, r0), __builtin_bit_cast(float, r1)};
}
__device__ __forceinline__ float dot2(unsigned a, unsigned b, float c) {
  return __builtin_amdgcn_fdot2_f32_bf16(__builtin_bit_cast(bf2v, a), __builtin_bit_cast(bf2v, b), c, false);
}
__device__ __forceinline__ v4u nosent2(v4u v) { return v4u{nosent(v.x), v.y, nosent(v.z), v.w}; }

__device__ __forceinline__ v4u pk8(const f32x16& a, int r0, float sc = 1.0f) {
  return v4u{pk(a[r0] * sc, a[r0 + 1] * sc), pk(a[r0 + 2] * sc, a[r0 + 3] * sc), pk(a[r0 + 4] * sc, a[r0 + 5] * sc), pk(a[r0 + 6] * sc, a[r0 + 7] * sc)};
}
__device__ __forceinline__ v4u swap_T8(v4u p) {
  auto r0 = __builtin_amdgcn_permlane32_swap(p[0], p[2], false, false);
  auto r1 = __builtin_amdgcn_permlane32_swap(p[1], p[3], false, false);
  return v4u{r0[0], r1[0], r0[1], r1[1]};
}
__device__ __forceinline__ f32x4 q4(const f32x16& a, int u) { return f32x4{a[4 * u], a[4 * u + 1], a[4 * u + 2], a[4 * u + 3]}; }
#define FRESH() int lane = lane_, c = c_, j = j_, v = v_; asm volatile("" : "+v"(lane), "+s"(c), "+s"(j), "+s"(v)); \
  const int vo = lane * 16, tk = lane & 31, hh = lane >> 5; (void)vo; (void)tk; (void)hh
#define LDS4(off) (*(const LAS v4u*)(L + (off)))
#define LDSF4(off) (*(const LAS f32x4*)(L + (off)))
constexpr int OOB = 0x7FF00000;

template <bool STATS>
__device__ __forceinline__ void fill_half(LAS unsigned char* L, int hf, int v, int lane, const v4u (&d)[4]) {
#pragma unroll
  for (int i = 0; i < 4; ++i) *(LAS v4u*)(L + L_TILE + hf * 32768 + v * 4096 + i * 1024 + lane * 16) = d[i];
  if (STATS) {
    float s0 = 0.f, s1 = 0.f, q0 = 0.f, q1 = 0.f;
#pragma unroll
    for (int i = 0; i < 4; ++i) {
      const v4u x = d[i];
      s0 = dot2(x[0], 0x3F803F80u, s0); q0 = dot2(x[0], x[0], q0);
      s1 = dot2(x[1], 0x3F803F80u, s1); q1 = dot2(x[1], x[1], q1);
      s0 = dot2(x[2], 0x3F803F80u, s0); q0 = dot2(x[2], x[2], q0);
      s1 = dot2(x[3], 0x3F803F80u, s1); q1 = dot2(x[3], x[3], q1);
    }
    const f32x2 ss = both_halves(s0 + s1), qq = both_halves(q0 + q1);
    if (lane < 32) *(LAS f32x2*)(L + L_PST + ((hf * 8 + v) * 32 + lane) * 8) = f32x2{ss.x + ss.y, qq.x + qq.y};
  }
}
template <bool DOUBLE_LN>
__device__ __forceinline__ f32x2 final_stats(LAS unsigned char* L, int lane, int hf) {
  const int t = lane & 31, hh = lane >> 5;
  f32x2 pv[4];
#pragma unroll
  for (int k = 0; k < 4; ++k) pv[k] = *(LAS f32x2*)(L + L_PST + ((hf * 8 + hh * 4 + k) * 32 + t) * 8);
  SB();
  const f32x2 s = (pv[0] + pv[1]) + (pv[2] + pv[3]);
  const f32x2 rs_ = both_halves(s.x), rq = both_halves(s.y);
  const float mean = (rs_.x + rs_.y) * (1.0f / DM);
  float var = (rq.x + rq.y) * (1.0f / DM) - mean * mean;
  var = var > 0.f ? var : 0.f;
  float r = __builtin_amdgcn_rsqf(var + 1e-5f);
  if (DOUBLE_LN) {
    const float v2 = var * r * r;
    r *= __builtin_amdgcn_rsqf(v2 + 1e-5f);
  }
  const f32x2 ab = {r, -mean * r};
  *(LAS f32x2*)(L + L_FST + (hf * 32 + t) * 8) = ab;
  return ab;
}

__global__ void __launch_bounds__(512, 2) k_main(Params p) {
  extern __shared__ __attribute__((aligned(16))) unsigned char smem[];
  LAS unsigned char* L = (LAS unsigned char*)smem;
  const int b = blockIdx.x, tid = threadIdx.x, lane_ = tid & 63, v_ = __builtin_amdgcn_readfirstlane(tid >> 6);
  int c_, j_;
  c_ = ((b & 7) >> 1) * 8 + (b >> 5); j_ = (b & 1) * 4 + ((b >> 3) & 3);
  const rsrc_t rs = __builtin_amdgcn_make_buffer_rsrc((void*)p.ws, 0, WS_END, 0x00020000);
  const rsrc_t rx = __builtin_amdgcn_make_buffer_rsrc((void*)p.x, 0, SEQ * DM * 2, 0x00020000);
  GAS unsigned* tmo = (GAS unsigned*)(p.ws + CTRL_OFF);
  v2u xr[2]; v4u wq[24], stA[4];
  f32x16 Z;
#pragma unroll
  for (int q = 0; q < 16; ++q) Z[q] = 0.f;
  {
    FRESH();
    const float* cs = (const float*)(p.ws + CS_OFF);
    float k0 = 0.f, k1 = 0.f;
    if (tid < 224) { k0 = cs[1536 + 224 * j + tid]; k1 = bf2f(p.b1[224 * j + tid]); }
    else if (tid >= 256) { const int t = tid - 256; k0 = t < 192 ? cs[(t >> 6) * 512 + 64 * j + (t & 63)] : bf2f(p.b2[64 * j + t - 192]); }
#pragma unroll
    for (int hf = 0; hf < 2; ++hf) xr[hf] = *(const v2u*)(p.x + (size_t)(64 * c + 32 * hf + tk) * DM + 64 * j + 32 * (v & 1) + 8 * (v >> 1) + 4 * hh);
#pragma unroll
    for (int i = 0; i < 4; ++i) stA[i] = __builtin_amdgcn_raw_buffer_load_b128(rx, tk * 1024 + hh * 16 + i * 32, (64 * c) * 1024 + v * 128, 0);
    SB();
    __builtin_amdgcn_s_barrier();
    SB();
    const int wqb = RFL(WQ_OFF + (j * 192 + v * 24) * KB);
#pragma unroll
    for (int i = 0; i < 16; ++i) wq[i] = ldw(rs, vo, wqb, i);
    SB();
    if (tid < 224) { *(LAS float*)(L + L_C1 + tid * 4) = k0; *(LAS float*)(L + L_C1 + 1024 + tid * 4) = k1; }
    else if (tid >= 256) *(LAS float*)(L + L_CQ + (tid - 256) * 4) = k0;
    else if (tid < 228) *(LAS unsigned*)(L + L_ONES + (tid - 224) * 4) = 0x3F803F80u;
    v4u SENT4 = {SENT, SENT, SENT, SENT};
    asm volatile("" : "+v"(SENT4));
#pragma unroll
    for (int hf = 0; hf < 2; ++hf) {
      if (v < 4) st_sc1(SENT4, rs, vo, KV1_OFF + ((4 * j + v) * 64 + 2 * c + hf) * 2048);
      else st_sc1(SENT4, rs, ((4 * j + 2 * ((v - 4) >> 1) + ((lane >> 4) & 1)) * 64) * 2048 + 1024 + (32 * hh + 16 * (v & 1) + (lane & 15)) * 16, KV1_OFF + (2 * c + hf) * 2048);
    }
    SB();
  }

  for (int lay = 0; lay < 2; ++lay) {
    const int KV_OFF = lay ? KV1_OFF : KV0_OFF, OT_OFF = lay ? OT1_OFF : OT0_OFF, X1T_OFF = lay ? X1T1_OFF : X1T0_OFF, HT_OFF = lay ? HT1_OFF : HT0_OFF;
    v4u wo[8], w1[32], w2[28], stB[4];
    {
      FRESH();
      const int xtb = RFL(XT1_OFF + c * TILE_B + v * 4096);
      const int wob = RFL(WO_OFF + (j * 64 + v * 8) * KB);
      const int kh = v & 1;
      f32x16 acc[2];
      auto xb_issue = [&]() {
        if (lay) {
#pragma unroll
          for (int i = 0; i < 4; ++i) stB[i] = ld_seq(rs, vo, xtb + 32768, i);
        } else {
#pragma unroll
          for (int i = 0; i < 4; ++i) stB[i] = __builtin_amdgcn_raw_buffer_load_b128(rx, tk * 1024 + hh * 16 + i * 32, (64 * c + 32) * 1024 + v * 128, 0);
        }
      };
      auto pass_k = [&](int hf) {
        v4u xs[3];
        const int x0 = L_TILE + hf * 32768 + kh * 16384 + vo;
#pragma unroll
        for (int s = 0; s < 2; ++s) xs[s] = LDS4(x0 + s * 1024);
        acc[hf] = Z;
        PRIO(1);
#pragma unroll
        for (int s = 0; s < 16; ++s) {
          if (s + 2 < 16) xs[(s + 2) % 3] = LDS4(x0 + (s + 2) * 1024);
          SB();
          acc[hf] = mfma32(wq[s], xs[s % 3], acc[hf]);
          if (hf == 0 && s == 8) xb_issue();
          SB();
        }
        KEEP(acc[hf]);
        PRIO(0);
      };
      auto pass_v = [&](int hf) {
        v4u xs[3];
        const int x0 = L_TILE + hf * 32768 + kh * 16384 + (vo ^ (kh * 256));
#pragma unroll
        for (int s = 0; s < 2; ++s) xs[s] = LDS4(x0 + s * 1024);
        acc[hf] = Z;
        PRIO(1);
#pragma unroll
        for (int s = 0; s < 16; ++s) {
          if (s + 2 < 16) xs[(s + 2) % 3] = LDS4(x0 + (s + 2) * 1024);
          SB();
          acc[hf] = mfma32(xs[s % 3], wq[s], acc[hf]);
          if (hf == 0 && s == 8) xb_issue();
          SB();
        }
        KEEP(acc[hf]);
        PRIO(0);
      };
      auto send = [&](int hf) {
#pragma unroll
        for (int u = 0; u < 2; ++u) *(LAS f32x4*)(L + (hf ? L_XK1 : L_XCH) + (v * 2 + u) * 1024 + vo) = q4(acc[hf], 2 + u);
      };
      auto epi_k = [&](int hf) {
        const f32x2 ab = final_stats<false>(L, lane, hf);
        f32x16 val;
#pragma unroll
        for (int u = 0; u < 2; ++u) {
          const f32x4 o = LDSF4((hf ? L_XK1 : L_XCH) + ((v ^ 1) * 2 + u) * 1024 + vo);
          const f32x4 csn = LDSF4(L_CQ + (64 + 16 * v + 8 * u + 4 * hh) * 4);
#pragma unroll
          for (int i = 0; i < 4; ++i) val[4 * u + i] = (acc[hf][4 * u + i] + o[i]) * ab.x + csn[i] * ab.y;
        }
        const v4u K = nosent2(pk8(val, 0));
        st_sc1(K, rs, vo, KV_OFF + ((4 * j + v) * 64 + 2 * c + hf) * 2048);
        *(LAS v4u*)(L + L_KV + ((8 + hf) * 4 + v) * 2048 + vo) = K;
      };
      auto epi_v = [&](int hf) {
        const int pp = (v - 4) >> 1;
        (void)final_stats<false>(L, lane, hf);
        const float csv = *(LAS float*)(L + L_CQ + (128 + 32 * pp + tk) * 4);
        f32x16 val;
#pragma unroll
        for (int u = 0; u < 2; ++u) {
          const f32x4 o = LDSF4((hf ? L_XK1 : L_XCH) + ((v ^ 1) * 2 + u) * 1024 + vo);
          const f32x4 t0 = LDSF4(L_FST + (hf * 32 + 16 * kh + 8 * u + 4 * hh) * 8), t1 = LDSF4(L_FST + (hf * 32 + 16 * kh + 8 * u + 4 * hh + 2) * 8);
          val[4 * u + 0] = (acc[hf][4 * u + 0] + o[0]) * t0[0] + csv * t0[1];
          val[4 * u + 1] = (acc[hf][4 * u + 1] + o[1]) * t0[2] + csv * t0[3];
          val[4 * u + 2] = (acc[hf][4 * u + 2] + o[2]) * t1[0] + csv * t1[1];
          val[4 * u + 3] = (acc[hf][4 * u + 3] + o[3]) * t1[2] + csv * t1[3];
        }
        const int hb = (lane >> 4) & 1, d = lane & 15;
        const v4u V = nosent2(pk8(val, 0));
        st_sc1(V, rs, ((4 * j + 2 * pp + hb) * 64) * 2048 + 1024 + (32 * hh + 16 * kh + d) * 16, KV_OFF + (2 * c + hf) * 2048);
        *(LAS v4u*)(L + L_KV + ((8 + hf) * 4 + 2 * pp + hb) * 2048 + 1024 + (32 * hh + 16 * kh + d) * 16) = V;
      };
      if (lay) verify<4>(stA, [&](int i) { return ld_seq(rs, vo, xtb, i); }, tmo, 0x10, lane);
      fill_half<true>(L, 0, v, lane, stA);
      __syncthreads();
      if (v < 4) pass_k(0); else pass_v(0);
      send(0);
      if (lay) verify<4>(stB, [&](int i) { return ld_seq(rs, vo, xtb + 32768, i); }, tmo, 0x11, lane);
      fill_half<true>(L, 1, v, lane, stB);
      __syncthreads();
      {
        const int wqb = RFL(WQ_OFF + (j * 192 + v * 24) * KB);
#pragma unroll
        for (int i = 16; i < 24; ++i) wq[i] = ldw(rs, vo, wqb, i);
        SB();
      }
      if (v < 4) { epi_k(0); pass_k(1); } else { pass_v(1); epi_v(0); }
      send(1);
      __syncthreads();
      if (v < 4) epi_k(1); else epi_v(1);
      {
        const int ho = (v >> 1) & 1;
        const int x0 = L_TILE + ho * 32768 + (v >> 1) * 8192 + vo, x1 = L_TILE + (1 - ho) * 32768 + (v >> 1) * 8192 + vo;
        v4u xs0[3], xs1[3];
#pragma unroll
        for (int s = 0; s < 2; ++s) { xs0[s] = LDS4(x0 + s * 1024); xs1[s] = LDS4(x1 + s * 1024); }
        acc[0] = Z; acc[1] = Z;
        PRIO(1);
#pragma unroll
        for (int s = 0; s < 8; ++s) {
          if (s + 2 < 8) { xs0[(s + 2) % 3] = LDS4(x0 + (s + 2) * 1024); xs1[(s + 2) % 3] = LDS4(x1 + (s + 2) * 1024); }
          SB();
          acc[0] = mfma32(wq[16 + s], xs0[s % 3], acc[0]); acc[1] = mfma32(wq[16 + s], xs1[s % 3], acc[1]);
          wo[s] = ldw(rs, vo, wob, s);
          SB();
        }
        KEEP(acc[0]); KEEP(acc[1]);
        PRIO(0);
#pragma unroll
        for (int u = 0; u < 2; ++u) {
          *(LAS f32x4*)(L + L_XCH + (((v ^ 4) * 3 + 0) * 2 + u) * 1024 + vo) = q4(acc[0], 2 + u);
          *(LAS f32x4*)(L + L_XCH + (((v ^ 2) * 3 + 1) * 2 + u) * 1024 + vo) = q4(acc[1], u);
          *(LAS f32x4*)(L + L_XCH + (((v ^ 6) * 3 + 2) * 2 + u) * 1024 + vo) = q4(acc[1], 2 + u);
        }
      }
      v4u kvn[8];
      auto kv_load = [&](int i) {
        const int kt = 2 * c - 8 + 4 * (v >> 2) + (i >> 1);
        return ld_sc1(rs, vo + (i & 1) * 1024, RFL(kt >= 0 ? KV_OFF + ((4 * j + (v & 3)) * 64 + kt) * 2048 : OOB));
      };
      auto kv_issue = [&]() {
#pragma unroll
        for (int i = 0; i < 8; ++i) kvn[i] = kv_load(i);
      };
      WAITV0();
      __syncthreads();
      kv_issue();
      {
        const int ho = (v >> 1) & 1, hl = 2 * (v & 1) + (v >> 2);
        const f32x2 ab = *(LAS f32x2*)(L + L_FST + (ho * 32 + tk) * 8);
        const float cexp = 0.25f * 1.4426950408889634f;
        f32x16 val;
#pragma unroll
        for (int u = 0; u < 2; ++u) {
          f32x4 o = q4(acc[0], u);
#pragma unroll
          for (int k = 0; k < 3; ++k) o += LDSF4(L_XCH + ((v * 3 + k) * 2 + u) * 1024 + vo);
          const f32x4 csn = LDSF4(L_CQ + (16 * hl + 8 * u + 4 * hh) * 4);
#pragma unroll
          for (int i = 0; i < 4; ++i) val[4 * u + i] = o[i] * (ab.x * cexp) + csn[i] * (ab.y * cexp);
        }
        *(LAS v4u*)(L + L_Q + (hl * 2 + ho) * 1024 + vo) = pk8(val, 0);
      }
      const int w1b = RFL(v < 7 ? W1_OFF + (j * 224 + v * 32) * KB : OOB);
#pragma unroll
      for (int i = 0; i < 4; ++i) w1[i] = ldw(rs, vo, w1b, i);
      SB();
      verify<8>(kvn, kv_load, tmo, 0x20, lane);
      SB();
#pragma unroll
      for (int i = 0; i < 4; ++i) {
        *(LAS v4u*)(L + L_KV + ((4 * (v >> 2) + i) * 4 + (v & 3)) * 2048 + vo) = kvn[2 * i];
        *(LAS v4u*)(L + L_KV + ((4 * (v >> 2) + i) * 4 + (v & 3)) * 2048 + 1024 + vo) = kvn[2 * i + 1];
      }
      __syncthreads();
    }
    {
      FRESH();
      const int hq = v & 3, r = v >> 2, tq = tk - 4 * hh;
      const int kb = L_KV + (r * 4 + hq) * 2048;
      const v4u Q = LDS4(L_Q + (hq * 2 + r) * 1024 + vo);
      v4u Kf[9];
#pragma unroll
      for (int i = 0; i < 9; ++i) Kf[i] = LDS4(kb + i * 8192 + vo);
      const bool ones_lane = (lane & 16) != 0;
      const unsigned vsel = ones_lane ? 0u : 1u, vbase = ones_lane ? (unsigned)L_ONES : (unsigned)(kb + 1024 + (32 * hh + (lane & 15)) * 16);
      auto vop = [&](int i, int s) -> v4u { return LDS4(vbase + vsel * (unsigned)(i * 8192 + s * 256)); };
      const int first_ok = 8 - r - 2 * c;
      v4u SENT4 = {SENT, SENT, SENT, SENT};
      asm volatile("" : "+v"(SENT4));
      const int tsl = (8 * j + 4 * (v & 1) + (v >> 1)) * 512 + tk * 16 + hh * 8;
#pragma unroll
      for (int hf = 0; hf < 2; ++hf) __builtin_amdgcn_raw_buffer_store_b64(v2u{SENT4.x, SENT4.y}, rs, tsl + X1T_OFF + c * TILE_B + hf * 32768, 0, AUX_SC1);
      const int hcb = RFL(v < 7 ? HT_OFF + c * HT_C + (28 * j + 4 * v) * 512 : OOB);
      const int xcb = RFL(lay == 0 ? XT1_OFF + c * TILE_B : OOB), ocb = RFL(lay == 0 ? OT1_OFF + c * TILE_B + (v >> 2) * 32768 + (2 * (4 * j + (v & 3))) * 512 : OOB);
      auto clean = [&](int i) {
        if (i < 4) st_sc1(SENT4, rs, vo, hcb + (i >> 1) * 114688 + 2 * (i & 1) * 512);
        else if (i < 6) __builtin_amdgcn_raw_buffer_store_b64(v2u{SENT4.x, SENT4.y}, rs, tsl + xcb + (i - 4) * 32768, 0, AUX_SC1);
        else st_sc1(SENT4, rs, vo, ocb);
      };
      SB();
      const int w1b = RFL(v < 7 ? W1_OFF + (j * 224 + v * 32) * KB : OOB);
      auto tmax = [&](const f32x16& x, float m) {
        const float a = max3f(x[0], x[1], x[2]), b2_ = max3f(x[3], x[4], x[5]), c2 = max3f(x[6], x[7], x[8]), d = max3f(x[9], x[10], x[11]), e = max3f(x[12], x[13], x[14]);
        return max3f(max3f(a, b2_, x[15]), max3f(c2, d, e), m);
      };
      f32x16 Sm, S[2];
      {
        f32x16 Sa = mfma32(Kf[0], Q, Z);
        const f32x16 Sb = mfma32(Kf[8], Q, Z);
        S[1] = mfma32(Kf[1], Q, Z);
        if (first_ok > 0) {
#pragma unroll
          for (int q = 0; q < 16; ++q) Sa[q] = -1e30f;
        }
#pragma unroll
        for (int q = 0; q < 16; ++q) { const int row = (q & 3) + 8 * (q >> 2); Sm[q] = row <= tq ? Sb[q] : Sa[q]; }
      }
      float mx = tmax(Sm, -1e30f);
      SB();
#pragma unroll
      for (int i = 1; i < 8; ++i) {
        if (i + 1 < 8) S[(i + 1) & 1] = mfma32(Kf[i + 1], Q, Z);
        if (i >= first_ok) mx = tmax(S[i & 1], mx);
        if (i < 6) w1[4 + (i - 1)] = ldw(rs, vo, w1b, 4 + (i - 1));
        if (i == 1) clean(0);
        if (i == 3) clean(4);
        if (i == 5) clean(5);
        SB();
      }
      { const f32x2 rr = both_halves(mx); mx = max3f(rr.x, rr.y, rr.y); }
      f32x16 Cm;
#pragma unroll
      for (int q = 0; q < 16; ++q) Cm[q] = -mx;
      S[1] = mfma32(Kf[1], Q, Cm);
      f32x16 O;
      {
        const v4u vn0 = vop(8, 0), vn1 = vop(8, 1), vo0 = vop(0, 0), vo1 = vop(0, 1);
        unsigned pp[8];
#pragma unroll
        for (int q = 0; q < 8; ++q) pp[q] = pk(__builtin_amdgcn_exp2f(Sm[2 * q] - mx), __builtin_amdgcn_exp2f(Sm[2 * q + 1] - mx));
        v4u pn[2], po[2];
#pragma unroll
        for (int q = 0; q < 8; ++q) {
          const int row0 = ((2 * q) & 3) + 8 * ((2 * q) >> 2);
          const unsigned mk = (row0 <= tq ? 0xFFFFu : 0u) | (row0 + 1 <= tq ? 0xFFFF0000u : 0u);
          pn[q >> 2][q & 3] = pp[q] & mk; po[q >> 2][q & 3] = pp[q] & ~mk;
        }
        O = mfma32(vn0, pn[0], Z); O = mfma32(vn1, pn[1], O);
        O = mfma32(vo0, po[0], O); O = mfma32(vo1, po[1], O);
        SB();
      }
#pragma unroll
      for (int i = 1; i < 8; ++i) {
        if (i + 1 < 8) S[(i + 1) & 1] = mfma32(Kf[i + 1], Q, Cm);
        const v4u va0 = vop(i, 0), va1 = vop(i, 1);
        if (i >= first_ok) {
          const f32x16& cur = S[i & 1];
          v4u pp[2];
#pragma unroll
          for (int q = 0; q < 8; ++q) pp[q >> 2][q & 3] = pk(__builtin_amdgcn_exp2f(cur[2 * q]), __builtin_amdgcn_exp2f(cur[2 * q + 1]));
          O = mfma32(va0, pp[0], O); O = mfma32(va1, pp[1], O);
        }
        w1[9 + (i - 1)] = ldw(rs, vo, w1b, 9 + (i - 1));
        if (i == 1) clean(1);
        if (i == 3) clean(2);
        if (i == 5) clean(3);
        if (i == 7) clean(6);
        SB();
      }
      KEEP(O);
      const float rl = __builtin_amdgcn_rcpf(O[8]);
      asm volatile("s_waitcnt vmcnt(19)" ::: "memory");
      st_sc1(nosent2(swap_T8(pk8(O, 0, rl))), rs, vo, OT_OFF + c * TILE_B + r * 32768 + (2 * (4 * j + hq)) * 512);
      if (v < 4) WAITV0();
    }
    {
      FRESH();
      const int otb = RFL(OT_OFF + c * TILE_B + v * 4096);
      const int w1b = RFL(v < 7 ? W1_OFF + (j * 224 + v * 32) * KB : OOB);
      auto oa_issue = [&]() {
#pragma unroll
        for (int i = 0; i < 4; ++i) stA[i] = ld_seq(rs, vo, otb, i);
      };
      auto ob_issue = [&]() {
#pragma unroll
        for (int i = 0; i < 4; ++i) stB[i] = ld_seq(rs, vo, otb + 32768, i);
      };
      f32x16 a;
      auto pass = [&](int hf) {
        const int x0 = L_TILE + hf * 32768 + (v >> 1) * 8192 + vo;
        v4u xs[8];
#pragma unroll
        for (int s = 0; s < 8; ++s) xs[s] = LDS4(x0 + s * 1024);
        a = Z;
#pragma unroll
        for (int s = 0; s < 8; ++s) a = mfma32(wo[s], xs[s], a);
        KEEP(a);
#pragma unroll
        for (int u = 1; u < 4; ++u) *(LAS f32x4*)(L + L_XCH + ((hf * 8 + (v ^ (2 * u))) * 3 + (u - 1)) * 1024 + vo) = q4(a, u);
      };
      auto epi = [&](int hf) {
        f32x4 o = q4(a, 0);
#pragma unroll
        for (int k = 0; k < 3; ++k) o += LDSF4(L_XCH + ((hf * 8 + v) * 3 + k) * 1024 + vo);
        xr[hf] = pk4(unpk4(xr[hf]) + o);
        __builtin_amdgcn_raw_buffer_store_b64(v2u{nosent(xr[hf].x), xr[hf].y}, rs, (8 * j + 4 * (v & 1) + (v >> 1)) * 512 + tk * 16 + hh * 8 + X1T_OFF + c * TILE_B + hf * 32768, 0, AUX_SC1);
      };
      oa_issue();
#pragma unroll
      for (int i = 16; i < 24; ++i) w1[i] = ldw(rs, vo, w1b, i);
      SB();
      verify<4>(stA, [&](int i) { return ld_seq(rs, vo, otb, i); }, tmo, 0x30, lane);
      __syncthreads();
      fill_half<false>(L, 0, v, lane, stA);
      ob_issue();
      __syncthreads();
      pass(0);
      verify<4>(stB, [&](int i) { return ld_seq(rs, vo, otb + 32768, i); }, tmo, 0x31, lane);
      fill_half<false>(L, 1, v, lane, stB);
      __syncthreads();
      epi(0);
      pass(1);
#pragma unroll
      for (int i = 24; i < 32; ++i) w1[i] = ldw(rs, vo, w1b, i);
      __syncthreads();
#pragma unroll
      for (int i = 0; i < 4; ++i) stA[i] = ld_seq(rs, vo, RFL(X1T_OFF + c * TILE_B + v * 4096), i);
      SB();
      epi(1);
    }
    v4u hA[14];
    {
      FRESH();
      const int x1b = RFL(X1T_OFF + c * TILE_B + v * 4096);
      const int w2b = RFL(W2_OFF + (j * 224 + v * 28) * KB);
      const int hb = RFL(v < 7 ? HT_OFF + c * HT_C + (28 * j + 4 * v) * 512 : OOB);
      auto xb_issue = [&]() {
#pragma unroll
        for (int i = 0; i < 4; ++i) stB[i] = ld_seq(rs, vo, x1b + 32768, i);
      };
      f32x16 a;
      auto pass = [&](int hf) {
        v4u xs[3];
        const int x0 = L_TILE + hf * 32768 + vo;
#pragma unroll
        for (int s = 0; s < 2; ++s) xs[s] = LDS4(x0 + s * 1024);
        a = Z;
        PRIO(1);
#pragma unroll
        for (int s = 0; s < 32; ++s) {
          if (s + 2 < 32) xs[(s + 2) % 3] = LDS4(x0 + (s + 2) * 1024);
          SB();
          a = mfma32(w1[s], xs[s % 3], a);
          if (hf == 0 && s == 8) xb_issue();
          if (hf == 0 && s >= 22) w2[s - 22] = ldw(rs, vo, w2b, s - 22);
          if (hf == 1 && s >= 2 && s < 20) w2[10 + (s - 2)] = ldw(rs, vo, w2b, 10 + (s - 2));
          SB();
        }
        KEEP(a);
        PRIO(0);
      };
      v4u ho[2];
      auto gelu = [&](int hf) {
        const f32x2 ab = final_stats<true>(L, lane, hf);
        const float c1 = -2.0f * 0.7978845608028654f * 1.4426950408889634f, c2 = c1 * 0.044715f;
#pragma unroll
        for (int u2 = 0; u2 < 2; ++u2) {
          f32x16 hv;
#pragma unroll
          for (int u = 2 * u2; u < 2 * u2 + 2; ++u) {
            const f32x4 csn = LDSF4(L_C1 + (32 * v + 8 * u + 4 * hh) * 4), bn = LDSF4(L_C1 + 1024 + (32 * v + 8 * u + 4 * hh) * 4);
#pragma unroll
            for (int i = 0; i < 4; ++i) {
              const float z = a[4 * u + i] * ab.x + (csn[i] * ab.y + bn[i]);
              const float e = __builtin_amdgcn_exp2f(z * (z * z * c2 + c1));
              hv[4 * u + i] = z * __builtin_amdgcn_rcpf(1.0f + e);
            }
          }
          ho[u2] = nosent2(swap_T8(pk8(hv, 8 * u2)));
          if (hf == 0) st_sc1(ho[u2], rs, vo, hb + 2 * u2 * 512);
        }
      };
      verify<4>(stA, [&](int i) { return ld_seq(rs, vo, x1b, i); }, tmo, 0x40, lane);
      fill_half<true>(L, 0, v, lane, stA);
      __syncthreads();
      pass(0);
      gelu(0);
      verify<4>(stB, [&](int i) { return ld_seq(rs, vo, x1b + 32768, i); }, tmo, 0x41, lane);
      fill_half<true>(L, 1, v, lane, stB);
      __syncthreads();
      pass(1);
#pragma unroll
      for (int s = 0; s < 14; ++s) hA[s] = ld_seq(rs, vo, RFL(HT_OFF + c * HT_C + 28 * v * 512), s);
      SB();
      gelu(1);
#pragma unroll
      for (int u2 = 0; u2 < 2; ++u2) st_sc1(ho[u2], rs, vo, hb + 114688 + 2 * u2 * 512);
    }
    {
      FRESH();
      const int htb = RFL(HT_OFF + c * HT_C + 28 * v * 512);
      const int wqb = RFL(lay == 0 ? WQ_OFF + (j * 192 + v * 24) * KB : OOB);
      const int xnb = RFL(lay == 0 ? XT1_OFF + c * TILE_B + v * 4096 : OOB);
#pragma unroll
      for (int hf = 0; hf < 2; ++hf) {
        verify<14>(hA, [&](int i) { return ld_seq(rs, vo, htb + hf * 114688, i); }, tmo, 0x50 + hf, lane);
        if (hf == 0) __syncthreads();
        f32x16 ac[2] = {Z, Z};
        PRIO(1);
#pragma unroll
        for (int s = 0; s < 14; ++s) {
          ac[0] = mfma32(w2[2 * s], hA[s], ac[0]);
          ac[1] = mfma32(w2[2 * s + 1], hA[s], ac[1]);
          if (hf == 1) wq[s] = ldw(rs, vo, wqb, s);
          else hA[s] = ld_seq(rs, vo, htb + 114688, s);
          SB();
        }
        KEEP(ac[0]); KEEP(ac[1]);
        PRIO(0);
        LAS unsigned char* R = L + (hf ? L_TILE : L_RB);
#pragma unroll
        for (int nt = 0; nt < 2; ++nt)
#pragma unroll
          for (int u = 0; u < 4; ++u) *(LAS f32x4*)(R + ((nt + 2 * u) * 8 + v) * 1024 + vo) = q4(ac[nt], u);
        if (hf == 1) { wq[14] = ldw(rs, vo, wqb, 14); wq[15] = ldw(rs, vo, wqb, 15); }
        __syncthreads();
        f32x4 s0 = {0, 0, 0, 0};
#pragma unroll
        for (int k = 0; k < 8; ++k) s0 += *(LAS f32x4*)(R + (v * 8 + k) * 1024 + vo);
        const f32x4 b0 = LDSF4(L_CQ + (192 + 32 * (v & 1) + 8 * (v >> 1) + 4 * hh) * 4);
        xr[hf] = pk4(unpk4(xr[hf]) + s0 + b0);
        if (hf == 1) {
#pragma unroll
          for (int i = 0; i < 4; ++i) stA[i] = ld_seq(rs, vo, xnb, i);
          SB();
        }
        if (lay == 0) {
          __builtin_amdgcn_raw_buffer_store_b64(v2u{nosent(xr[hf].x), xr[hf].y}, rs, (8 * j + 4 * (v & 1) + (v >> 1)) * 512 + tk * 16 + hh * 8 + XT1_OFF + c * TILE_B + hf * 32768, 0, AUX_SC1);
        } else {
          *(v2u*)(p.out + (size_t)(64 * c + 32 * hf + tk) * DM + 64 * j + 32 * (v & 1) + 8 * (v >> 1) + 4 * hh) = xr[hf];
        }
      }
      __syncthreads();
    }
  }
}
}

extern "C" void kernel_launch(void* const* d_in, const int* in_sizes, int n_in, void* d_out, int out_size, void* d_ws, size_t ws_size,
                              hipStream_t stream) {
  using namespace fk;
  Params p;
  p.x = (const u16*)d_in[0]; p.wqkv = (const u16*)d_in[1]; p.wo = (const u16*)d_in[2];
  p.w1 = (const u16*)d_in[4]; p.b1 = (const u16*)d_in[5]; p.w2 = (const u16*)d_in[6]; p.b2 = (const u16*)d_in[7];
  p.out = (u16*)d_out; p.ws = (unsigned char*)d_ws;
  (void)hipFuncSetAttribute((const void*)k_main, hipFuncAttributeMaxDynamicSharedMemorySize, L_BYTES);
  k_prep<<<PREP_BLOCKS, 256, 0, stream>>>(p);
  k_main<<<256, 512, L_BYTES, stream>>>(p);
}
```

```cpp
#include <hip/hip_runtime.h>
#include <stdint.h>

namespace fk {
typedef unsigned short u16;
typedef unsigned v4u __attribute__((ext_vector_type(4)));
typedef unsigned v2u __attribute__((ext_vector_type(2)));
typedef float f32x4 __attribute__((ext_vector_type(4)));
typedef float f32x2 __attribute__((ext_vector_type(2)));
typedef float f32x16 __attribute__((ext_vector_type(16)));
typedef __bf16 bf8v __attribute__((ext_vector_type(8)));
typedef __bf16 bf2v __attribute__((ext_vector_type(2)));
#define LAS __attribute__((address_space(3)))
#define GAS __attribute__((address_space(1)))
typedef __amdgpu_buffer_rsrc_t rsrc_t;

constexpr int SEQ = 2048, DM = 512, FF = 1792;
constexpr int KB = 1024;
constexpr int WQ_OFF = 0;
constexpr int WO_OFF = WQ_OFF + 8 * 192 * KB;
constexpr int W1_OFF = WO_OFF + 8 * 64 * KB;
constexpr int W2_OFF = W1_OFF + 8 * 224 * KB;
constexpr int CS_OFF = W2_OFF + 8 * 224 * KB;
constexpr int CTRL_OFF = CS_OFF + 16 * KB;
constexpr int TILE_B = 64 * KB;
constexpr int KV_B = 32 * 64 * 2 * KB;
constexpr int HT_C = 2 * 224 * 512;
constexpr int KV0_OFF = CTRL_OFF + 16 * KB;
constexpr int OT0_OFF = KV0_OFF + KV_B;
constexpr int KV1_OFF = OT0_OFF + 32 * TILE_B;
constexpr int OT1_OFF = KV1_OFF + KV_B;
constexpr int X1T0_OFF = OT1_OFF + 32 * TILE_B;
constexpr int X1T1_OFF = X1T0_OFF + 32 * TILE_B;
constexpr int XT1_OFF = X1T1_OFF + 32 * TILE_B;
constexpr int HT0_OFF = XT1_OFF + 32 * TILE_B;
constexpr int HT1_OFF = HT0_OFF + 32 * HT_C;
constexpr int WS_END = HT1_OFF + 32 * HT_C;
constexpr unsigned SENT = 0xFFFFFFFFu;
constexpr int AUX_SC1 = 16;
constexpr unsigned SPIN_CAP = 1u << 14;

constexpr int L_TILE = 0;
constexpr int L_KV = 0;
constexpr int L_RB = 64 * KB;
constexpr int L_Q = 80 * KB;
constexpr int L_XK1 = 88 * KB;
constexpr int L_XCH = 104 * KB;
constexpr int L_PST = 152 * KB;
constexpr int L_FST = 156 * KB;
constexpr int L_C1 = 156 * KB + 512;
constexpr int L_CQ = 158 * KB + 512;
constexpr int L_ONES = 159 * KB + 512;
constexpr int L_BYTES = 159 * KB + 576;

__device__ __forceinline__ float bf2f(unsigned b) { return __builtin_bit_cast(float, b << 16); }
__device__ __forceinline__ float bflo(unsigned d) { return __builtin_bit_cast(float, d << 16); }
__device__ __forceinline__ float bfhi(unsigned d) { return __builtin_bit_cast(float, d & 0xFFFF0000u); }
__device__ __forceinline__ unsigned pk(float lo, float hi) {
  bf2v r = {(__bf16)lo, (__bf16)hi};
  return __builtin_bit_cast(unsigned, r);
}
__device__ __forceinline__ unsigned nosent(unsigned d) { return d < 0xFFFFFFFEu ? d : 0xFFFFFFFEu; }
__device__ __forceinline__ v2u pk4(f32x4 a) { return v2u{pk(a[0], a[1]), pk(a[2], a[3])}; }
__device__ __forceinline__ f32x4 unpk4(v2u v) { return f32x4{bflo(v.x), bfhi(v.x), bflo(v.y), bfhi(v.y)}; }
#define KEEP(x) asm volatile("" : "+v"(x))
#define WAITV0() asm volatile("s_waitcnt vmcnt(0)" ::: "memory")
#define PRIO(x) __builtin_amdgcn_s_setprio(x)
#define SB() __builtin_amdgcn_sched_barrier(0)

__device__ __forceinline__ v4u ldw(rsrc_t rs, int voff, int sbase, int i) {
  return __builtin_amdgcn_raw_buffer_load_b128(rs, voff + (i & 3) * 1024, sbase + (i >> 2) * 4096, 0);
}
__device__ __forceinline__ v4u ld_sc1(rsrc_t rs, int voff, int soff) {
  return __builtin_amdgcn_raw_buffer_load_b128(rs, voff, soff, AUX_SC1); }
__device__ __forceinline__ v4u ld_seq(rsrc_t rs, int voff, int sbase, int i) { return ld_sc1(rs, voff + (i & 3) * 1024, sbase + (i >> 2) * 4096); }
#define RFL(x) __builtin_amdgcn_readfirstlane(x)
__device__ __forceinline__ void st_sc1(v4u v, rsrc_t rs, int voff, int soff) {
  __builtin_amdgcn_raw_buffer_store_b128(v, rs, voff + soff, 0, AUX_SC1); }
__device__ __forceinline__ bool spin_fail(unsigned& spins, GAS unsigned* tmo, unsigned code, int lane) {
  ++spins;
  if (spins > SPIN_CAP) {
    if (lane == 0) __hip_atomic_store(tmo, code, __ATOMIC_RELAXED, __HIP_MEMORY_SCOPE_AGENT);
    return true;
  }
  if ((spins & 63u) == 0u && __hip_atomic_load(tmo, __ATOMIC_RELAXED, __HIP_MEMORY_SCOPE_AGENT) != 0u) return true;
  return false;
}

struct Params {
  const u16* x;
  const u16* wqkv;
  const u16* wo;
  const u16* w1;
  const u16* b1;
  const u16* w2;
  const u16* b2;
  u16* out;
  unsigned char* ws;
};

constexpr int NFRAG = 8 * 704;
constexpr int PB_TILE = NFRAG * 16 / 256;
constexpr int PB_CS = (1536 + 1792) / 64;
constexpr int CLEAN_B = KV_B + 32 * TILE_B;
constexpr int PB_CLEAN = CLEAN_B / (256 * 64);
constexpr int PREP_BLOCKS = PB_TILE + PB_CS + PB_CLEAN;

__global__ void __launch_bounds__(256) k_prep(Params p) {
  const int b = blockIdx.x, tid = threadIdx.x;
  if (b < PB_TILE) {
    const int F = b * 16 + (tid >> 4), t16 = tid & 15, cq = t16 & 7, h = t16 >> 3;
    const u16* W; int ldw_, k0, col;
    if (F < 8 * 192) {
      const int j = F / 192, v = (F % 192) / 24, i = F % 24;
      W = p.wqkv; ldw_ = 1536;
      if (i < 16) {
        k0 = 16 * (16 * (v & 1) + i);
        col = v < 4 ? 512 + (4 * j + 2 * (v >> 1)) * 16 + 4 * (cq ^ (4 * (v & 1))) : 1024 + (4 * j + 2 * ((v - 4) >> 1)) * 16 + 4 * cq;
      } else {
        k0 = 16 * (8 * (v >> 1) + i - 16);
        col = (4 * j + 2 * (v & 1)) * 16 + 4 * (cq ^ (4 * (v >> 2)));
      }
    } else if (F < 8 * 256) {
      const int f = F - 8 * 192, j = f >> 6, u = f & 63, v = u >> 3;
      W = p.wo; ldw_ = 512; k0 = 16 * (8 * (v >> 1) + (u & 7)); col = 64 * j + 32 * (v & 1) + 4 * (cq ^ (2 * (v >> 1)));
    } else if (F < 8 * 480) {
      const int f = F - 8 * 256, j = f / 224, u = f % 224;
      W = p.w1; ldw_ = 1792; k0 = 16 * (u & 31); col = 224 * j + 32 * (u >> 5) + 4 * cq;
    } else {
      const int f = F - 8 * 480, j = f / 224, u = f % 224, v = u / 28, q = u % 28;
      W = p.w2; ldw_ = 512; k0 = 224 * v + 16 * (q >> 1); col = 64 * j + 32 * (q & 1) + 4 * cq;
    }
    const u16* src = W + (size_t)(k0 + 8 * h) * ldw_ + col;
    v2u r[8];
#pragma unroll
    for (int e = 0; e < 8; ++e) r[e] = *(const v2u*)(src + (size_t)e * ldw_);
    v4u o[4];
#pragma unroll
    for (int e = 0; e < 4; ++e) {
      const unsigned a0 = r[2 * e].x, a1 = r[2 * e + 1].x, c0 = r[2 * e].y, c1 = r[2 * e + 1].y;
      o[0][e] = (a0 & 0xFFFFu) | (a1 << 16);
      o[1][e] = (a0 >> 16) | (a1 & 0xFFFF0000u);
      o[2][e] = (c0 & 0xFFFFu) | (c1 << 16);
      o[3][e] = (c0 >> 16) | (c1 & 0xFFFF0000u);
    }
    v4u* dst = (v4u*)(p.ws + (size_t)F * 1024 + (h * 32 + 4 * cq) * 16);
#pragma unroll
    for (int i = 0; i < 4; ++i) dst[i] = o[i];
  } else if (b < PB_TILE + PB_CS) {
    __shared__ float red[16][64];
    const int cb = b - PB_TILE;
    const u16* W; int ldw_, col;
    if (cb < 24) { W = p.wqkv; ldw_ = 1536; col = cb * 64; } else { W = p.w1; ldw_ = 1792; col = (cb - 24) * 64; }
    const int cq = tid & 15, rg = tid >> 4;
    float s0 = 0, s1 = 0, s2 = 0, s3 = 0;
    const u16* src = W + (size_t)(rg * 32) * ldw_ + col + 4 * cq;
#pragma unroll
    for (int e = 0; e < 32; ++e) {
      const v2u v = *(const v2u*)(src + (size_t)e * ldw_);
      s0 += bflo(v.x); s1 += bfhi(v.x); s2 += bflo(v.y); s3 += bfhi(v.y);
    }
    red[rg][4 * cq + 0] = s0; red[rg][4 * cq + 1] = s1; red[rg][4 * cq + 2] = s2; red[rg][4 * cq + 3] = s3;
    __syncthreads();
    if (tid < 64) {
      float s = 0;
#pragma unroll
      for (int r = 0; r < 16; ++r) s += red[r][tid];
      ((float*)(p.ws + CS_OFF))[cb * 64 + tid] = s;
    }
    if (cb == 0 && tid == 0) *(unsigned*)(p.ws + CTRL_OFF) = 0u;
  } else {
    const int cb = b - PB_TILE - PB_CS;
    v4u* dst = (v4u*)(p.ws + KV0_OFF + (size_t)cb * (256 * 64)) + tid;
    const v4u s = {SENT, SENT, SENT, SENT};
#pragma unroll
    for (int i = 0; i < 4; ++i) dst[i * 256] = s;
  }
}


__device__ __forceinline__ float max3f(float a, float b, float c) { float r; asm("v_max3_f32 %0, %1, %2, %3" : "=v"(r) : "v"(a), "v"(b), "v"(c)); return r; }
__device__ __forceinline__ unsigned max3u(unsigned a, unsigned b, unsigned c) { unsigned r; asm("v_max3_u32 %0, %1, %2, %3" : "=v"(r) : "v"(a), "v"(b), "v"(c)); return r; }
__device__ __forceinline__ f32x16 mfma32(v4u a, v4u b, f32x16 c) {
  return __builtin_amdgcn_mfma_f32_32x32x16_bf16(__builtin_bit_cast(bf8v, a), __builtin_bit_cast(bf8v, b), c, 0, 0, 0);
}
template <int N> __device__ __forceinline__ bool any_sent(const v4u (&d)[N]) {
  unsigned m0 = 0, m1 = 0;
#pragma unroll
  for (int i = 0; i < N; ++i) { if (i & 1) m1 = max3u(m1, d[i].x, d[i].z); else m0 = max3u(m0, d[i].x, d[i].z); }
  return __any(max3u(m0, m1, 0) == SENT);
}
template <int N, class F> __device__ __forceinline__ void verify(v4u (&d)[N], F load_i, GAS unsigned* tmo, unsigned code, int lane) {
  if (__builtin_expect(any_sent<N>(d), 0)) {
    unsigned spins = 0;
    do {
      __builtin_amdgcn_s_sleep(1);
      asm volatile("" ::: "memory");
#pragma unroll
      for (int i = 0; i < N; ++i)
        if (__any(max3u(d[i].x, d[i].z, 0u) == SENT)) d[i] = load_i(i);
    } while (any_sent<N>(d) && !spin_fail(spins, tmo, code, lane));
  }
}
__device__ __forceinline__ f32x2 both_halves(float v) {
  const unsigned a = __builtin_bit_cast(unsigned, v);
  auto r = __builtin_amdgcn_permlane32_swap(a, a, false, false);
  const unsigned r0 = r[0], r1 = r[1];
  return f32x2{__builtin_bit_cast(float, r0), __builtin_bit_cast(float, r1)};
}
__device__ __forceinline__ float dot2(unsigned a, unsigned b, float c) {
  return __builtin_amdgcn_fdot2_f32_bf16(__builtin_bit_cast(bf2v, a), __builtin_bit_cast(bf2v, b), c, false);
}
__device__ __forceinline__ v4u nosent2(v4u v) { return v4u{nosent(v.x), v.y, nosent(v.z), v.w}; }

__device__ __forceinline__ v4u pk8(const f32x16& a, int r0, float sc = 1.0f) {
  return v4u{pk(a[r0] * sc, a[r0 + 1] * sc), pk(a[r0 + 2] * sc, a[r0 + 3] * sc), pk(a[r0 + 4] * sc, a[r0 + 5] * sc), pk(a[r0 + 6] * sc, a[r0 + 7] * sc)};
}
__device__ __forceinline__ v4u swap_T8(v4u p) {
  auto r0 = __builtin_amdgcn_permlane32_swap(p[0], p[2], false, false);
  auto r1 = __builtin_amdgcn_permlane32_swap(p[1], p[3], false, false);
  return v4u{r0[0], r1[0], r0[1], r1[1]};
}
__device__ __forceinline__ f32x4 q4(const f32x16& a, int u) { return f32x4{a[4 * u], a[4 * u + 1], a[4 * u + 2], a[4 * u + 3]}; }
#define FRESH() int lane = lane_, c = c_, j = j_, v = v_; asm volatile("" : "+v"(lane), "+s"(c), "+s"(j), "+s"(v)); \
  const int vo = lane * 16, tk = lane & 31, hh = lane >> 5; (void)vo; (void)tk; (void)hh
#define LDS4(off) (*(const LAS v4u*)(L + (off)))
#define LDSF4(off) (*(const LAS f32x4*)(L + (off)))
constexpr int OOB = 0x7FF00000;

template <bool STATS>
__device__ __forceinline__ void fill_half(LAS unsigned char* L, int hf, int v, int lane, const v4u (&d)[4]) {
#pragma unroll
  for (int i = 0; i < 4; ++i) *(LAS v4u*)(L + L_TILE + hf * 32768 + v * 4096 + i * 1024 + lane * 16) = d[i];
  if (STATS) {
    float s0 = 0.f, s1 = 0.f, q0 = 0.f, q1 = 0.f;
#pragma unroll
    for (int i = 0; i < 4; ++i) {
      const v4u x = d[i];
      s0 = dot2(x[0], 0x3F803F80u, s0); q0 = dot2(x[0], x[0], q0);
      s1 = dot2(x[1], 0x3F803F80u, s1); q1 = dot2(x[1], x[1], q1);
      s0 = dot2(x[2], 0x3F803F80u, s0); q0 = dot2(x[2], x[2], q0);
      s1 = dot2(x[3], 0x3F803F80u, s1); q1 = dot2(x[3], x[3], q1);
    }
    const f32x2 ss = both_halves(s0 + s1), qq = both_halves(q0 + q1);
    if (lane < 32) *(LAS f32x2*)(L + L_PST + ((hf * 8 + v) * 32 + lane) * 8) = f32x2{ss.x + ss.y, qq.x + qq.y};
  }
}
template <bool DOUBLE_LN>
__device__ __forceinline__ f32x2 final_stats(LAS unsigned char* L, int lane, int hf) {
  const int t = lane & 31, hh = lane >> 5;
  f32x2 pv[4];
#pragma unroll
  for (int k = 0; k < 4; ++k) pv[k] = *(LAS f32x2*)(L + L_PST + ((hf * 8 + hh * 4 + k) * 32 + t) * 8);
  SB();
  const f32x2 s = (pv[0] + pv[1]) + (pv[2] + pv[3]);
  const f32x2 rs_ = both_halves(s.x), rq = both_halves(s.y);
  const float mean = (rs_.x + rs_.y) * (1.0f / DM);
  float var = (rq.x + rq.y) * (1.0f / DM) - mean * mean;
  var = var > 0.f ? var : 0.f;
  float r = __builtin_amdgcn_rsqf(var + 1e-5f);
  if (DOUBLE_LN) {
    const float v2 = var * r * r;
    r *= __builtin_amdgcn_rsqf(v2 + 1e-5f);
  }
  const f32x2 ab = {r, -mean * r};
  *(LAS f32x2*)(L + L_FST + (hf * 32 + t) * 8) = ab;
  return ab;
}

__global__ void __launch_bounds__(512, 2) k_main(Params p) {
  extern __shared__ __attribute__((aligned(16))) unsigned char smem[];
  LAS unsigned char* L = (LAS unsigned char*)smem;
  const int b = blockIdx.x, tid = threadIdx.x, lane_ = tid & 63, v_ = __builtin_amdgcn_readfirstlane(tid >> 6);
  int c_, j_;
  c_ = ((b & 7) >> 1) * 8 + (b >> 5); j_ = (b & 1) * 4 + ((b >> 3) & 3);
  const rsrc_t rs = __builtin_amdgcn_make_buffer_rsrc((void*)p.ws, 0, WS_END, 0x00020000);
  const rsrc_t rx = __builtin_amdgcn_make_buffer_rsrc((void*)p.x, 0, SEQ * DM * 2, 0x00020000);
  GAS unsigned* tmo = (GAS unsigned*)(p.ws + CTRL_OFF);
  v2u xr[2]; v4u wq[24], stA[4];
  f32x16 Z;
#pragma unroll
  for (int q = 0; q < 16; ++q) Z[q] = 0.f;
  {
    FRESH();
    const float* cs = (const float*)(p.ws + CS_OFF);
    float k0 = 0.f, k1 = 0.f;
    if (tid < 224) { k0 = cs[1536 + 224 * j + tid]; k1 = bf2f(p.b1[224 * j + tid]); }
    else if (tid >= 256) { const int t = tid - 256; k0 = t < 192 ? cs[(t >> 6) * 512 + 64 * j + (t & 63)] : bf2f(p.b2[64 * j + t - 192]); }
#pragma unroll
    for (int hf = 0; hf < 2; ++hf) xr[hf] = *(const v2u*)(p.x + (size_t)(64 * c + 32 * hf + tk) * DM + 64 * j + 32 * (v & 1) + 8 * (v >> 1) + 4 * hh);
#pragma unroll
    for (int i = 0; i < 4; ++i) stA[i] = __builtin_amdgcn_raw_buffer_load_b128(rx, tk * 1024 + hh * 16 + i * 32, (64 * c) * 1024 + v * 128, 0);
    SB();
    __builtin_amdgcn_s_barrier();
    SB();
    const int wqb = RFL(WQ_OFF + (j * 192 + v * 24) * KB);
#pragma unroll
    for (int i = 0; i < 16; ++i) wq[i] = ldw(rs, vo, wqb, i);
    SB();
    if (tid < 224) { *(LAS float*)(L + L_C1 + tid * 4) = k0; *(LAS float*)(L + L_C1 + 1024 + tid * 4) = k1; }
    else if (tid >= 256) *(LAS float*)(L + L_CQ + (tid - 256) * 4) = k0;
    else if (tid < 228) *(LAS unsigned*)(L + L_ONES + (tid - 224) * 4) = 0x3F803F80u;
    v4u SENT4 = {SENT, SENT, SENT, SENT};
    asm volatile("" : "+v"(SENT4));
#pragma unroll
    for (int hf = 0; hf < 2; ++hf) {
      if (v < 4) st_sc1(SENT4, rs, vo, KV1_OFF + ((4 * j + v) * 64 + 2 * c + hf) * 2048);
      else st_sc1(SENT4, rs, ((4 * j + 2 * ((v - 4) >> 1) + ((lane >> 4) & 1)) * 64) * 2048 + 1024 + (32 * hh + 16 * (v & 1) + (lane & 15)) * 16, KV1_OFF + (2 * c + hf) * 2048);
    }
    SB();
  }

  for (int lay = 0; lay < 2; ++lay) {
    const int KV_OFF = lay ? KV1_OFF : KV0_OFF, OT_OFF = lay ? OT1_OFF : OT0_OFF, X1T_OFF = lay ? X1T1_OFF : X1T0_OFF, HT_OFF = lay ? HT1_OFF : HT0_OFF;
    v4u wo[8], w1[32], w2[28], stB[4];
    {
      FRESH();
      const int xtb = RFL(XT1_OFF + c * TILE_B + v * 4096);
      const int wob = RFL(WO_OFF + (j * 64 + v * 8) * KB);
      const int kh = v & 1;
      f32x16 acc[2];
      auto xb_issue = [&]() {
        if (lay) {
#pragma unroll
          for (int i = 0; i < 4; ++i) stB[i] = ld_seq(rs, vo, xtb + 32768, i);
        } else {
#pragma unroll
          for (int i = 0; i < 4; ++i) stB[i] = __builtin_amdgcn_raw_buffer_load_b128(rx, tk * 1024 + hh * 16 + i * 32, (64 * c + 32) * 1024 + v * 128, 0);
        }
      };
      auto pass_k = [&](int hf) {
        v4u xs[3];
        const int x0 = L_TILE + hf * 32768 + kh * 16384 + vo;
#pragma unroll
        for (int s = 0; s < 2; ++s) xs[s] = LDS4(x0 + s * 1024);
        acc[hf] = Z;
        PRIO(1);
#pragma unroll
        for (int s = 0; s < 16; ++s) {
          if (s + 2 < 16) xs[(s + 2) % 3] = LDS4(x0 + (s + 2) * 1024);
          SB();
          acc[hf] = mfma32(wq[s], xs[s % 3], acc[hf]);
          if (hf == 0 && s == 8) xb_issue();
          SB();
        }
        KEEP(acc[hf]);
        PRIO(0);
      };
      auto pass_v = [&](int hf) {
        v4u xs[3];
        const int x0 = L_TILE + hf * 32768 + kh * 16384 + (vo ^ (kh * 256));
#pragma unroll
        for (int s = 0; s < 2; ++s) xs[s] = LDS4(x0 + s * 1024);
        acc[hf] = Z;
        PRIO(1);
#pragma unroll
        for (int s = 0; s < 16; ++s) {
          if (s + 2 < 16) xs[(s + 2) % 3] = LDS4(x0 + (s + 2) * 1024);
          SB();
          acc[hf] = mfma32(xs[s % 3], wq[s], acc[hf]);
          if (hf == 0 && s == 8) xb_issue();
          SB();
        }
        KEEP(acc[hf]);
        PRIO(0);
      };
      auto send = [&](int hf) {
#pragma unroll
        for (int u = 0; u < 2; ++u) *(LAS f32x4*)(L + (hf ? L_XK1 : L_XCH) + (v * 2 + u) * 1024 + vo) = q4(acc[hf], 2 + u);
      };
      auto epi_k = [&](int hf) {
        const f32x2 ab = final_stats<false>(L, lane, hf);
        f32x16 val;
#pragma unroll
        for (int u = 0; u < 2; ++u) {
          const f32x4 o = LDSF4((hf ? L_XK1 : L_XCH) + ((v ^ 1) * 2 + u) * 1024 + vo);
          const f32x4 csn = LDSF4(L_CQ + (64 + 16 * v + 8 * u + 4 * hh) * 4);
#pragma unroll
          for (int i = 0; i < 4; ++i) val[4 * u + i] = (acc[hf][4 * u + i] + o[i]) * ab.x + csn[i] * ab.y;
        }
        const v4u K = nosent2(pk8(val, 0));
        st_sc1(K, rs, vo, KV_OFF + ((4 * j + v) * 64 + 2 * c + hf) * 2048);
        *(LAS v4u*)(L + L_KV + ((8 + hf) * 4 + v) * 2048 + vo) = K;
      };
      auto epi_v = [&](int hf) {
        const int pp = (v - 4) >> 1;
        (void)final_stats<false>(L, lane, hf);
        const float csv = *(LAS float*)(L + L_CQ + (128 + 32 * pp + tk) * 4);
        f32x16 val;
#pragma unroll
        for (int u = 0; u < 2; ++u) {
          const f32x4 o = LDSF4((hf ? L_XK1 : L_XCH) + ((v ^ 1) * 2 + u) * 1024 + vo);
          const f32x4 t0 = LDSF4(L_FST + (hf * 32 + 16 * kh + 8 * u + 4 * hh) * 8), t1 = LDSF4(L_FST + (hf * 32 + 16 * kh + 8 * u + 4 * hh + 2) * 8);
          val[4 * u + 0] = (acc[hf][4 * u + 0] + o[0]) * t0[0] + csv * t0[1];
          val[4 * u + 1] = (acc[hf][4 * u + 1] + o[1]) * t0[2] + csv * t0[3];
          val[4 * u + 2] = (acc[hf][4 * u + 2] + o[2]) * t1[0] + csv * t1[1];
          val[4 * u + 3] = (acc[hf][4 * u + 3] + o[3]) * t1[2] + csv * t1[3];
        }
        const int hb = (lane >> 4) & 1, d = lane & 15;
        const v4u V = nosent2(pk8(val, 0));
        st_sc1(V, rs, ((4 * j + 2 * pp + hb) * 64) * 2048 + 1024 + (32 * hh + 16 * kh + d) * 16, KV_OFF + (2 * c + hf) * 2048);
        *(LAS v4u*)(L + L_KV + ((8 + hf) * 4 + 2 * pp + hb) * 2048 + 1024 + (32 * hh + 16 * kh + d) * 16) = V;
      };
      if (lay) verify<4>(stA, [&](int i) { return ld_seq(rs, vo, xtb, i); }, tmo, 0x10, lane);
      fill_half<true>(L, 0, v, lane, stA);
      __syncthreads();
      if (v < 4) pass_k(0); else pass_v(0);
      send(0);
      if (lay) verify<4>(stB, [&](int i) { return ld_seq(rs, vo, xtb + 32768, i); }, tmo, 0x11, lane);
      fill_half<true>(L, 1, v, lane, stB);
      __syncthreads();
      {
        const int wqb = RFL(WQ_OFF + (j * 192 + v * 24) * KB);
#pragma unroll
        for (int i = 16; i < 24; ++i) wq[i] = ldw(rs, vo, wqb, i);
        SB();
      }
      if (v < 4) { epi_k(0); pass_k(1); } else { pass_v(1); epi_v(0); }
      send(1);
      __syncthreads();
      if (v < 4) epi_k(1); else epi_v(1);
      {
        const int ho = (v >> 1) & 1;
        const int x0 = L_TILE + ho * 32768 + (v >> 1) * 8192 + vo, x1 = L_TILE + (1 - ho) * 32768 + (v >> 1) * 8192 + vo;
        v4u xs0[3], xs1[3];
#pragma unroll
        for (int s = 0; s < 2; ++s) { xs0[s] = LDS4(x0 + s * 1024); xs1[s] = LDS4(x1 + s * 1024); }
        acc[0] = Z; acc[1] = Z;
        PRIO(1);
#pragma unroll
        for (int s = 0; s < 8; ++s) {
          if (s + 2 < 8) { xs0[(s + 2) % 3] = LDS4(x0 + (s + 2) * 1024); xs1[(s + 2) % 3] = LDS4(x1 + (s + 2) * 1024); }
          SB();
          acc[0] = mfma32(wq[16 + s], xs0[s % 3], acc[0]); acc[1] = mfma32(wq[16 + s], xs1[s % 3], acc[1]);
          wo[s] = ldw(rs, vo, wob, s);
          SB();
        }
        KEEP(acc[0]); KEEP(acc[1]);
        PRIO(0);
#pragma unroll
        for (int u = 0; u < 2; ++u) {
          *(LAS f32x4*)(L + L_XCH + (((v ^ 4) * 3 + 0) * 2 + u) * 1024 + vo) = q4(acc[0], 2 + u);
          *(LAS f32x4*)(L + L_XCH + (((v ^ 2) * 3 + 1) * 2 + u) * 1024 + vo) = q4(acc[1], u);
          *(LAS f32x4*)(L + L_XCH + (((v ^ 6) * 3 + 2) * 2 + u) * 1024 + vo) = q4(acc[1], 2 + u);
        }
      }
      v4u kvn[8];
      auto kv_load = [&](int i) {
        const int kt = 2 * c - 8 + 4 * (v >> 2) + (i >> 1);
        return ld_sc1(rs, vo + (i & 1) * 1024, RFL(kt >= 0 ? KV_OFF + ((4 * j + (v & 3)) * 64 + kt) * 2048 : OOB));
      };
      auto kv_issue = [&]() {
#pragma unroll
        for (int i = 0; i < 8; ++i) kvn[i] = kv_load(i);
      };
      WAITV0();
      __syncthreads();
      kv_issue();
      {
        const int ho = (v >> 1) & 1, hl = 2 * (v & 1) + (v >> 2);
        const f32x2 ab = *(LAS f32x2*)(L + L_FST + (ho * 32 + tk) * 8);
        const float cexp = 0.25f * 1.4426950408889634f;
        f32x16 val;
#pragma unroll
        for (int u = 0; u < 2; ++u) {
          f32x4 o = q4(acc[0], u);
#pragma unroll
          for (int k = 0; k < 3; ++k) o += LDSF4(L_XCH + ((v * 3 + k) * 2 + u) * 1024 + vo);
          const f32x4 csn = LDSF4(L_CQ + (16 * hl + 8 * u + 4 * hh) * 4);
#pragma unroll
          for (int i = 0; i < 4; ++i) val[4 * u + i] = o[i] * (ab.x * cexp) + csn[i] * (ab.y * cexp);
        }
        *(LAS v4u*)(L + L_Q + (hl * 2 + ho) * 1024 + vo) = pk8(val, 0);
      }
      const int w1b = RFL(v < 7 ? W1_OFF + (j * 224 + v * 32) * KB : OOB);
#pragma unroll
      for (int i = 0; i < 4; ++i) w1[i] = ldw(rs, vo, w1b, i);
      SB();
      verify<8>(kvn, kv_load, tmo, 0x20, lane);
      SB();
#pragma unroll
      for (int i = 0; i < 4; ++i) {
        *(LAS v4u*)(L + L_KV + ((4 * (v >> 2) + i) * 4 + (v & 3)) * 2048 + vo) = kvn[2 * i];
        *(LAS v4u*)(L + L_KV + ((4 * (v >> 2) + i) * 4 + (v & 3)) * 2048 + 1024 + vo) = kvn[2 * i + 1];
      }
      __syncthreads();
    }
    {
      FRESH();
      const int hq = v & 3, r = v >> 2, tq = tk - 4 * hh;
      const int kb = L_KV + (r * 4 + hq) * 2048;
      const v4u Q = LDS4(L_Q + (hq * 2 + r) * 1024 + vo);
      v4u Kf[9];
#pragma unroll
      for (int i = 0; i < 9; ++i) Kf[i] = LDS4(kb + i * 8192 + vo);
      const bool ones_lane = (lane & 16) != 0;
      const unsigned vsel = ones_lane ? 0u : 1u, vbase = ones_lane ? (unsigned)L_ONES : (unsigned)(kb + 1024 + (32 * hh + (lane & 15)) * 16);
      auto vop = [&](int i, int s) -> v4u { return LDS4(vbase + vsel * (unsigned)(i * 8192 + s * 256)); };
      const int first_ok = 8 - r - 2 * c;
      v4u SENT4 = {SENT, SENT, SENT, SENT};
      asm volatile("" : "+v"(SENT4));
      const int tsl = (8 * j + 4 * (v & 1) + (v >> 1)) * 512 + tk * 16 + hh * 8;
#pragma unroll
      for (int hf = 0; hf < 2; ++hf) __builtin_amdgcn_raw_buffer_store_b64(v2u{SENT4.x, SENT4.y}, rs, tsl + X1T_OFF + c * TILE_B + hf * 32768, 0, AUX_SC1);
      const int hcb = RFL(v < 7 ? HT_OFF + c * HT_C + (28 * j + 4 * v) * 512 : OOB);
      const int xcb = RFL(lay == 0 ? XT1_OFF + c * TILE_B : OOB), ocb = RFL(lay == 0 ? OT1_OFF + c * TILE_B + (v >> 2) * 32768 + (2 * (4 * j + (v & 3))) * 512 : OOB);
      auto clean = [&](int i) {
        if (i < 4) st_sc1(SENT4, rs, vo, hcb + (i >> 1) * 114688 + 2 * (i & 1) * 512);
        else if (i < 6) __builtin_amdgcn_raw_buffer_store_b64(v2u{SENT4.x, SENT4.y}, rs, tsl + xcb + (i - 4) * 32768, 0, AUX_SC1);
        else st_sc1(SENT4, rs, vo, ocb);
      };
      SB();
      const int w1b = RFL(v < 7 ? W1_OFF + (j * 224 + v * 32) * KB : OOB);
      auto tmax = [&](const f32x16& x, float m) {
        const float a = max3f(x[0], x[1], x[2]), b2_ = max3f(x[3], x[4], x[5]), c2 = max3f(x[6], x[7], x[8]), d = max3f(x[9], x[10], x[11]), e = max3f(x[12], x[13], x[14]);
        return max3f(max3f(a, b2_, x[15]), max3f(c2, d, e), m);
      };
      f32x16 Sm, S[2];
      {
        f32x16 Sa = mfma32(Kf[0], Q, Z);
        const f32x16 Sb = mfma32(Kf[8], Q, Z);
        S[1] = mfma32(Kf[1], Q, Z);
        if (first_ok > 0) {
#pragma unroll
          for (int q = 0; q < 16; ++q) Sa[q] = -1e30f;
        }
#pragma unroll
        for (int q = 0; q < 16; ++q) { const int row = (q & 3) + 8 * (q >> 2); Sm[q] = row <= tq ? Sb[q] : Sa[q]; }
      }
      float mx = tmax(Sm, -1e30f);
      SB();
#pragma unroll
      for (int i = 1; i < 8; ++i) {
        if (i + 1 < 8) S[(i + 1) & 1] = mfma32(Kf[i + 1], Q, Z);
        if (i >= first_ok) mx = tmax(S[i & 1], mx);
        if (i < 6) w1[4 + (i - 1)] = ldw(rs, vo, w1b, 4 + (i - 1));
        if (i == 1) clean(0);
        if (i == 3) clean(4);
        if (i == 5) clean(5);
        SB();
      }
      { const f32x2 rr = both_halves(mx); mx = max3f(rr.x, rr.y, rr.y); }
      f32x16 Cm;
#pragma unroll
      for (int q = 0; q < 16; ++q) Cm[q] = -mx;
      S[1] = mfma32(Kf[1], Q, Cm);
      f32x16 O;
      {
        const v4u vn0 = vop(8, 0), vn1 = vop(8, 1), vo0 = vop(0, 0), vo1 = vop(0, 1);
        unsigned pp[8];
#pragma unroll
        for (int q = 0; q < 8; ++q) pp[q] = pk(__builtin_amdgcn_exp2f(Sm[2 * q] - mx), __builtin_amdgcn_exp2f(Sm[2 * q + 1] - mx));
        v4u pn[2], po[2];
#pragma unroll
        for (int q = 0; q < 8; ++q) {
          const int row0 = ((2 * q) & 3) + 8 * ((2 * q) >> 2);
          const unsigned mk = (row0 <= tq ? 0xFFFFu : 0u) | (row0 + 1 <= tq ? 0xFFFF0000u : 0u);
          pn[q >> 2][q & 3] = pp[q] & mk; po[q >> 2][q & 3] = pp[q] & ~mk;
        }
        O = mfma32(vn0, pn[0], Z); O = mfma32(vn1, pn[1], O);
        O = mfma32(vo0, po[0], O); O = mfma32(vo1, po[1], O);
        SB();
      }
#pragma unroll
      for (int i = 1; i < 8; ++i) {
        if (i + 1 < 8) S[(i + 1) & 1] = mfma32(Kf[i + 1], Q, Cm);
        const v4u va0 = vop(i, 0), va1 = vop(i, 1);
        if (i >= first_ok) {
          const f32x16& cur = S[i & 1];
          v4u pp[2];
#pragma unroll
          for (int q = 0; q < 8; ++q) pp[q >> 2][q & 3] = pk(__builtin_amdgcn_exp2f(cur[2 * q]), __builtin_amdgcn_exp2f(cur[2 * q + 1]));
          O = mfma32(va0, pp[0], O); O = mfma32(va1, pp[1], O);
        }
        w1[9 + (i - 1)] = ldw(rs, vo, w1b, 9 + (i - 1));
        if (i == 1) clean(1);
        if (i == 3) clean(2);
        if (i == 5) clean(3);
        if (i == 7) clean(6);
        SB();
      }
      KEEP(O);
      const float rl = __builtin_amdgcn_rcpf(O[8]);
      asm volatile("s_waitcnt vmcnt(19)" ::: "memory");
      st_sc1(nosent2(swap_T8(pk8(O, 0, rl))), rs, vo, OT_OFF + c * TILE_B + r * 32768 + (2 * (4 * j + hq)) * 512);
      if (v < 4) WAITV0();
    }
    {
      FRESH();
      const int otb = RFL(OT_OFF + c * TILE_B + v * 4096);
      const int w1b = RFL(v < 7 ? W1_OFF + (j * 224 + v * 32) * KB : OOB);
      auto oa_issue = [&]() {
#pragma unroll
        for (int i = 0; i < 4; ++i) stA[i] = ld_seq(rs, vo, otb, i);
      };
      auto ob_issue = [&]() {
#pragma unroll
        for (int i = 0; i < 4; ++i) stB[i] = ld_seq(rs, vo, otb + 32768, i);
      };
      f32x16 a;
      auto pass = [&](int hf) {
        const int x0 = L_TILE + hf * 32768 + (v >> 1) * 8192 + vo;
        v4u xs[8];
#pragma unroll
        for (int s = 0; s < 8; ++s) xs[s] = LDS4(x0 + s * 1024);
        a = Z;
#pragma unroll
        for (int s = 0; s < 8; ++s) a = mfma32(wo[s], xs[s], a);
        KEEP(a);
#pragma unroll
        for (int u = 1; u < 4; ++u) *(LAS f32x4*)(L + L_XCH + ((hf * 8 + (v ^ (2 * u))) * 3 + (u - 1)) * 1024 + vo) = q4(a, u);
      };
      auto epi = [&](int hf) {
        f32x4 o = q4(a, 0);
#pragma unroll
        for (int k = 0; k < 3; ++k) o += LDSF4(L_XCH + ((hf * 8 + v) * 3 + k) * 1024 + vo);
        xr[hf] = pk4(unpk4(xr[hf]) + o);
        __builtin_amdgcn_raw_buffer_store_b64(v2u{nosent(xr[hf].x), xr[hf].y}, rs, (8 * j + 4 * (v & 1) + (v >> 1)) * 512 + tk * 16 + hh * 8 + X1T_OFF + c * TILE_B + hf * 32768, 0, AUX_SC1);
      };
      oa_issue();
#pragma unroll
      for (int i = 16; i < 24; ++i) w1[i] = ldw(rs, vo, w1b, i);
      SB();
      verify<4>(stA, [&](int i) { return ld_seq(rs, vo, otb, i); }, tmo, 0x30, lane);
      __syncthreads();
      fill_half<false>(L, 0, v, lane, stA);
      ob_issue();
      __syncthreads();
      pass(0);
      verify<4>(stB, [&](int i) { return ld_seq(rs, vo, otb + 32768, i); }, tmo, 0x31, lane);
      fill_half<false>(L, 1, v, lane, stB);
      __syncthreads();
      epi(0);
      pass(1);
#pragma unroll
      for (int i = 24; i < 32; ++i) w1[i] = ldw(rs, vo, w1b, i);
      __syncthreads();
#pragma unroll
      for (int i = 0; i < 4; ++i) stA[i] = ld_seq(rs, vo, RFL(X1T_OFF + c * TILE_B + v * 4096), i);
      SB();
      epi(1);
    }
    v4u hA[14];
    {
      FRESH();
      const int x1b = RFL(X1T_OFF + c * TILE_B + v * 4096);
      const int w2b = RFL(W2_OFF + (j * 224 + v * 28) * KB);
      const int hb = RFL(v < 7 ? HT_OFF + c * HT_C + (28 * j + 4 * v) * 512 : OOB);
      auto xb_issue = [&]() {
#pragma unroll
        for (int i = 0; i < 4; ++i) stB[i] = ld_seq(rs, vo, x1b + 32768, i);
      };
      f32x16 a;
      auto pass = [&](int hf) {
        v4u xs[3];
        const int x0 = L_TILE + hf * 32768 + vo;
#pragma unroll
        for (int s = 0; s < 2; ++s) xs[s] = LDS4(x0 + s * 1024);
        a = Z;
        PRIO(1);
#pragma unroll
        for (int s = 0; s < 32; ++s) {
          if (s + 2 < 32) xs[(s + 2) % 3] = LDS4(x0 + (s + 2) * 1024);
          SB();
          a = mfma32(w1[s], xs[s % 3], a);
          if (hf == 0 && s == 8) xb_issue();
          if (hf == 0 && s >= 22) w2[s - 22] = ldw(rs, vo, w2b, s - 22);
          if (hf == 1 && s >= 2 && s < 20) w2[10 + (s - 2)] = ldw(rs, vo, w2b, 10 + (s - 2));
          SB();
        }
        KEEP(a);
        PRIO(0);
      };
      v4u ho[2];
      auto gelu = [&](int hf) {
        const f32x2 ab = final_stats<true>(L, lane, hf);
        const float c1 = -2.0f * 0.7978845608028654f * 1.4426950408889634f, c2 = c1 * 0.044715f;
#pragma unroll
        for (int u2 = 0; u2 < 2; ++u2) {
          f32x16 hv;
#pragma unroll
          for (int u = 2 * u2; u < 2 * u2 + 2; ++u) {
            const f32x4 csn = LDSF4(L_C1 + (32 * v + 8 * u + 4 * hh) * 4), bn = LDSF4(L_C1 + 1024 + (32 * v + 8 * u + 4 * hh) * 4);
#pragma unroll
            for (int i = 0; i < 4; ++i) {
              const float z = a[4 * u + i] * ab.x + (csn[i] * ab.y + bn[i]);
              const float e = __builtin_amdgcn_exp2f(z * (z * z * c2 + c1));
              hv[4 * u + i] = z * __builtin_amdgcn_rcpf(1.0f + e);
            }
          }
          ho[u2] = nosent2(swap_T8(pk8(hv, 8 * u2)));
          if (hf == 0) st_sc1(ho[u2], rs, vo, hb + 2 * u2 * 512);
        }
      };
      verify<4>(stA, [&](int i) { return ld_seq(rs, vo, x1b, i); }, tmo, 0x40, lane);
      fill_half<true>(L, 0, v, lane, stA);
      __syncthreads();
      pass(0);
      gelu(0);
      verify<4>(stB, [&](int i) { return ld_seq(rs, vo, x1b + 32768, i); }, tmo, 0x41, lane);
      fill_half<true>(L, 1, v, lane, stB);
      __syncthreads();
      pass(1);
#pragma unroll
      for (int s = 0; s < 14; ++s) hA[s] = ld_seq(rs, vo, RFL(HT_OFF + c * HT_C + 28 * v * 512), s);
      SB();
      gelu(1);
#pragma unroll
      for (int u2 = 0; u2 < 2; ++u2) st_sc1(ho[u2], rs, vo, hb + 114688 + 2 * u2 * 512);
    }
    {
      FRESH();
      const int htb = RFL(HT_OFF + c * HT_C + 28 * v * 512);
      const int wqb = RFL(lay == 0 ? WQ_OFF + (j * 192 + v * 24) * KB : OOB);
      const int xnb = RFL(lay == 0 ? XT1_OFF + c * TILE_B + v * 4096 : OOB);
#pragma unroll
      for (int hf = 0; hf < 2; ++hf) {
        verify<14>(hA, [&](int i) { return ld_seq(rs, vo, htb + hf * 114688, i); }, tmo, 0x50 + hf, lane);
        if (hf == 0) __syncthreads();
        f32x16 ac[2] = {Z, Z};
        PRIO(1);
#pragma unroll
        for (int s = 0; s < 14; ++s) {
          ac[0] = mfma32(w2[2 * s], hA[s], ac[0]);
          ac[1] = mfma32(w2[2 * s + 1], hA[s], ac[1]);
          if (hf == 1) wq[s] = ldw(rs, vo, wqb, s);
          else hA[s] = ld_seq(rs, vo, htb + 114688, s);
          SB();
        }
        KEEP(ac[0]); KEEP(ac[1]);
        PRIO(0);
        LAS unsigned char* R = L + (hf ? L_TILE : L_RB);
#pragma unroll
        for (int nt = 0; nt < 2; ++nt)
#pragma unroll
          for (int u = 0; u < 4; ++u) *(LAS f32x4*)(R + ((nt + 2 * u) * 8 + v) * 1024 + vo) = q4(ac[nt], u);
        if (hf == 1) { wq[14] = ldw(rs, vo, wqb, 14); wq[15] = ldw(rs, vo, wqb, 15); }
        __syncthreads();
        f32x4 s0 = {0, 0, 0, 0};
#pragma unroll
        for (int k = 0; k < 8; ++k) s0 += *(LAS f32x4*)(R + (v * 8 + k) * 1024 + vo);
        const f32x4 b0 = LDSF4(L_CQ + (192 + 32 * (v & 1) + 8 * (v >> 1) + 4 * hh) * 4);
        xr[hf] = pk4(unpk4(xr[hf]) + s0 + b0);
        if (hf == 1) {
#pragma unroll
          for (int i = 0; i < 4; ++i) stA[i] = ld_seq(rs, vo, xnb, i);
          SB();
        }
        if (lay == 0) {
          __builtin_amdgcn_raw_buffer_store_b64(v2u{nosent(xr[hf].x), xr[hf].y}, rs, (8 * j + 4 * (v & 1) + (v >> 1)) * 512 + tk * 16 + hh * 8 + XT1_OFF + c * TILE_B + hf * 32768, 0, AUX_SC1);
        } else {
          *(v2u*)(p.out + (size_t)(64 * c + 32 * hf + tk) * DM + 64 * j + 32 * (v & 1) + 8 * (v >> 1) + 4 * hh) = xr[hf];
        }
      }
      __syncthreads();
    }
  }
}
}

extern "C" void kernel_launch(void* const* d_in, const int* in_sizes, int n_in, void* d_out, int out_size, void* d_ws, size_t ws_size,
                              hipStream_t stream) {
  using namespace fk;
  Params p;
  p.x = (const u16*)d_in[0]; p.wqkv = (const u16*)d_in[1]; p.wo = (const u16*)d_in[2];
  p.w1 = (const u16*)d_in[4]; p.b1 = (const u16*)d_in[5]; p.w2 = (const u16*)d_in[6]; p.b2 = (const u16*)d_in[7];
  p.out = (u16*)d_out; p.ws = (unsigned char*)d_ws;
  (void)hipFuncSetAttribute((const void*)k_main, hipFuncAttributeMaxDynamicSharedMemorySize, L_BYTES);
  k_prep<<<PREP_BLOCKS, 256, 0, stream>>>(p);
  k_main<<<256, 512, L_BYTES, stream>>>(p);
}
```
